```python
import torch

def _softmax(x, axis=-1):
    return torch.softmax(x, dim=axis)
def _gelu(x):
    return torch.nn.functional.gelu(x, approximate="tanh")
def _silu(x):
    return torch.nn.functional.silu(x)
def _sigmoid(x):
    return torch.sigmoid(x)
def _layernorm(x, axis=-1, eps=1e-5):
    m = x.mean(dim=axis, keepdim=True)
    v = x.var(dim=axis, keepdim=True, unbiased=False)
    return (x - m) / torch.sqrt(v + eps)
def _scatter_add(tgt, idx, src, axis=0):
    return tgt.clone().index_add_(axis, idx, src)
def _segment_sum(src, seg, n):
    out = torch.zeros((n,) + src.shape[1:], dtype=src.dtype)
    return out.index_add_(0, seg, src)
def _pad(x, pw):
    flat = tuple(v for pair in reversed(pw) for v in pair)
    return torch.nn.functional.pad(x, flat)

kernel_name = 'sequential_preLN__linear_attn_block__swiglu_ffn__prompt'

def setup_inputs():

    _w = (lambda a: (lambda t: t.to(torch.bfloat16) if t.is_floating_point() else t)(torch.as_tensor(a)))
    def _A_setup():
        B, S, D = 1, 2048, 576
        return {
            "x": _w((lambda *s: torch.randn(*s, dtype=torch.bfloat16) * 0.05)(B, S, D)),
            "wqkv": _w((lambda *s: torch.randn(*s, dtype=torch.bfloat16) * 0.05)(D, 3 * D)),
            "wo": _w((lambda *s: torch.randn(*s, dtype=torch.bfloat16) * 0.05)(D, D)),
        }
    _A_in = _A_setup()

    def _B_setup():
        B, S, D, F = 1, 2048, 576, 4608
        return {
            "x": _w((lambda *s: torch.randn(*s, dtype=torch.bfloat16) * 0.05)(B, S, D)),
            "w_gate": _w((lambda *s: torch.randn(*s, dtype=torch.bfloat16) * 0.05)(D, F)),
            "w_up": _w((lambda *s: torch.randn(*s, dtype=torch.bfloat16) * 0.05)(D, F)),
            "w_down": _w((lambda *s: torch.randn(*s, dtype=torch.bfloat16) * 0.05)(F, D)),
        }
    _B_in = _B_setup()

    return {
        "A_x": _A_in["x"],
        "A_wqkv": _A_in["wqkv"],
        "A_wo": _A_in["wo"],
        "B_x": _B_in["x"],
        "B_w_gate": _B_in["w_gate"],
        "B_w_up": _B_in["w_up"],
        "B_w_down": _B_in["w_down"],
    }

def reference(A_x, A_wqkv, A_wo, B_x, B_w_gate, B_w_up, B_w_down):
    def _A_ref(x, wqkv, wo):
        B, S, D = x.shape
        H = 32; Dh = D // H
        qkv = (lambda x, *d: x.permute(*d))((x @ wqkv).reshape(B, S, 3, H, Dh), 2, 0, 3, 1, 4)
        q, k, v = qkv[0], qkv[1], qkv[2]
        phi = lambda t: torch.where(t > 0, t + 1, torch.exp(t)) + 1e-6
        pq, pk = phi(q), phi(k)
        kv = torch.einsum("bhsd,bhse->bhde", pk, v)
        z = torch.einsum("bhsd->bhd", pk)
        num = torch.einsum("bhqd,bhde->bhqe", pq, kv)
        den = torch.einsum("bhqd,bhd->bhq", pq, z)[..., None] + 1e-6
        o = num / den
        return (lambda x, *d: x.permute(*d))(o, 0, 2, 1, 3).reshape(B, S, D) @ wo

    def _B_ref(x, w_gate, w_up, w_down):
        g = _silu(x @ w_gate)
        u = x @ w_up
        return (g * u) @ w_down

    x = A_x
    h = x + _A_ref(_layernorm(x, axis=-1), A_wqkv, A_wo)
    h2 = h + _B_ref(_layernorm(h, axis=-1), B_w_gate, B_w_up, B_w_down)
    return h2
```

```cpp
#include <hip/hip_runtime.h>
#include <hip/hip_bf16.h>
typedef unsigned short u16;
__device__ __forceinline__ float bf2f(u16 v){ return __uint_as_float(((unsigned)v)<<16); }
__device__ __forceinline__ u16 f2bf(float f){ __hip_bfloat16 b = __float2bfloat16(f); return *(u16*)&b; }

constexpr int S=2048, D=576, F=4608, H=32, DH=18;

__global__ void ln_k(const u16* x, u16* y){
  int row=blockIdx.x; int t=threadIdx.x;
  float v[9]; float s=0;
  for(int i=0;i<9;i++){ v[i]=bf2f(x[row*D+t+64*i]); s+=v[i]; }
  for(int o=32;o;o>>=1) s+=__shfl_xor(s,o);
  float m=s/D; float q=0;
  for(int i=0;i<9;i++){ float d=v[i]-m; q+=d*d; }
  for(int o=32;o;o>>=1) q+=__shfl_xor(q,o);
  float r=rsqrtf(q/D+1e-5f);
  for(int i=0;i<9;i++) y[row*D+t+64*i]=f2bf((v[i]-m)*r);
}
__global__ void gemm_k(const u16* A,const u16* B,float* C,int M,int N,int K){
  __shared__ float As[16][65], Bs[16][65];
  int tx=threadIdx.x&15, ty=threadIdx.x>>4;
  int bm=blockIdx.y*64, bn=blockIdx.x*64;
  float acc[4][4]={};
  for(int k0=0;k0<K;k0+=16){
    for(int i=threadIdx.x;i<1024;i+=256){ int r=i>>4,c=i&15; As[c][r]=bf2f(A[(bm+r)*K+k0+c]); }
    for(int i=threadIdx.x;i<1024;i+=256){ int r=i>>6,c=i&63; Bs[r][c]=(bn+c<N)?bf2f(B[(k0+r)*N+bn+c]):0.f; }
    __syncthreads();
    for(int k=0;k<16;k++){
      float a[4],b[4];
      for(int i=0;i<4;i++){a[i]=As[k][ty*4+i]; b[i]=Bs[k][tx*4+i];}
      for(int i=0;i<4;i++)for(int j=0;j<4;j++)acc[i][j]+=a[i]*b[j];
    }
    __syncthreads();
  }
  for(int i=0;i<4;i++)for(int j=0;j<4;j++) if(bn+tx*4+j<N) C[(long)(bm+ty*4+i)*N+bn+tx*4+j]=acc[i][j];
}
__device__ __forceinline__ float phi(float t){ return (t>0? t+1.f: __expf(t))+1e-6f; }
__global__ void kv_k(const float* qkv,float* kv){
  int h=blockIdx.x; int t=threadIdx.x; if(t>=18*19) return;
  int d=t/19,e=t%19; float s=0;
  for(int r=0;r<S;r++){ float pk=phi(qkv[r*1728+576+h*18+d]); float v=(e<18)?qkv[r*1728+1152+h*18+e]:1.f; s+=pk*v; }
  kv[h*342+t]=s;
}
__global__ void o_k(const float* qkv,const float* kv,u16* o){
  int i=blockIdx.x*blockDim.x+threadIdx.x; if(i>=S*D) return;
  int r=i/D,c=i%D,h=c/18,e=c%18; float num=0,den=0;
  for(int d=0;d<18;d++){ float pq=phi(qkv[r*1728+h*18+d]); num+=pq*kv[h*342+d*19+e]; den+=pq*kv[h*342+d*19+18]; }
  o[i]=f2bf(num/(den+1e-6f));
}
__global__ void add_k(const u16* x,const float* a,u16* h){ int i=blockIdx.x*blockDim.x+threadIdx.x; if(i<S*D) h[i]=f2bf(bf2f(x[i])+a[i]); }
__global__ void act_k(const float* g,const float* u,u16* a){ int i=blockIdx.x*blockDim.x+threadIdx.x; if(i<S*F){ float x=g[i]; a[i]=f2bf(x/(1.f+__expf(-x))*u[i]); } }

extern "C" void kernel_launch(void* const* d_in, const int* in_sizes, int n_in,
                              void* d_out, int out_size, void* d_ws, size_t ws_size,
                              hipStream_t stream) {
  const u16* x=(const u16*)d_in[0]; const u16* wqkv=(const u16*)d_in[1]; const u16* wo=(const u16*)d_in[2];
  const u16* wg=(const u16*)d_in[4]; const u16* wu=(const u16*)d_in[5]; const u16* wd=(const u16*)d_in[6];
  char* w=(char*)d_ws;
  u16* xn=(u16*)w; w+=S*D*2;
  float* qkv=(float*)w; w+=S*1728*4;
  float* kv=(float*)w; w+=65536;
  u16* o=(u16*)w; w+=S*D*2;
  float* tmp=(float*)w; w+=S*D*4;
  u16* h=(u16*)w; w+=S*D*2;
  u16* hn=(u16*)w; w+=S*D*2;
  float* g=(float*)w; w+=(size_t)S*F*4;
  float* u=(float*)w; w+=(size_t)S*F*4;
  u16* act=(u16*)w; w+=(size_t)S*F*2;
  ln_k<<<S,64,0,stream>>>(x,xn);
  gemm_k<<<dim3(27,32),256,0,stream>>>(xn,wqkv,qkv,S,1728,D);
  kv_k<<<32,384,0,stream>>>(qkv,kv);
  o_k<<<S*D/256,256,0,stream>>>(qkv,kv,o);
  gemm_k<<<dim3(9,32),256,0,stream>>>(o,wo,tmp,S,D,D);
  add_k<<<S*D/256,256,0,stream>>>(x,tmp,h);
  ln_k<<<S,64,0,stream>>>(h,hn);
  gemm_k<<<dim3(72,32),256,0,stream>>>(hn,wg,g,S,F,D);
  gemm_k<<<dim3(72,32),256,0,stream>>>(hn,wu,u,S,F,D);
  act_k<<<S*F/256,256,0,stream>>>(g,u,act);
  gemm_k<<<dim3(9,32),256,0,stream>>>(act,wd,tmp,S,D,F);
  add_k<<<S*D/256,256,0,stream>>>(h,tmp,(u16*)d_out);
}
```

```cpp
#include <hip/hip_runtime.h>
#include <hip/hip_bf16.h>
typedef unsigned short u16;
typedef short s16x4 __attribute__((ext_vector_type(4)));
typedef short s16x8 __attribute__((ext_vector_type(8)));
typedef __bf16 bf16x8 __attribute__((ext_vector_type(8)));
typedef __bf16 bf16x2 __attribute__((ext_vector_type(2)));
typedef float f32x2 __attribute__((ext_vector_type(2)));
typedef float f32x4 __attribute__((ext_vector_type(4)));
typedef float f32x16 __attribute__((ext_vector_type(16)));
typedef unsigned u32x4 __attribute__((ext_vector_type(4)));
typedef unsigned u32x2 __attribute__((ext_vector_type(2)));
#define LAS __attribute__((address_space(3)))
#define GAS __attribute__((address_space(1)))
#define DI __device__ __forceinline__

#define STAMP_DECL
#define STAMP(i)
#define STAMP_OUT(kid)
namespace k {
constexpr int S=2048, D=576, F=4608;
DI float bf2f(u16 v){ return __uint_as_float(((unsigned)v)<<16); }
DI u16 f2bf(float f){ __bf16 b=(__bf16)f; return __builtin_bit_cast(u16,b); }
DI unsigned pk2(float lo,float hi){ bf16x2 v; v[0]=(__bf16)lo; v[1]=(__bf16)hi; return __builtin_bit_cast(unsigned,v); }

#define BAR() do{ asm volatile("":::"memory"); __builtin_amdgcn_s_barrier(); asm volatile("":::"memory"); }while(0)
#define MF16(a,bb,c) __builtin_amdgcn_mfma_f32_16x16x32_bf16(a,bb,c,0,0,0)
#define SB() __builtin_amdgcn_sched_barrier(0)
template<int I> struct IC{ static constexpr int v=I; };
template<int B,int E,class Fn> DI void sfor(Fn&& f){ if constexpr(B<E){ f(IC<B>{}); sfor<B+1,E>(f); } }
DI void glds16(const void* sbase, unsigned voff, unsigned lds){
  unsigned keep;
  asm volatile("s_mov_b32 %0, m0\n\ts_mov_b32 m0, %3\n\ts_nop 0\n\tglobal_load_lds_dwordx4 %1, %2\n\ts_mov_b32 m0, %0" : "=&s"(keep) : "v"(voff), "s"(sbase), "s"(lds) : "memory");
}
template<int OFF> DI void gload16(bf16x8& r,const void* sbase,unsigned voff){ asm volatile("global_load_dwordx4 %0, %1, %2 offset:%3" : "=v"(r) : "v"(voff), "s"(sbase), "i"(OFF) : "memory"); }
template<int N> DI void waitv(){ asm volatile("s_waitcnt vmcnt(%0)" :: "i"(N) : "memory"); }

DI s16x4 trread(const LAS char* p){ return __builtin_amdgcn_ds_read_tr16_b64_v4i16((LAS s16x4*)p); }
DI float lo16(unsigned v){ return __uint_as_float(v<<16); }
DI float hi16(unsigned v){ return __uint_as_float(v&0xffff0000u); }
DI float phi(float t){ return (t>0.f? t+1.f: __builtin_amdgcn_exp2f(t*1.44269504089f))+1e-6f; }
DI float bcast_hi(float x){
  unsigned a=__float_as_uint(x); auto r=__builtin_amdgcn_permlane32_swap(a,a,false,false); return __uint_as_float(r[1]); }

DI void retile_gu(int blk,char* sm,const u16* __restrict__ wg,const u16* __restrict__ wu,u16* __restrict__ out){
  const int tid=threadIdx.x, lane=tid&63, w=tid>>6;
  const int kb=blk%9, fb=blk/9;
  u32x4 v4[4];
  #pragma unroll
  for(int i=0;i<4;i++){ int p=tid+256*i; int mat=p>>9, r=(p>>3)&63, c=p&7;
    v4[i]=*(const u32x4*)((mat?wu:wg)+(size_t)(kb*64+r)*F+fb*64+c*8); }
  #pragma unroll
  for(int i=0;i<4;i++){ int p=tid+256*i; int mat=p>>9, r=(p>>3)&63, c=p&7; *(u32x4*)(sm+(mat*64+r)*144+c*16)=v4[i]; }
  __syncthreads();
  const int g=lane>>4, q=(lane>>2)&3, p=lane&3;
  #pragma unroll
  for(int i=0;i<4;i++){ int T=w, s=i>>1, mat=i&1;
    const LAS char* base=(const LAS char*)sm+(mat*64+32*s+8*g+q)*144+(16*T+4*p)*2;
    s16x4 a=trread(base), b=trread(base+4*144);
    s16x8 v=__builtin_shufflevector(a,b,0,1,2,3,4,5,6,7);
    *(s16x8*)(out+((size_t)(((fb*4+T)*18+kb*2+s)*2+mat)*64+lane)*8)=v;
  }
}
DI void retile_d(int blk,char* sm,const u16* __restrict__ wd,u16* __restrict__ out){
  const int tid=threadIdx.x, lane=tid&63, w=tid>>6;
  const int P=blk>>1, hf=blk&1;
  u32x4 v4[5];
  #pragma unroll
  for(int i=0;i<5;i++){ int p=tid+256*i; if(p<32*36){ int r=p/36,c=p%36; v4[i]=*(const u32x4*)(wd+(size_t)(P*32+r)*D+hf*288+c*8); } }
  #pragma unroll
  for(int i=0;i<5;i++){ int p=tid+256*i; if(p<32*36){ int r=p/36,c=p%36; *(u32x4*)(sm+r*592+c*16)=v4[i]; } }
  __syncthreads();
  const int g=lane>>4, q=(lane>>2)&3, p=lane&3;
  #pragma unroll
  for(int i=0;i<5;i++){ int nt=w+4*i; if(nt<18){
    const LAS char* base=(const LAS char*)sm+(4*g+q)*592+(16*nt+4*p)*2;
    s16x4 a=trread(base), b=trread(base+16*592);
    s16x8 v=__builtin_shufflevector(a,b,0,1,2,3,4,5,6,7);
    int ct=hf*2+(nt>=9), n9=nt>=9?nt-9:nt;
    *(s16x8*)(out+((size_t)((ct*144+P)*9+n9)*64+lane)*8)=v; }
  }
}
struct P0 { const u16 *x,*wqkv,*wo,*wg,*wu,*wd; u16 *xn,*wqkvt,*wot,*wgu,*wdt; float *mx,*kvf; unsigned* cnt; };
constexpr int K0_LN=128, K0_QKV=72, K0_WO=24, K0_Z=8;
__global__ __launch_bounds__(256) void k0_k(P0 p,int gu0,int gu1,int d0,int d1){
  __shared__ __attribute__((aligned(16))) char sm[38*1024];
  const int tid=threadIdx.x, lane=tid&63, w=tid>>6;
  int b=blockIdx.x;
  if(b<K0_LN){
    const u16* xr=p.x+(size_t)(b*16+(lane&15))*D+(lane>>4)*8;
    u32x4 v[5]; float f[5][8];
    #pragma unroll
    for(int i=0;i<5;i++){ int s=w+4*i; v[i]= s<18? *(const u32x4*)(xr+s*32) : (u32x4){0,0,0,0}; }
    float sum=0;
    #pragma unroll
    for(int i=0;i<5;i++)
      #pragma unroll
      for(int j=0;j<4;j++){ f[i][2*j]=lo16(v[i][j]); f[i][2*j+1]=hi16(v[i][j]); sum+=f[i][2*j]+f[i][2*j+1]; }
    sum+=__shfl_xor(sum,16); sum+=__shfl_xor(sum,32);
    float* red=(float*)sm;
    if(lane<16) red[w*16+lane]=sum;
    __syncthreads();
    const int t=lane&15;
    const float m=((red[t]+red[16+t])+(red[32+t]+red[48+t]))*(1.f/576.f);
    float q=0;
    #pragma unroll
    for(int i=0;i<5;i++){ if(w+4*i<18){
      #pragma unroll
      for(int j=0;j<8;j++){ f[i][j]-=m; q+=f[i][j]*f[i][j]; } } }
    q+=__shfl_xor(q,16); q+=__shfl_xor(q,32);
    if(lane<16) red[64+w*16+lane]=q;
    __syncthreads();
    const float r=__builtin_amdgcn_rsqf(((red[64+t]+red[80+t])+(red[96+t]+red[112+t]))*(1.f/576.f)+1e-5f);
    #pragma unroll
    for(int i=0;i<5;i++){ int s=w+4*i; if(s<18){ u32x4 o;
      #pragma unroll
      for(int j=0;j<4;j++) o[j]=pk2(f[i][2*j]*r,f[i][2*j+1]*r);
      *(u32x4*)(p.xn+((size_t)(b*18+s)*64+lane)*8)=o; } }
    if(tid<16) p.mx[b*16+tid]=m;
    return;
  }
  b-=K0_LN;
  const int g=lane>>4, q=(lane>>2)&3, pp=lane&3;
  if(b<K0_QKV){
    const int s=b>>2, hq=b&3;
    u32x4 v4[7];
    #pragma unroll
    for(int i=0;i<7;i++){ int e=tid+256*i; if(e<32*54){ int r=e/54,c=e%54, part=c/18, cc=c%18; v4[i]=*(const u32x4*)(p.wqkv+(size_t)(s*32+r)*1728+part*576+hq*144+cc*8); } }
    #pragma unroll
    for(int i=0;i<7;i++){ int e=tid+256*i; if(e<32*54){ int r=e/54,c=e%54; *(u32x4*)(sm+r*880+c*16)=v4[i]; } }
    __syncthreads();
    #pragma unroll
    for(int t=0;t<8;t++){
      int vc=(t<3? 16*t:16*(t-3))+4*pp;
      int col= t<3? (vc<36? vc:0) : (vc<36? 144+vc : (vc<72? 288+vc-36 : 0));
      const LAS char* base=(const LAS char*)sm+(8*g+q)*880+(w*36+col)*2;
      s16x4 a=trread(base), bb=trread(base+4*880);
      s16x8 v=__builtin_shufflevector(a,bb,0,1,2,3,4,5,6,7);
      int myc=(t<3? 16*t:16*(t-3))+(lane&15);
      if(myc>=(t<3?36:72)) v=(s16x8){0,0,0,0,0,0,0,0};
      *(s16x8*)(p.wqkvt+((size_t)(((hq*4+w)*18+s)*8+t)*64+lane)*8)=v;
    }
    return;
  }
  b-=K0_QKV;
  if(b<K0_WO){
    u32x4 v4[9];
    #pragma unroll
    for(int i=0;i<9;i++){ int e=tid+256*i; int r=e/72,c=e%72; int rho=2*b+(r>>4), hp=rho/3, ty=rho%3, row=r&15;
      int ee= ty==0? row-4 : (ty==1? 12+row : 28+row); bool ok= ee>=0 && ee<36;
      v4[i]= ok? *(const u32x4*)(p.wo+(size_t)(hp*36+ee)*D+c*8) : (u32x4){0,0,0,0}; }
    #pragma unroll
    for(int i=0;i<9;i++){ int e=tid+256*i; int r=e/72,c=e%72; *(u32x4*)(sm+r*1168+c*16)=v4[i]; }
    __syncthreads();
    { int r=tid>>3, part=tid&7; float sacc=0;
      #pragma unroll
      for(int i=0;i<9;i++){ u32x4 v=*(const u32x4*)(sm+r*1168+(part*9+i)*16);
        #pragma unroll
        for(int j=0;j<4;j++) sacc+=lo16(v[j])+hi16(v[j]); }
      sacc+=__shfl_xor(sacc,1); sacc+=__shfl_xor(sacc,2); sacc+=__shfl_xor(sacc,4);
      if(part==0) *(u32x2*)(sm+r*1168+1152)=(u32x2){(unsigned)f2bf(sacc*(1.f/576.f)),0u}; }
    __syncthreads();
    #pragma unroll
    for(int i=0;i<10;i++){ int fr=w*10+i; int ct=fr/5, nt=fr%5;
      int col=ct*72+nt*16+4*pp; if(nt==4 && pp>=2) col=576;
      const LAS char* base=(const LAS char*)sm+(4*g+q)*1168+col*2;
      s16x4 a=trread(base), bb=trread(base+16*1168);
      s16x8 v=__builtin_shufflevector(a,bb,0,1,2,3,4,5,6,7);
      *(s16x8*)(p.wot+((size_t)((ct*24+b)*5+nt)*64+lane)*8)=v;
    }
    return;
  }
  b-=K0_WO;
  if(b<K0_Z){
    #pragma unroll
    for(int i=0;i<4;i++) *(u32x4*)((char*)p.kvf+(size_t)b*16384+i*4096+tid*16)=(u32x4){0,0,0,0};
    if(b==0){ for(int i=tid;i<4096;i+=256) p.cnt[i]=0; }
    return;
  }
  b-=K0_Z;
  if(b<gu1-gu0){ retile_gu(gu0+b,sm,p.wg,p.wu,p.wgu); return; }
  b-=gu1-gu0;
  if(b<d1-d0){ retile_d(d0+b,sm,p.wd,p.wdt); return; }
}

#define K1_LA 6
__global__ __launch_bounds__(512,2) void k1_k(const u16* __restrict__ xn,const u16* __restrict__ wqkvt,u16* __restrict__ pq,float* __restrict__ kvf){
  __shared__ __attribute__((aligned(16))) char sm[144*1024];
  STAMP_DECL;
  constexpr int LA=K1_LA, PF=6;
  const int tid=threadIdx.x, lane=tid&63, w=__builtin_amdgcn_readfirstlane(tid>>6);
  const int b=blockIdx.x, xcd=b&7, idx=b>>3;
  const int rt=(xcd>>1)*4+(idx&3), hp=(xcd&1)*8+(idx>>2);
  const int tt=rt*8+w;
  const unsigned smb=(unsigned)(size_t)(LAS char*)sm;
  const char* wbase=(const char*)wqkvt+(size_t)hp*144*1024+w*1024;
  const char* abase=(const char*)xn+(size_t)tt*18*1024;
  const unsigned voff=lane*16;
  STAMP(0);
  bf16x8 af[18];
  auto ld=[&](auto S){ constexpr int s=S.v; glds16(wbase+s*8192,voff,smb+(s*8+w)*1024); gload16<(s&3)*1024>(af[s],abase+(s>>2)*4096,voff); };
  sfor<0,LA>(ld);
  f32x4 acc[8];
  unsigned lwb[3];
  #pragma unroll
  for(int i=0;i<3;i++){ lwb[i]=smb+lane*16+i*49152; asm volatile("" : "+v"(lwb[i])); }
  bf16x8 wf[PF];
  #define LDW1(i) (*(const LAS bf16x8*)((const LAS char*)(size_t)lwb[(i)/48]+((i)%48)*1024))
  #define WAITP(m,n) asm volatile("s_waitcnt vmcnt(%2)" : "+v"(af[2*(m)]),"+v"(af[2*(m)+1]) : "i"(n) : "memory")
  WAITP(0,2*(LA-2)); BAR();
  STAMP(1);
  sfor<0,PF-1>([&](auto I){ wf[I.v]=LDW1(I.v); });
  sfor<0,144>([&](auto I){ constexpr int i=I.v; constexpr int s=i>>3,t=i&7;
    if constexpr((i&15)==8 && (i>>4)<8){ constexpr int m=(i>>4)+1;
      constexpr int iss=(LA+2*m-1)<18?(LA+2*m-1):18; WAITP(m,2*(iss-(2*m+2))); BAR(); }
    if constexpr(i+PF-1<144) wf[(i+PF-1)%PF]=LDW1(i+PF-1);
    SB();
    if constexpr(t<3){ if constexpr(s==0) acc[t]=MF16(wf[i%PF],af[s],(f32x4){}); else acc[t]=MF16(wf[i%PF],af[s],acc[t]); }
    else { if constexpr(s==0) acc[t]=MF16(af[s],wf[i%PF],(f32x4){}); else acc[t]=MF16(af[s],wf[i%PF],acc[t]); }
    SB();
    if constexpr(t==1 && s+LA<18) ld(IC<s+LA>{});
    SB();
  });
  STAMP(2);
  { char* o=(char*)pq+((size_t)tt*16+hp)*1536;
    u32x4 v; v[0]=pk2(phi(acc[0][0]),phi(acc[0][1])); v[1]=pk2(phi(acc[0][2]),phi(acc[0][3])); v[2]=pk2(phi(acc[1][0]),phi(acc[1][1])); v[3]=pk2(phi(acc[1][2]),phi(acc[1][3]));
    *(u32x4*)(o+lane*16)=v;
    u32x2 v2; v2[0]=pk2(phi(acc[2][0]),phi(acc[2][1])); v2[1]=pk2(phi(acc[2][2]),phi(acc[2][3]));
    *(u32x2*)(o+1024+lane*8)=v2; }
  const int c=lane&15;
  s16x4 kvb[5];
  #pragma unroll
  for(int t=0;t<5;t++){ float x[4];
    #pragma unroll
    for(int r=0;r<4;r++){ float a=acc[3+t][r];
      if(t<2) a=phi(a); else if(t==2) a= c<4? phi(a):a; else if(t==4) a= c<8? a : 1.f;
      x[r]=a; }
    u32x2 pk; pk[0]=pk2(x[0],x[1]); pk[1]=pk2(x[2],x[3]); kvb[t]=__builtin_bit_cast(s16x4,pk); }
  BAR();
  { char* xs=sm+w*8192+lane*16;
    #define KVM(idx,kt,vt) *(f32x4*)(xs+(idx)*1024)=__builtin_amdgcn_mfma_f32_16x16x16bf16_1k(kvb[kt],kvb[vt],(f32x4){},0,0,0)
    KVM(0,0,2); KVM(1,1,2); KVM(2,0,3); KVM(3,1,3); KVM(4,2,3); KVM(5,0,4); KVM(6,1,4); KVM(7,2,4); }
  __syncthreads();
  STAMP(3);
  {
    const int kt=(w==0||w==2||w==5)?0:((w==1||w==3||w==6)?1:2), vt= w<2?2:(w<5?3:4);
    float* dst=kvf+((size_t)hp*8+w)*256;
    #pragma unroll
    for(int i=0;i<4;i++){ int idx=i*64+lane; float sacc=0;
      #pragma unroll
      for(int ww=0;ww<8;ww++) sacc+=*(const float*)(sm+ww*8192+w*1024+idx*4);
      int fl=idx>>2, r=idx&3; int d=16*kt+4*(fl>>4)+r, cc=fl&15, v=16*vt+cc;
      bool ok;
      if(v<72) ok = v>=36 && d<36 && ((d<18)==(v-36<18));
      else ok = (cc==8||cc==12)? d<18 : ((cc==9||cc==13)? (d>=18&&d<36) : false);
      if(ok) __hip_atomic_fetch_add((GAS float*)(dst+idx),sacc,__ATOMIC_RELAXED,__HIP_MEMORY_SCOPE_AGENT);
    } }
  STAMP(7); STAMP_OUT(2);
}

__global__ __launch_bounds__(512,2) void k2_k(const u16* __restrict__ pq,const float* __restrict__ kvf,const u16* __restrict__ wot,const u16* __restrict__ x,const float* __restrict__ mx,
                                              u16* __restrict__ h,u16* __restrict__ cfrag,float* __restrict__ ssq){
  __shared__ __attribute__((aligned(16))) char sm[160*1024];
  STAMP_DECL;
  const int tid=threadIdx.x, lane=tid&63, w=__builtin_amdgcn_readfirstlane(tid>>6);
  const int b=blockIdx.x, xcd=b&7, idx=b>>3;
  const int rt=xcd*4+(idx&3), ct=idx>>2;
  const int tq=w&3, hf=w>>2;
  const int tt=rt*4+tq;
  const unsigned smb=(unsigned)(size_t)(LAS char*)sm;
  const unsigned voff=lane*16;
  STAMP(0);
  f32x4 kvr[16];
  #pragma unroll
  for(int i=0;i<16;i++) kvr[i]=*(const f32x4*)((const char*)kvf+(size_t)(w*16+i)*1024+voff);
  const char* wb=(const char*)wot+((size_t)ct*24+hf*12)*5*1024;
  const unsigned ring=smb+65536+hf*49152;
  #pragma unroll
  for(int i=0;i<12;i++) glds16(wb+(i*4+tq)*1024,voff,ring+(i*4+tq)*1024);
  bf16x8 q01[8]; s16x4 q2[8];
  { const char* qb=(const char*)pq+((size_t)tt*16+hf*8)*1536;
    #pragma unroll
    for(int i=0;i<8;i++){ q01[i]=*(const bf16x8*)(qb+i*1536+voff); q2[i]=*(const s16x4*)(qb+i*1536+1024+lane*8); } }
  const int tok=tt*16+(lane&15), g=lane>>4;
  u32x2 xv[3];
  #pragma unroll
  for(int i=0;i<3;i++){ int nt= hf? 2+i : (i<2? i:1); int n=nt*16+4*g; if(n>=72) n=68; xv[i]=*(const u32x2*)(x+(size_t)tok*D+ct*72+n); }
  const float mxv=mx[tok];
  #pragma unroll
  for(int i=0;i<16;i++){ u32x2 pk; pk[0]=pk2(kvr[i][0],kvr[i][1]); pk[1]=pk2(kvr[i][2],kvr[i][3]); *(u32x2*)(sm+(w*16+i)*512+lane*8)=pk; }
  asm volatile("s_waitcnt vmcnt(0) lgkmcnt(0)":::"memory");
  BAR();
  STAMP(1);
  f32x4 acc[5]={};
  s16x4 ot[6]; const s16x4 z4={0,0,0,0};
  const char* kb=sm+hf*32768+lane*8;
  const char* rb=sm+65536+hf*49152+lane*16;
  auto cat=[](s16x4 a,s16x4 bb){ return __builtin_bit_cast(bf16x8,__builtin_shufflevector(a,bb,0,1,2,3,4,5,6,7)); };
  #pragma unroll
  for(int pr=0;pr<4;pr++){
    if(pr==1){ BAR();
      #pragma unroll
      for(int i=0;i<3;i++) glds16(wb+(48+i*4+tq)*1024,voff,ring+(i*4+tq)*1024); }
    if(pr==3){ asm volatile("s_waitcnt vmcnt(0)":::"memory"); BAR(); }
    #pragma unroll
    for(int hh=0;hh<2;hh++){ const int hl=pr*2+hh;
      const char* kk=kb+hl*4096;
      #define KF(i) (*(const s16x4*)(kk+(i)*512))
      f32x4 A=MF16(cat(KF(0),KF(1)),q01[hl],(f32x4){});
      f32x4 B=MF16(cat(KF(2),KF(3)),q01[hl],(f32x4){}); B=MF16(cat(KF(4),z4),cat(q2[hl],z4),B);
      f32x4 C=MF16(cat(KF(5),KF(6)),q01[hl],(f32x4){}); C=MF16(cat(KF(7),z4),cat(q2[hl],z4),C);
      const float ra=__builtin_amdgcn_rcpf(bcast_hi(C[0])+1e-6f), rbv=__builtin_amdgcn_rcpf(bcast_hi(C[1])+1e-6f);
      const float b01= g<2? ra:rbv, b23= g<1? ra:rbv;
      u32x2 pk;
      pk[0]=pk2(A[0]*ra,A[1]*ra); pk[1]=pk2(A[2]*ra,A[3]*ra); ot[hh*3]=__builtin_bit_cast(s16x4,pk);
      pk[0]=pk2(B[0]*b01,B[1]*b01); pk[1]=pk2(B[2]*b23,B[3]*b23); ot[hh*3+1]=__builtin_bit_cast(s16x4,pk);
      pk[0]=pk2(C[0]*rbv,C[1]*rbv); pk[1]=pk2(C[2]*rbv,C[3]*rbv); ot[hh*3+2]=__builtin_bit_cast(s16x4,pk);
    }
    #pragma unroll
    for(int ks=0;ks<3;ks++){ bf16x8 ob=cat(ot[2*ks],ot[2*ks+1]);
      #pragma unroll
      for(int n=0;n<5;n++){ int pc=((pr*3+ks)*5+n)%48; acc[n]=MF16(*(const bf16x8*)(rb+pc*1024),ob,acc[n]); } }
  }
  STAMP(2);
  BAR();
  { char* xs=sm+w*5120+lane*16;
    #pragma unroll
    for(int n=0;n<5;n++) *(f32x4*)(xs+n*1024)=acc[n]; }
  __syncthreads();
  { const char* xr=sm+(w^4)*5120+lane*16;
    f32x4 t4=acc[4]+*(const f32x4*)(xr+4096);
    const float m=mxv+bcast_hi(t4[0]);
    float q=0;
    #pragma unroll
    for(int i=0;i<3;i++){
      if(i==2 && !hf) break;
      f32x4 v; int nt;
      if(i<2){ nt=hf*2+i; f32x4 mine= hf? acc[2+i]:acc[i]; v=mine+*(const f32x4*)(xr+nt*1024); } else { nt=4; v=t4; }
      const bool ok= i<2 || g<2;
      u32x2 hp2; hp2[0]=pk2(lo16(xv[i][0])+v[0],hi16(xv[i][0])+v[1]); hp2[1]=pk2(lo16(xv[i][1])+v[2],hi16(xv[i][1])+v[3]);
      float c0=lo16(hp2[0])-m,c1=hi16(hp2[0])-m,c2=lo16(hp2[1])-m,c3=hi16(hp2[1])-m;
      if(ok){ q+=(c0*c0+c1*c1)+(c2*c2+c3*c3);
        int n=ct*72+nt*16+4*g;
        *(u32x2*)(h+(size_t)tok*D+n)=hp2;
        *(u32x2*)((char*)cfrag+((size_t)(tt*18+(n>>5))*64+((n>>3)&3)*16+(lane&15))*16+(n&4)*2)=(u32x2){pk2(c0,c1),pk2(c2,c3)}; }
    }
    q+=__shfl_xor(q,16); q+=__shfl_xor(q,32);
    if(lane<16) ssq[tok*16+ct*2+hf]=q;
  }
  STAMP(7); STAMP_OUT(3);
}

#define G1_LA 2
#define G1_LW 5
#define G1_PF 4
namespace g1 {
constexpr int LA=G1_LA, LW=G1_LW, PF=G1_PF, RING=144;
constexpr int pT(int i){ return i<108? (i%6)/2 : 3+(i-108)/36; }
constexpr int pS(int i){ return i<108? i/6 : ((i-108)%36)/2; }
constexpr int nA(int u){ return (u>=0&&u<9)?4:0; }
constexpr int nW(int u){ return (u>=0&&u<27)?2:0; }
constexpr int younger(int g,int need){
  int cnt=0;
  auto add=[&](int isA,int u){ int n=isA?nA(u):nW(u); if(n==0) return; if(u<=need) cnt=0; else cnt+=n; };
  for(int u=0;u<LA;u++){ add(0,u); add(1,u); }
  for(int u=LA;u<LW;u++) add(0,u);
  for(int x=0;x<=g;x++){ add(1,x+LA); add(0,x+LW); }
  return cnt;
}
}
__global__ __launch_bounds__(512,2) void gemm1_k(const u16* __restrict__ cfrag,const float* __restrict__ ssq,const u16* __restrict__ wgu,u16* __restrict__ act){
  using namespace g1;
  __shared__ __attribute__((aligned(16))) char sm[RING*1024];
  STAMP_DECL;
  const int tid=threadIdx.x; const int b=blockIdx.x;
  const int lane=tid&63, w=__builtin_amdgcn_readfirstlane(tid>>6);
  const int xcd=b&7, idx=b>>3;
  const int rt=(xcd&1)*4+(idx&3), fs=(xcd>>1)*8+(idx>>2);
  const int tt=rt*16+w*2;
  const int odd=fs&1;
  const unsigned smb=(unsigned)(size_t)(LAS char*)sm;
  const char* wbase=(const char*)wgu+(size_t)(fs*9+odd)*36864;
  const char* wlast=(const char*)wgu+(size_t)(fs*9+(odd?0:8))*36864;
  const char* abase=(const char*)cfrag+(size_t)tt*18*1024;
  const unsigned voff=lane*16;
  const bool lo=lane<32;
  STAMP(0);
  bf16x8 af[18][2];
  f32x4 sq[2];
  { const float* sp=ssq+(size_t)(tt*16)*16; unsigned vo=(lane&15)*64+(lane>>4)*16;
    asm volatile("global_load_dwordx4 %0, %1, %2" : "=v"(sq[0]) : "v"(vo), "s"(sp) : "memory");
    asm volatile("global_load_dwordx4 %0, %1, %2 offset:1024" : "=v"(sq[1]) : "v"(vo), "s"(sp) : "memory"); }
  auto loadW=[&](auto U,auto H){ constexpr int u=U.v, half=H.v;
    const int pl= half? 8+(w>>1) : w;
    const char* src;
    if constexpr(u<9){
      int s=2*u+(pl>=6), r6=pl-(pl>=6?6:0); src=wbase+(r6>>1)*36864+(s*2+(r6&1))*1024;
    } else { constexpr int T=3+(u-9)/3, o=((u-9)%3)*12; src=(T==8? wlast: wbase+T*36864)+(o+pl)*1024; }
    unsigned dst=smb+((u*12)%RING+pl)*1024;
    if constexpr(half){ if(lo) glds16(src+(w&1)*512, voff, dst+(w&1)*512); } else glds16(src,voff,dst);
  };
  auto loadA=[&](auto U,auto I){ constexpr int u=U.v,i=I.v; constexpr int s=2*u+(i>>1), t=i&1;
    gload16<(i>>1)*1024>(af[s][t], abase+t*18*1024+u*2048, voff); };
  sfor<0,LA>([&](auto U){ loadW(U,IC<0>{}); loadW(U,IC<1>{}); sfor<0,4>([&](auto I){ loadA(U,I); }); });
  sfor<LA,LW>([&](auto U){ loadW(U,IC<0>{}); loadW(U,IC<1>{}); });
  f32x4 acc[4][2][2];
  unsigned lwb[3];
  #pragma unroll
  for(int i=0;i<3;i++){ lwb[i]=smb+lane*16+i*49152; asm volatile("" : "+v"(lwb[i])); }
  bf16x8 wf[PF];
  float c1[2];
  float ee[2],pp[2],oo[2];
  u32x4 hold[2];
#define ACT_AUX 16
  __amdgpu_buffer_rsrc_t ors=__builtin_amdgcn_make_buffer_rsrc((char*)act+(size_t)tt*144*1024,0,2*144*1024,0x00020000);
  const int obase=((fs*9+odd)>>1)*1024;
  const int olast=((fs*9+(odd?0:8))>>1)*1024+(odd?8:0);
  #define LDW(i) (*(const LAS bf16x8*)((const LAS char*)(size_t)lwb[((i)%RING)/48]+(((i)%RING)%48)*1024))
  #define WAITU(u,n) asm volatile("s_waitcnt vmcnt(%4)" : "+v"(af[2*(u)][0]),"+v"(af[2*(u)][1]),"+v"(af[2*(u)+1][0]),"+v"(af[2*(u)+1][1]) : "i"(n) : "memory")
  STAMP(1);
  WAITU(0,younger(-1,0));
  asm volatile("" : "+v"(sq[0]),"+v"(sq[1]));
  BAR();
  STAMP(2);
  #pragma unroll
  for(int t=0;t<2;t++){ f32x4 s4=sq[t]; float ss=(s4[0]+s4[1])+(s4[2]+s4[3]); ss+=__shfl_xor(ss,16); ss+=__shfl_xor(ss,32);
    c1[t]=-1.44269504089f*__builtin_amdgcn_rsqf(ss*(1.f/576.f)+1e-5f); }
  asm volatile("" : "+v"(c1[0]),"+v"(c1[1]));
  sfor<0,PF-1>([&](auto I){ wf[I.v]=LDW(I.v); });
  float ea,pa;
  auto epi=[&](auto E,auto K){ constexpr int e=E.v,k=K.v; constexpr int ET=e>>3, t=(e>>2)&1, r=e&3;
    if constexpr(k==0){ float g=acc[ET&3][0][t][r], u=acc[ET&3][1][t][r]; ea=g*c1[t]; pa=g*u; asm volatile("" : "+v"(ea),"+v"(pa)); }
    if constexpr(k==1){ ea=__builtin_amdgcn_exp2f(ea); asm volatile("" : "+v"(ea)); }
    if constexpr(k==2){ ea=1.f+ea; asm volatile("" : "+v"(ea)); }
    if constexpr(k==3){ ea=__builtin_amdgcn_rcpf(ea); asm volatile("" : "+v"(ea)); }
    if constexpr(k==4){
      oo[r&1]=pa*ea;
      if constexpr((r&1)==0) asm volatile("" : "+v"(oo[0]));
      else {
        constexpr int hi=(ET==8||(ET&1)==0)?0:2;
        hold[t][hi+(r>>1)]=pk2(oo[0],oo[1]);
        asm volatile("" : "+v"(hold[t][hi+(r>>1)]));
        if constexpr(r==3){
          if constexpr(ET==8){ __builtin_amdgcn_raw_buffer_store_b64((u32x2){hold[t][0],hold[t][1]},ors,voff,olast+t*144*1024,ACT_AUX); }
          else if constexpr((ET&1)==1){ __builtin_amdgcn_raw_buffer_store_b128(hold[t],ors,voff,obase+t*144*1024+(ET>>1)*1024,ACT_AUX); }
        }
      }
    } };
  auto slot=[&](auto M){ constexpr int m=M.v-216-2;
    if constexpr(m>=0){ constexpr int e=(m*4+3)/27; constexpr int k=m-(e*27)/4;
      if constexpr(e<64 && k>=0 && k<5) epi(IC<e>{},IC<k>{});
    } };
  sfor<0,324>([&](auto I){ constexpr int i=I.v; constexpr int T=pT(i), s=pS(i), mat=i&1, g=i/12, pos=i%12;
    if constexpr(i==108) STAMP(3);
    if constexpr(i==108+72) STAMP(5);
    if constexpr(pos==7){
      if constexpr(g<8){ WAITU(g+1,younger(g,g+1)); BAR(); }
      else if constexpr(g<26 && (g&1)==0){ waitv<younger(g,g+2)>(); BAR(); }
    }
    if constexpr(i+PF-1<324) wf[(i+PF-1)%PF]=LDW(i+PF-1);
    SB();
    if constexpr(s==0) acc[T&3][mat][0]=MF16(wf[i%PF],af[s][0],(f32x4){}); else acc[T&3][mat][0]=MF16(wf[i%PF],af[s][0],acc[T&3][mat][0]);
    SB();
    slot(IC<2*i>{});
    SB();
    if constexpr(s==0) acc[T&3][mat][1]=MF16(wf[i%PF],af[s][1],(f32x4){}); else acc[T&3][mat][1]=MF16(wf[i%PF],af[s][1],acc[T&3][mat][1]);
    SB();
    slot(IC<2*i+1>{});
    if constexpr(pos<4 && g+LA<9) loadA(IC<g+LA>{},IC<pos>{});
    if constexpr((pos==4||pos==5) && g+LW<27) loadW(IC<g+LW>{},IC<pos-4>{});
    SB();
  });
  STAMP(6);
  sfor<64,72>([&](auto E){ sfor<0,5>([&](auto K){ epi(E,K); }); });
  STAMP(7);
  STAMP_OUT(0);
}

#define G2_LWP 44
#define G2_LAK 5
#define G2_PF 5
namespace g2 {
constexpr int LWP=G2_LWP, LAK=G2_LAK, PF=G2_PF, NP=162, RING=72, NR=41;
constexpr int vmc(int iw,int ja,int pw){
  int cnt=0;
  auto W=[&](int r){ if(4*r<pw) cnt=0; else cnt++; };
  auto A=[&](int j){ if(j<=ja) cnt=0; else cnt++; };
  int r=0;
  for(int j=0;j<LAK;j++){ while(4*r<9*(j+1) && 4*r<LWP){ W(r); r++; } A(j); A(j); }
  while(4*r<LWP){ W(r); r++; }
  for(int i=0;i<iw;i++){
    if((i%9==0||i%9==4) && i/9+LAK<18) A(i/9+LAK);
    if((i+LWP)%4==0 && (i+LWP)/4<NR) W((i+LWP)/4);
  }
  return cnt;
}
constexpr int wr_before(int j){
  int r=0; for(int x=0;x<=j;x++){ while(4*r<9*(x+1) && 4*r<LWP) r++; } return r; }
}
__global__ __launch_bounds__(512,2) void gemm2_k(const u16* __restrict__ act,const u16* __restrict__ wdt,const u16* __restrict__ h,const float* __restrict__ ssq,
                                                 float* __restrict__ xbuf,unsigned* __restrict__ cnt,u16* __restrict__ out){
  using namespace g2;
  __shared__ __attribute__((aligned(16))) char sm[2*RING*1024];
  STAMP_DECL;
  const int tid=threadIdx.x, lane=tid&63, w=__builtin_amdgcn_readfirstlane(tid>>6);
  const int b=blockIdx.x, xcd=b&7, idx=b>>3;
  const int sp=xcd>>1, rt=(xcd&1)*8+(idx>>2), ct=idx&3;
  const int tq=w&3, kh=w>>2;
  const int P0=sp*36+kh*18;
  const unsigned smb=(unsigned)(size_t)(LAS char*)sm+kh*RING*1024;
  const char* wbase=(const char*)wdt+(size_t)(ct*144+P0)*9*1024;
  const char* abase=(const char*)act+((size_t)(rt*8+tq*2)*144+P0)*1024;
  const unsigned voff=lane*16;
  STAMP(0);
  constexpr int AR=LAK+1;
  bf16x8 af[AR][2];
  auto loadW=[&](auto R){ constexpr int r=R.v; int q=(r==NR-1)? (tq>1?1:tq): tq; glds16(wbase+(4*r+q)*1024, voff, smb+((4*r)%RING+q)*1024); };
  auto loadA=[&](auto J,auto T){ constexpr int j=J.v,t=T.v; gload16<0>(af[j%AR][t], abase+t*144*1024+j*1024, voff); };
  sfor<0,LAK>([&](auto J){ constexpr int j=J.v; sfor<(j? wr_before(j-1):0),wr_before(j)>([&](auto R){ loadW(R); }); loadA(J,IC<0>{}); loadA(J,IC<1>{}); });
  sfor<wr_before(LAK-1),(LWP+3)/4>([&](auto R){ loadW(R); });
  f32x4 acc[9][2];
  unsigned lwb[2];
  #pragma unroll
  for(int i=0;i<2;i++){ lwb[i]=smb+lane*16+i*36864; asm volatile("" : "+v"(lwb[i])); }
  bf16x8 wf[PF];
  #define LDW2(i) (*(const LAS bf16x8*)((const LAS char*)(size_t)lwb[((i)%RING)/36]+(((i)%RING)%36)*1024))
  #define WAITA(j,n) asm volatile("s_waitcnt vmcnt(%2)" : "+v"(af[(j)%AR][0]),"+v"(af[(j)%AR][1]) : "i"(n) : "memory")
  STAMP(1);
  waitv<vmc(0,-1,18)>(); BAR();
  STAMP(2);
  sfor<0,PF-1>([&](auto I){ wf[I.v]=LDW2(I.v); });
  sfor<0,NP>([&](auto I){ constexpr int i=I.v; constexpr int j=i/9, nt=i%9;
    if constexpr(i==81) STAMP(3);
    if constexpr((i+PF)%18==0 && i+PF<NP){ waitv<vmc(i,-1,i+PF+18)>(); BAR(); }
    if constexpr(nt==0) WAITA(j,vmc(i,j,0));
    if constexpr(i+PF-1<NP) wf[(i+PF-1)%PF]=LDW2(i+PF-1);
    SB();
    if constexpr(j==0) acc[nt][0]=MF16(wf[i%PF],af[j%AR][0],(f32x4){}); else acc[nt][0]=MF16(wf[i%PF],af[j%AR][0],acc[nt][0]);
    if constexpr(j==0) acc[nt][1]=MF16(wf[i%PF],af[j%AR][1],(f32x4){}); else acc[nt][1]=MF16(wf[i%PF],af[j%AR][1],acc[nt][1]);
    SB();
    if constexpr(nt==0 && j+LAK<18) loadA(IC<j+LAK>{},IC<0>{});
    if constexpr(nt==4 && j+LAK<18) loadA(IC<j+LAK>{},IC<1>{});
    if constexpr((i+LWP)%4==0 && (i+LWP)/4<NR) loadW(IC<(i+LWP)/4>{});
    SB();
  });
  STAMP(4);
  BAR();
  { char* xs=sm+(w*9)*1024+lane*16;
    #pragma unroll
    for(int n=0;n<9;n++){ f32x4 v= kh? acc[n][0]:acc[n][1]; *(f32x4*)(xs+n*1024)=v; } }
  __syncthreads();
  f32x4 part[9];
  { const char* xr=sm+((w^4)*9)*1024+lane*16;
    #pragma unroll
    for(int n=0;n<9;n++){ f32x4 mine= kh? acc[n][1]:acc[n][0]; part[n]=mine+*(const f32x4*)(xr+n*1024); } }
  const int grp=rt*4+ct;
  __amdgpu_buffer_rsrc_t xr=__builtin_amdgcn_make_buffer_rsrc((void*)(xbuf+(size_t)grp*4*4*18*256),0,4*4*18*1024,0x00020000);
  if(tq!=sp){
    #pragma unroll
    for(int n=0;n<9;n++) __builtin_amdgcn_raw_buffer_store_b128(__builtin_bit_cast(u32x4,part[n]),xr,voff,((tq*4+sp)*18+kh*9+n)*1024,16);
  } else {
    char* xs=sm+(72+kh*9)*1024+lane*16;
    #pragma unroll
    for(int n=0;n<9;n++) *(f32x4*)(xs+n*1024)=part[n];
  }
  asm volatile("s_waitcnt vmcnt(0) lgkmcnt(0)":::"memory");
  BAR();
  if(tid==0) __hip_atomic_fetch_add((GAS unsigned*)(cnt+grp*64),1u,__ATOMIC_RELAXED,__HIP_MEMORY_SCOPE_AGENT);
  STAMP(5);
  const int nf=(w<2)?3:2;
  u32x2 hv[3]; float r2[3]; size_t go[3];
  #pragma unroll
  for(int x=0;x<3;x++){ int f=w+8*x; if(f>17) f=17; int tok=rt*128+sp*32+(f>=9?16:0)+(lane&15); int n=ct*144+(f>=9?f-9:f)*16+(lane>>4)*4;
    go[x]=(size_t)tok*D+n; hv[x]=*(const u32x2*)(h+go[x]);
    f32x4 s4=*(const f32x4*)(ssq+tok*16+(lane>>4)*4);
    float ss=(s4[0]+s4[1])+(s4[2]+s4[3]); ss+=__shfl_xor(ss,16); ss+=__shfl_xor(ss,32);
    r2[x]=__builtin_amdgcn_rcpf(ss*(1.f/576.f)+1e-5f); }
  { unsigned spins=0;
    while((unsigned)__builtin_amdgcn_readfirstlane(__hip_atomic_load((GAS unsigned*)(cnt+grp*64),__ATOMIC_RELAXED,__HIP_MEMORY_SCOPE_AGENT))<4u){
      __builtin_amdgcn_s_sleep(1); if(++spins>(1u<<22)) break; } }
  __builtin_amdgcn_fence(__ATOMIC_ACQUIRE,"wavefront");
  STAMP(6);
  u32x4 rv[3][3];
  #pragma unroll
  for(int x=0;x<3;x++){ int f=w+8*x; if(f>17) f=17;
    #pragma unroll
    for(int y=0;y<3;y++){ int src=(sp+1+y)&3; rv[x][y]=__builtin_amdgcn_raw_buffer_load_b128(xr,voff,((sp*4+src)*18+f)*1024,16); } }
  #pragma unroll
  for(int x=0;x<3;x++){ if(x<nf){ int f=w+8*x;
    f32x4 own=*(const f32x4*)(sm+(72+f)*1024+lane*16);
    f32x4 v=(own+__builtin_bit_cast(f32x4,rv[x][1]))+(__builtin_bit_cast(f32x4,rv[x][0])+__builtin_bit_cast(f32x4,rv[x][2]));
    float h0=__uint_as_float(hv[x][0]<<16),h1=__uint_as_float(hv[x][0]&0xffff0000u),h2=__uint_as_float(hv[x][1]<<16),h3=__uint_as_float(hv[x][1]&0xffff0000u);
    u32x2 ov; ov[0]=pk2(v[0]*r2[x]+h0,v[1]*r2[x]+h1); ov[1]=pk2(v[2]*r2[x]+h2,v[3]*r2[x]+h3);
    *(u32x2*)(out+go[x])=ov; } }
  STAMP(7); STAMP_OUT(1);
}
}

extern "C" void kernel_launch(void* const* d_in, const int* in_sizes, int n_in,
                              void* d_out, int out_size, void* d_ws, size_t ws_size,
                              hipStream_t stream) {
  using namespace k;
  P0 p;
  p.x=(const u16*)d_in[0]; p.wqkv=(const u16*)d_in[1]; p.wo=(const u16*)d_in[2];
  p.wg=(const u16*)d_in[4]; p.wu=(const u16*)d_in[5]; p.wd=(const u16*)d_in[6];
  char* w=(char*)d_ws;
  p.xn=(u16*)w; w+=S*D*2;
  p.wqkvt=(u16*)w; w+=16*144*1024;
  p.wot=(u16*)w; w+=8*24*5*1024;
  p.mx=(float*)w; w+=S*4;
  p.kvf=(float*)w; w+=128*1024;
  p.cnt=(unsigned*)w; w+=64*64*4;
  u16* pq=(u16*)w; w+=128*16*1536;
  u16* h=(u16*)w; w+=S*D*2;
  u16* cfrag=(u16*)w; w+=S*D*2;
  float* ssq=(float*)w; w+=S*16*4;
  p.wgu=(u16*)w; w+=(size_t)2*D*F*2;
  p.wdt=(u16*)w; w+=(size_t)D*F*2;
  u16* act=(u16*)w; w+=(size_t)S*F*2;
  float* xbuf=(float*)w; w+=(size_t)64*4*4*18*1024;
  k0_k<<<K0_LN+K0_QKV+K0_WO+K0_Z+648+288,256,0,stream>>>(p,0,648,0,288);
  k1_k<<<256,512,0,stream>>>(p.xn,p.wqkvt,pq,p.kvf);
  k2_k<<<256,512,0,stream>>>(pq,p.kvf,p.wot,p.x,p.mx,h,cfrag,ssq);
  gemm1_k<<<256,512,0,stream>>>(cfrag,ssq,p.wgu,act);
  gemm2_k<<<256,512,0,stream>>>(act,p.wdt,h,ssq,xbuf,p.cnt,(u16*)d_out);
}
```

```cpp
#include <hip/hip_runtime.h>
#include <hip/hip_bf16.h>
typedef unsigned short u16;
typedef unsigned long long u64;
typedef short s16x4 __attribute__((ext_vector_type(4)));
typedef short s16x8 __attribute__((ext_vector_type(8)));
typedef __bf16 bf16x8 __attribute__((ext_vector_type(8)));
typedef __bf16 bf16x2 __attribute__((ext_vector_type(2)));
typedef float f32x4 __attribute__((ext_vector_type(4)));
typedef unsigned u32x4 __attribute__((ext_vector_type(4)));
typedef unsigned u32x2 __attribute__((ext_vector_type(2)));
#define LAS __attribute__((address_space(3)))
#define GAS __attribute__((address_space(1)))
#define DI __device__ __forceinline__

#define STAMP(i)
#define STAMP_OUT()

#define WARM 21
#define WARM_SLEEP 20
#define STAG 1
#define F1_LA 6
namespace k {
constexpr int S=2048, D=576, F=4608;
constexpr int STAMP_LDS=156*1024, TAG_LDS=155*1024;
DI int fresh_lane(){ int l; asm volatile("v_mbcnt_lo_u32_b32 %0, -1, 0\n\tv_mbcnt_hi_u32_b32 %0, -1, %0" : "=v"(l)); return l; }
DI u16 f2bf(float f){ __bf16 b=(__bf16)f; return __builtin_bit_cast(u16,b); }
DI unsigned pk2(float lo,float hi){ bf16x2 v; v[0]=(__bf16)lo; v[1]=(__bf16)hi; return __builtin_bit_cast(unsigned,v); }
DI float lo16(unsigned v){ return __uint_as_float(v<<16); }
DI float hi16(unsigned v){ return __uint_as_float(v&0xffff0000u); }
DI float phi(float t){ return (t>0.f? t+1.f: __builtin_amdgcn_exp2f(t*1.44269504089f))+1e-6f; }
DI float bcast_hi(float x){ unsigned a=__float_as_uint(x); auto r=__builtin_amdgcn_permlane32_swap(a,a,false,false); return __uint_as_float(r[1]); }
DI float sum_g(float x){
  unsigned a=__float_as_uint(x); auto r=__builtin_amdgcn_permlane32_swap(a,a,false,false); x=__uint_as_float(r[0])+__uint_as_float(r[1]);
  a=__float_as_uint(x); auto r2=__builtin_amdgcn_permlane16_swap(a,a,false,false); return __uint_as_float(r2[0])+__uint_as_float(r2[1]); }
DI float dot2(unsigned a,unsigned b,float c){ return __builtin_amdgcn_fdot2_f32_bf16(__builtin_bit_cast(bf16x2,a),__builtin_bit_cast(bf16x2,b),c,false); }
DI s16x4 trread(unsigned a){ return __builtin_amdgcn_ds_read_tr16_b64_v4i16((LAS s16x4*)(size_t)a); }
DI bf16x8 cat(s16x4 a,s16x4 b){ return __builtin_bit_cast(bf16x8,__builtin_shufflevector(a,b,0,1,2,3,4,5,6,7)); }
DI bf16x8 cat2(u32x2 a,u32x2 b){ return __builtin_bit_cast(bf16x8,(u32x4){a[0],a[1],b[0],b[1]}); }
#define BAR() do{ asm volatile("":::"memory"); __builtin_amdgcn_s_barrier(); asm volatile("":::"memory"); }while(0)
#define LBAR() do{ asm volatile("s_waitcnt lgkmcnt(0)":::"memory"); __builtin_amdgcn_s_barrier(); asm volatile("":::"memory"); }while(0)
#define MF16(a,bb,c) __builtin_amdgcn_mfma_f32_16x16x32_bf16(a,bb,c,0,0,0)
#define SB() __builtin_amdgcn_sched_barrier(0)
#define WAITV0() __builtin_amdgcn_s_waitcnt(0x0F70)
template<int I> struct IC{ static constexpr int v=I; };
template<int B,int E,class Fn> DI void sfor(Fn&& f){ if constexpr(B<E){ f(IC<B>{}); sfor<B+1,E>(f); } }
DI void glds16(const void* sbase, unsigned voff, unsigned lds){
  unsigned keep;
  asm volatile("s_mov_b32 %0, m0\n\ts_mov_b32 m0, %3\n\ts_nop 0\n\tglobal_load_lds_dwordx4 %1, %2\n\ts_mov_b32 m0, %0" : "=&s"(keep) : "v"(voff), "s"(sbase), "s"(lds) : "memory");
}
DI void glds16c(const void* sbase, unsigned voff, unsigned lds){
  unsigned keep;
  asm volatile("s_mov_b32 %0, m0\n\ts_mov_b32 m0, %3\n\ts_nop 0\n\tglobal_load_lds_dwordx4 %1, %2 sc1\n\ts_mov_b32 m0, %0" : "=&s"(keep) : "v"(voff), "s"(sbase), "s"(lds) : "memory");
}
DI void glds16m(const void* sbase, unsigned voff, unsigned lds, u64 mask){
  unsigned keep;
  asm volatile("s_mov_b32 %0, m0\n\ts_mov_b32 m0, %3\n\ts_mov_b64 exec, %4\n\tglobal_load_lds_dwordx4 %1, %2\n\ts_mov_b64 exec, -1\n\ts_mov_b32 m0, %0" : "=&s"(keep) : "v"(voff), "s"(sbase), "s"(lds), "s"(mask) : "memory");
}
DI void glds16ms(const void* sbase, unsigned voff, unsigned lds, u64 mask, int skip){
  unsigned keep;
  asm volatile("s_cmp_lg_u32 %5, 0\n\ts_cbranch_scc1 1f\n\ts_mov_b32 %0, m0\n\ts_mov_b32 m0, %3\n\ts_mov_b64 exec, %4\n\tglobal_load_lds_dwordx4 %1, %2\n\ts_mov_b64 exec, -1\n\ts_mov_b32 m0, %0\n1:" : "=&s"(keep) : "v"(voff), "s"(sbase), "s"(lds), "s"(mask), "s"(skip) : "memory","scc");
}
template<int OFF,class T> DI void gload16(T& r,const void* sbase,unsigned voff){ asm volatile("global_load_dwordx4 %0, %1, %2 offset:%3" : "=v"(r) : "v"(voff), "s"(sbase), "i"(OFF) : "memory"); }
template<int OFF,class T> DI void gload16c(T& r,const void* sbase,unsigned voff){ asm volatile("global_load_dwordx4 %0, %1, %2 offset:%3 sc1" : "=v"(r) : "v"(voff), "s"(sbase), "i"(OFF) : "memory"); }
template<int N> DI void waitv(){ asm volatile("s_waitcnt vmcnt(%0)" :: "i"(N) : "memory"); }
typedef __amdgpu_buffer_rsrc_t rsrc_t;
DI rsrc_t mkrs(const void* p,unsigned bytes){ return __builtin_amdgcn_make_buffer_rsrc((void*)p,0,bytes,0x00020000); }
#define ST16(v,rs,vo,so) __builtin_amdgcn_raw_buffer_store_b128(__builtin_bit_cast(u32x4,v),rs,vo,so,16)
#define ST8(v,rs,vo,so)  __builtin_amdgcn_raw_buffer_store_b64(__builtin_bit_cast(u32x2,v),rs,vo,so,16)
#define ST4(v,rs,vo,so)  __builtin_amdgcn_raw_buffer_store_b32(__float_as_uint(v),rs,vo,so,16)
#define LD16(rs,vo,so) __builtin_amdgcn_raw_buffer_load_b128(rs,vo,so,16)
#define LD8(rs,vo,so)  __builtin_amdgcn_raw_buffer_load_b64(rs,vo,so,16)

constexpr size_t O_EP=0;
constexpr size_t O_F0 =256;
constexpr size_t O_FW =O_F0+2048;
constexpr size_t O_FWO=O_FW+2048;
constexpr size_t O_FKV=O_FWO+1024;
constexpr size_t O_FO =O_FKV+16*256;
constexpr size_t O_FH =O_FO+16*256;
constexpr size_t O_FA =O_FH+8*256;
constexpr size_t O_FX =O_FA+8*256;
constexpr size_t O_KVP=32768;
constexpr size_t O_XN =O_KVP+(size_t)256*8192;
constexpr size_t O_MX =O_XN+(size_t)S*D*2;
constexpr size_t O_WB =O_MX+(size_t)S*4;
constexpr size_t O_WQ =O_WB+4096;
constexpr size_t O_WO =O_WQ+(size_t)16*144*1024;
constexpr size_t O_O  =O_WO+(size_t)8*120*1024;
constexpr size_t O_MU =O_O+(size_t)128*16*1536;
constexpr size_t O_MH =O_MU+(size_t)S*64;
constexpr size_t O_C  =O_MH+(size_t)S*4;
constexpr size_t O_SSQ=O_C+(size_t)S*D*2;
constexpr size_t O_ACT=O_SSQ+(size_t)S*64;
constexpr size_t O_X  =O_ACT+(size_t)S*F*2;
struct Params { const u16 *x,*wqkv,*wo,*wg,*wu,*wd; u16* out; char* ws; };

DI void publish(char* ws,size_t off,u64 tag,int w){
  asm volatile("s_waitcnt vmcnt(0) lgkmcnt(0)":::"memory");
  BAR();
  if(w==0 && fresh_lane()==0) __hip_atomic_store((GAS u64*)(ws+off),tag,__ATOMIC_RELAXED,__HIP_MEMORY_SCOPE_AGENT);
}
DI void pollp(const GAS u64* p,u64 tag){
  for(unsigned spins=0; spins<(1u<<20); ++spins){
    u64 v=__hip_atomic_load(p,__ATOMIC_RELAXED,__HIP_MEMORY_SCOPE_AGENT);
    if(__all(v==tag)) break;
    __builtin_amdgcn_s_sleep(1);
  }
  __builtin_amdgcn_fence(__ATOMIC_ACQUIRE,"wavefront");
}
template<int N> DI void poll(char* ws,size_t off,u64 tag,int lane){ pollp((const GAS u64*)(ws+off)+(lane<N?lane:0),tag); }

namespace g1 {
constexpr int PF=4, LAK=5;
constexpr int pT(int i){ return i<108? (i%6)/2 : 3+(i-108)/36; }
constexpr int pS(int i){ return i<108? i/6 : ((i-108)%36)/2; }
constexpr int slotOf(int T){ return T<4? T : (T<8? T-4 : 0); }
constexpr int krow(int kc){ return kc<16? 72*(kc>>1)+32*(kc&1) : 288*(kc-16)+64; }
constexpr int kstep8(int kc){ return kc<16? 8:72; }
}
namespace g2 {
constexpr int LWP=44, PRE=72, LAK=5, PF=5, NP=162, RING=72, NR=41;
constexpr int vmc(int iw,int ja,int pw){
  int cnt=0;
  for(int j=0;j<LAK;j++){ if(j<=ja) cnt=0; else cnt+=2; }
  for(int i=0;i<iw;i++){
    if((i%9==0||i%9==4) && i/9+LAK<18){ if(i/9+LAK<=ja) cnt=0; else cnt++; }
    if((i+LWP)%4==0 && (i+LWP)/4<NR && (i+LWP)/4>=PRE/4){ if(i+LWP<pw) cnt=0; else cnt++; }
  }
  return cnt;
}
}

__global__ __launch_bounds__(512,2) void fused_k(Params p){
  __shared__ __attribute__((aligned(16))) char sm[160*1024];
  const int tid=threadIdx.x, lane=tid&63, w=__builtin_amdgcn_readfirstlane(tid>>6);
  const int b=blockIdx.x, xcd=b&7, idx=b>>3;
  const unsigned smb=(unsigned)(size_t)(LAS char*)sm;
  const unsigned voff=lane*16;
  const int g=lane>>4, q=(lane>>2)&3, pp=lane&3, c16=lane&15;
  char* const ws=p.ws;
  STAMP(0);
  u64 tag;
  const int hp=(xcd&1)*8+(idx>>2), rt=(xcd>>1)*4+(idx&3);
  {
    char* const smw=sm+w*16384;
    const unsigned smwb=smb+w*16384;
    if(w<2){
      u32x2 ep; { const unsigned z=0; asm volatile("global_load_dwordx2 %0, %1, %2 sc1" : "=v"(ep) : "v"(z), "s"(ws+(w?(size_t)O_XN:0)) : "memory"); }
      u32x4 v[5];
      { const char* xb=(const char*)p.x+(size_t)(b*8+w*4)*1152; const unsigned v0=g*1152+c16*16, v4=g*1152+(64+(c16&7))*16;
        gload16<0>(v[0],xb,v0); gload16<256>(v[1],xb,v0); gload16<512>(v[2],xb,v0); gload16<768>(v[3],xb,v0); gload16<0>(v[4],xb,v4); }
      asm volatile("s_waitcnt vmcnt(0)" : "+v"(ep),"+v"(v[0]),"+v"(v[1]),"+v"(v[2]),"+v"(v[3]),"+v"(v[4]) :: "memory");
      if(tid==0) *(u32x2*)(sm+TAG_LDS)=ep;
      float s1=0;
      #pragma unroll
      for(int i=0;i<5;i++){ const unsigned one=(i<4||c16<8)?0x3f803f80u:0u;
        #pragma unroll
        for(int j=0;j<4;j++) s1=dot2(v[i][j],one,s1); }
      s1+=__shfl_xor(s1,1); s1+=__shfl_xor(s1,2); s1+=__shfl_xor(s1,4); s1+=__shfl_xor(s1,8);
      const float m=s1*(1.f/576.f);
      float f[5][8]; float s2=0,s2t=0;
      #pragma unroll
      for(int i=0;i<5;i++)
        #pragma unroll
        for(int j=0;j<4;j++){ f[i][2*j]=lo16(v[i][j])-m; f[i][2*j+1]=hi16(v[i][j])-m;
          if(i<4){ s2=fmaf(f[i][2*j],f[i][2*j],s2); s2=fmaf(f[i][2*j+1],f[i][2*j+1],s2); } else { s2t=fmaf(f[i][2*j],f[i][2*j],s2t); s2t=fmaf(f[i][2*j+1],f[i][2*j+1],s2t); } }
      s2+= c16<8? s2t:0.f;
      s2+=__shfl_xor(s2,1); s2+=__shfl_xor(s2,2); s2+=__shfl_xor(s2,4); s2+=__shfl_xor(s2,8);
      const float r=__builtin_amdgcn_rsqf(s2*(1.f/576.f)+1e-5f);
      #pragma unroll
      for(int i=0;i<5;i++){ u32x4 o;
        #pragma unroll
        for(int j=0;j<4;j++) o[j]=pk2(f[i][2*j]*r,f[i][2*j+1]*r);
        *(u32x4*)(smw+(i<4? c16+16*i: 64+(c16&7))*80+g*16)=o; }
      asm volatile("s_waitcnt lgkmcnt(0)":::"memory");
      const rsrc_t rx=mkrs(ws+O_XN+(size_t)(b>>1)*18432+(b&1)*128+w*64,18432);
      #pragma unroll
      for(int i=0;i<5;i++){ const int ch=(lane>>2)+16*i; if(ch<72) ST16(*(const u32x4*)(smw+ch*80+pp*16),rx,ch*256+pp*16,0); }
      const rsrc_t rm=mkrs(ws+O_MX+(size_t)b*32+w*16,16);
      if(c16==0) ST4(m,rm,g*4,0);
    } else if(w<4){
      if(b<144){
        const int s=b>>3, hq=b&7, h2=w-2;
        u32x4 v[8];
        const char* src=(const char*)p.wqkv+(size_t)s*32*3456+hq*144+h2*64;
        int lo_[8];
        #pragma unroll
        for(int i=0;i<8;i++){ const int r=g+4*i; int c=c16<15?c16:14; const int part=c/5, cc=c-part*5; lo_[i]=r*272+c*16; gload16<0>(v[i],src,r*3456+part*1152+cc*16); }
        asm volatile("s_waitcnt vmcnt(0)" : "+v"(v[0]),"+v"(v[1]),"+v"(v[2]),"+v"(v[3]),"+v"(v[4]),"+v"(v[5]),"+v"(v[6]),"+v"(v[7]) :: "memory");
        #pragma unroll
        for(int i=0;i<8;i++) *(u32x4*)(smw+lo_[i])=v[i];
        asm volatile("s_waitcnt lgkmcnt(0)":::"memory");
        const rsrc_t rw=mkrs(ws+O_WQ+(size_t)(hq*2+h2)*144*1024,144*1024);
        #pragma unroll
        for(int t=0;t<8;t++){
          const int vc=(t<3? 16*t:16*(t-3))+4*pp;
          const int col= t<3? (vc<36? vc:0) : (vc<36? 40+vc : (vc<72? 80+vc-36 : 0));
          const unsigned ba=smwb+(8*g+q)*272+(h2*4+col)*2;
          u32x4 o=__builtin_bit_cast(u32x4,cat(trread(ba),trread(ba+4*272)));
          if((t<3? 16*t:16*(t-3))+c16>=(t<3?36:72)) o=(u32x4){0,0,0,0};
          ST16(o,rw,voff,(s*8+t)*1024); }
      } else if(b<240){
        const int jb=b-144, kap=jb>>2, ct=(jb&3)*2+(w-2);
        const int hf=kap/12, kl=kap%12;
        u32x4 v[5];
        const char* src=(const char*)p.wo+ct*144;
        bool okr[5]; int lo_[5];
        #pragma unroll
        for(int i=0;i<5;i++){ int e=lane+64*i; if(e>287) e=287; const int r=e/9, c=e-r*9, row=r&15, hi=r>>4;
          int kr; if(kl<8){ kr=(8*hf+kl)*36+(hi? 12+row: row-4); okr[i]= hi|| row>=4; } else { kr=(8*hf+2*(kl-8)+hi)*36+28+row; okr[i]=row<8; }
          lo_[i]=r*160+c*16; gload16<0>(v[i],src,(okr[i]?kr:0)*1152+c*16); }
        asm volatile("s_waitcnt vmcnt(0)" : "+v"(v[0]),"+v"(v[1]),"+v"(v[2]),"+v"(v[3]),"+v"(v[4]) :: "memory");
        #pragma unroll
        for(int i=0;i<5;i++) *(u32x4*)(smw+lo_[i])= okr[i]? v[i]:(u32x4){0,0,0,0};
        asm volatile("s_waitcnt lgkmcnt(0)":::"memory");
        const rsrc_t rw=mkrs(ws+O_WO+(size_t)ct*120*1024,120*1024);
        #pragma unroll
        for(int nt=0;nt<5;nt++){
          const int col=nt*16+4*((nt==4)?(pp&1):pp);
          const unsigned ba=smwb+(4*g+q)*160+col*2;
          ST16(cat(trread(ba),trread(ba+16*160)),rw,voff,(kap*5+nt)*1024); }
      }
    } else if(w==4 && rt<9){
      u32x4 v[5];
      const char* rp=(const char*)p.wo+(size_t)(hp*36+rt*4)*1152;
      const unsigned v0=g*1152+c16*16, v4=g*1152+(64+(c16&7))*16;
      gload16<0>(v[0],rp,v0); gload16<256>(v[1],rp,v0); gload16<512>(v[2],rp,v0); gload16<768>(v[3],rp,v0); gload16<0>(v[4],rp,v4);
      asm volatile("s_waitcnt vmcnt(0)" : "+v"(v[0]),"+v"(v[1]),"+v"(v[2]),"+v"(v[3]),"+v"(v[4]) :: "memory");
      float sacc=0;
      #pragma unroll
      for(int i=0;i<5;i++){ const unsigned one=(i<4||c16<8)?0x3f803f80u:0u;
        #pragma unroll
        for(int j=0;j<4;j++) sacc=dot2(v[i][j],one,sacc); }
      sacc+=__shfl_xor(sacc,1); sacc+=__shfl_xor(sacc,2); sacc+=__shfl_xor(sacc,4); sacc+=__shfl_xor(sacc,8);
      if(c16==0) ST4(sacc*(1.f/576.f),mkrs(ws+O_WB+(size_t)(hp*36+rt*4)*4,16),g*4,0);
    }
    u32x4 wm[WARM];
    if(w>=5){
      __builtin_amdgcn_s_sleep(WARM_SLEEP);
      const char* src=(const char*)(w==5? p.wg : (w==6? p.wu : p.wd))+(size_t)b*20736;
      #pragma unroll
      for(int i=0;i<WARM;i++){ unsigned o=i*1024+voff; if(o>20720) o=20720; gload16<0>(wm[i],src,o); }
    }
    STAMP(1);
    if(w<5) asm volatile("s_waitcnt vmcnt(0) lgkmcnt(0)":::"memory");
    BAR();
    { const u32x2 e=*(const u32x2*)(sm+TAG_LDS); unsigned lo=__builtin_amdgcn_readfirstlane(e[0]), hi=__builtin_amdgcn_readfirstlane(e[1]); tag=(((u64)hi<<32)|lo)+1ull; }
    if(tid<2 && (tid==0 || b<240)){
      const size_t fo= tid==0? O_F0+b*8 : (b<144? O_FW+(b&7)*256+(b>>3)*8 : O_FWO+((b-144)&3)*256+((b-144)>>2)*8);
      __hip_atomic_store((GAS u64*)(ws+fo),tag,__ATOMIC_RELAXED,__HIP_MEMORY_SCOPE_AGENT); }
    STAMP(2);
    if(w>=5){ waitv<0>();
      #pragma unroll
      for(int i=0;i<WARM;i++) asm volatile("" :: "v"(wm[i])); }
    if(w==0) pollp((const GAS u64*)(ws+(lane<16? O_F0+rt*128+lane*8 : O_FW+(hp>>1)*256+(lane<34?lane-16:0)*8)),tag);
    BAR();
  }
  STAMP(3);
  const int tt=rt*8+w;
  bf16x8 q01; u32x2 q2;
  {
    constexpr int LA=F1_LA, PF=6;
    const char* wbase=ws+O_WQ+(size_t)hp*144*1024+w*1024;
    const char* abase=ws+O_XN+(size_t)tt*18*1024;
    bf16x8 af[18];
    auto ld=[&](auto Sx){ constexpr int s=Sx.v; glds16c(wbase+s*8192,voff,smb+(s*8+w)*1024); gload16c<(s&3)*1024>(af[s],abase+(s>>2)*4096,voff); };
    sfor<0,LA>(ld);
    f32x4 acc[8];
    unsigned lwb[3];
    #pragma unroll
    for(int i=0;i<3;i++){ lwb[i]=smb+voff+i*49152; asm volatile("" : "+v"(lwb[i])); }
    bf16x8 wf[PF];
    #define LDW1(i) (*(const LAS bf16x8*)((const LAS char*)(size_t)lwb[(i)/48]+((i)%48)*1024))
    #define WAITP(m,n) asm volatile("s_waitcnt vmcnt(%2)" : "+v"(af[2*(m)]),"+v"(af[2*(m)+1]) : "i"(n) : "memory")
    WAITP(0,2*(LA-2)); BAR();
    STAMP(4);
    sfor<0,PF-1>([&](auto I){ wf[I.v]=LDW1(I.v); });
    sfor<0,144>([&](auto I){ constexpr int i=I.v; constexpr int s=i>>3,t=i&7;
      if constexpr((i&15)==8 && (i>>4)<8){ constexpr int m=(i>>4)+1; constexpr int iss=(LA+2*m-1)<18?(LA+2*m-1):18;
        WAITP(m,2*(iss-(2*m+2))); BAR(); }
      if constexpr(i+PF-1<144) wf[(i+PF-1)%PF]=LDW1(i+PF-1);
      SB();
      if constexpr(t<3){ if constexpr(s==0) acc[t]=MF16(wf[i%PF],af[s],(f32x4){}); else acc[t]=MF16(wf[i%PF],af[s],acc[t]); }
      else { if constexpr(s==0) acc[t]=MF16(af[s],wf[i%PF],(f32x4){}); else acc[t]=MF16(af[s],wf[i%PF],acc[t]); }
      SB();
      if constexpr(t==1 && s+LA<18) ld(IC<s+LA>{});
      SB();
    });
    STAMP(5);
    u32x2 kvb[5];
    #pragma unroll
    for(int t=0;t<5;t++){ float x[4];
      #pragma unroll
      for(int r4=0;r4<4;r4++){ float a=acc[3+t][r4];
        if(t<2) a=phi(a); else if(t==2) a= c16<4? phi(a):a; else if(t==4) a= c16<8? a : 1.f;
        x[r4]=a; }
      kvb[t][0]=pk2(x[0],x[1]); kvb[t][1]=pk2(x[2],x[3]); }
    BAR();
    { char* xs=sm+w*8192+voff;
      #define KVM(idx,kt,vt) *(f32x4*)(xs+(idx)*1024)=__builtin_amdgcn_mfma_f32_16x16x16bf16_1k(__builtin_bit_cast(s16x4,kvb[kt]),__builtin_bit_cast(s16x4,kvb[vt]),(f32x4){},0,0,0)
      KVM(0,0,2); KVM(1,1,2); KVM(2,0,3); KVM(3,1,3); KVM(4,2,3); KVM(5,0,4); KVM(6,1,4); KVM(7,2,4); }
    LBAR();
    { u32x4 v; v[0]=pk2(phi(acc[0][0]),phi(acc[0][1])); v[1]=pk2(phi(acc[0][2]),phi(acc[0][3])); v[2]=pk2(phi(acc[1][0]),phi(acc[1][1])); v[3]=pk2(phi(acc[1][2]),phi(acc[1][3]));
      q01=__builtin_bit_cast(bf16x8,v);
      q2[0]=pk2(phi(acc[2][0]),phi(acc[2][1])); q2[1]=pk2(phi(acc[2][2]),phi(acc[2][3])); }
  }
  const int kt=(w==0||w==2||w==5)?0:((w==1||w==3||w==6)?1:2), vt= w<2?2:(w<5?3:4);
  bool okv[4]; bool anyok=false;
  #pragma unroll
  for(int r4=0;r4<4;r4++){ int d=16*kt+4*g+r4, v=16*vt+c16;
    bool ok; if(v<72) ok= v>=36 && d<36 && ((d<18)==(v-36<18)); else ok=(c16==8||c16==12)? d<18 : ((c16==9||c16==13)? (d>=18&&d<36):false);
    okv[r4]=ok; anyok|=ok; }
  { const rsrc_t rkv=mkrs(ws+O_KVP+(size_t)hp*16*8192,16*8192);
    f32x4 sacc=*(const f32x4*)(sm+w*1024+voff);
    #pragma unroll
    for(int ww=1;ww<8;ww++) sacc+=*(const f32x4*)(sm+ww*8192+w*1024+voff);
    if(anyok) ST16(sacc,rkv,voff,rt*8192+w*1024); }
  publish(ws,O_FKV+hp*256+rt*8,tag,w);
  STAMP(6);
  const int ct3=idx&7, r3=xcd*4+(idx>>3);
  constexpr int WOL=36*1024;
  f32x4 wbv[3];
  if(w==0) pollp((const GAS u64*)(ws+(lane<16? O_FKV+hp*256+lane*8 : O_FWO+(ct3>>1)*256+(lane<40?lane-16:0)*8)),tag);
  BAR();
  { u32x4 pv[16];
    { const char* pb=ws+O_KVP+(size_t)hp*16*8192+w*1024; const unsigned vo=anyok?voff:0u;
      sfor<0,16>([&](auto I){ gload16c<0>(pv[I.v],pb+I.v*8192,vo); });
      const char* wbp=ws+O_WB+hp*144; const unsigned va=(g>0?g-1:0)*16, vc=(g<2?g:0)*16;
      gload16c<0>(wbv[0],wbp,va); gload16c<48>(wbv[1],wbp,g*16); gload16c<112>(wbv[2],wbp,vc); }
    asm volatile("s_waitcnt vmcnt(0)" : "+v"(pv[0]),"+v"(pv[1]),"+v"(pv[2]),"+v"(pv[3]),"+v"(pv[4]),"+v"(pv[5]),"+v"(pv[6]),"+v"(pv[7]),
       "+v"(pv[8]),"+v"(pv[9]),"+v"(pv[10]),"+v"(pv[11]),"+v"(pv[12]),"+v"(pv[13]),"+v"(pv[14]),"+v"(pv[15]),"+v"(wbv[0]),"+v"(wbv[1]),"+v"(wbv[2]) :: "memory");
    f32x4 sacc=__builtin_bit_cast(f32x4,pv[0]);
    #pragma unroll
    for(int i=1;i<16;i++) sacc+=__builtin_bit_cast(f32x4,pv[i]);
    u32x2 pk; pk[0]=pk2(okv[0]?sacc[0]:0.f,okv[1]?sacc[1]:0.f); pk[1]=pk2(okv[2]?sacc[2]:0.f,okv[3]?sacc[3]:0.f);
    *(u32x2*)(sm+w*512+lane*8)=pk; }
  LBAR();
  STAMP(7);
  { const char* kk=sm+lane*8; const u32x2 z2={0,0};
    #define KF(i) (*(const u32x2*)(kk+(i)*512))
    f32x4 A=MF16(cat2(KF(0),KF(1)),q01,(f32x4){});
    f32x4 B=MF16(cat2(KF(2),KF(3)),q01,(f32x4){}); B=MF16(cat2(KF(4),z2),cat2(q2,z2),B);
    f32x4 C=MF16(cat2(KF(5),KF(6)),q01,(f32x4){}); C=MF16(cat2(KF(7),z2),cat2(q2,z2),C);
    const float ra=__builtin_amdgcn_rcpf(bcast_hi(C[0])+1e-6f), rb=__builtin_amdgcn_rcpf(bcast_hi(C[1])+1e-6f);
    const float b01= g<2? ra:rb, b23= g<1? ra:rb, cc= g<2? rb:0.f;
    A*=ra; B[0]*=b01; B[1]*=b01; B[2]*=b23; B[3]*=b23; C*=cc;
    f32x4 t4=A*wbv[0]+B*wbv[1]+C*wbv[2]; const float mu=sum_g((t4[0]+t4[1])+(t4[2]+t4[3]));
    const rsrc_t ro=mkrs(ws+O_O+((size_t)tt*16+hp)*1536,1536);
    ST16(((u32x4){pk2(A[0],A[1]),pk2(A[2],A[3]),pk2(B[0],B[1]),pk2(B[2],B[3])}),ro,voff,0);
    ST8(((u32x2){pk2(C[0],C[1]),pk2(C[2],C[3])}),ro,lane*8,1024);
    const rsrc_t rm=mkrs(ws+O_MU+(size_t)tt*16*64,1024);
    if(lane<16) ST4(mu,rm,lane*64+hp*4,0);
  }
  publish(ws,O_FO+rt*256+hp*8,tag,w);
  STAMP(8);
  const int r1=(xcd&1)*4+(idx&3), fs=(xcd>>1)*8+(idx>>2);
  const int odd=fs&1;
  const char* wsrc=(const char*)((w>>2)? p.wu:p.wg)+(size_t)(fs*9+odd)*32;
  const unsigned wdst=smb+(w>>2)*73728+(w&3)*1024;
  unsigned vA,vC; u64 mB,mC;
  { const int r8=lane>>3, pos=lane&7, sl=(pos>>1)^((r8>>1)&3);
    vA=r8*(F*2)+sl*32+(pos&1)*16;
    vC=r8*(F*2)+(sl==3? 256:(odd?0:288))+(pos&1)*16;
    mB=__ballot(sl<3); mC=__ballot(sl==3||sl==0); }
  const int w3=w&3;
  auto wrow=[&](int kc0,int kst){ return wsrc+(size_t)(kc0+w3*kst)*(F*2); };
  {
    const int tq=w&3, hf=w>>2;
    const int t3=r3*4+tq;
    const int tok=t3*16+c16;
    if(w>0){
      const char* wob=ws+O_WO+(size_t)ct3*120*1024;
      #pragma unroll
      for(int i=0;i<18;i++){ int pc=(w-1)+7*i; if(pc>119) pc=119; glds16c(wob+pc*1024,voff,smb+WOL+pc*1024); }
    } else poll<16>(ws,O_FO+(r3>>1)*256,tag,lane);
    BAR();
    STAMP(9);
    u32x4 oab[8]; u32x2 oc[8]; u32x4 mu4; u32x2 xv[3]; unsigned mxu;
    { const char* ob=ws+O_O+((size_t)t3*16+hf*8)*1536;
      sfor<0,8>([&](auto I){ gload16c<0>(oab[I.v],ob+I.v*1536,voff); });
      #pragma unroll
      for(int i=0;i<8;i++) asm volatile("global_load_dwordx2 %0, %1, %2 offset:1024 sc1" : "=v"(oc[i]) : "v"(lane*8), "s"(ob+i*1536) : "memory");
      gload16c<0>(mu4,ws+O_MU+(size_t)t3*1024,c16*64+g*16);
      asm volatile("global_load_dword %0, %1, %2 sc1" : "=v"(mxu) : "v"(c16*4), "s"(ws+O_MX+(size_t)t3*64) : "memory");
      const char* xb=(const char*)p.x+(size_t)t3*16*1152+ct3*144;
      #pragma unroll
      for(int i=0;i<3;i++){ int nt= hf? 2+i : (i<2? i:1); int n=nt*16+4*g; if(n>=72) n=68;
        asm volatile("global_load_dwordx2 %0, %1, %2" : "=v"(xv[i]) : "v"(c16*1152+n*2), "s"(xb) : "memory"); } }
    waitv<21>();
    BAR();
    const char* rb=sm+WOL+hf*61440+voff;
    bf16x8 wpre[30];
    #pragma unroll
    for(int i=0;i<30;i++) wpre[i]=*(const bf16x8*)(rb+i*1024);
    SB();
    f32x4 acc[5]={};
    bf16x8 wl[30];
    sfor<0,6>([&](auto KL){ constexpr int kl=KL.v;
      asm volatile("s_waitcnt vmcnt(%1)" : "+v"(oab[kl]) : "i"(20-kl) : "memory");
      #pragma unroll
      for(int n=0;n<5;n++){ acc[n]=MF16(wpre[kl*5+n],__builtin_bit_cast(bf16x8,oab[kl]),acc[n]); wl[kl*5+n]=*(const bf16x8*)(rb+(30+kl*5+n)*1024); SB(); } });
    asm volatile("s_waitcnt vmcnt(0)" : "+v"(oab[6]),"+v"(oab[7]),"+v"(oc[0]),"+v"(oc[1]),"+v"(oc[2]),"+v"(oc[3]),"+v"(oc[4]),"+v"(oc[5]),"+v"(oc[6]),"+v"(oc[7]),"+v"(mu4),"+v"(mxu),"+v"(xv[0]),"+v"(xv[1]),"+v"(xv[2]) :: "memory");
    LBAR();
    sfor<0,18>([&](auto R){ constexpr int r=R.v;
      if constexpr(r>=2&&r<8){ if(w>=4) glds16(wrow(g1::krow(r),g1::kstep8(r)),vA,wdst+r*4096); } else glds16(wrow(g1::krow(r),g1::kstep8(r)),vA,wdst+r*4096); });
    #pragma unroll
    for(int kl=6;kl<12;kl++){ const bf16x8 ob= kl<8? __builtin_bit_cast(bf16x8,oab[kl<8?kl:0]) : cat2(oc[kl<8?0:2*(kl-8)],oc[kl<8?0:2*(kl-8)+1]);
      #pragma unroll
      for(int n=0;n<5;n++) acc[n]=MF16(wl[(kl-6)*5+n],ob,acc[n]); }
    STAMP(10);
    f32x4 keep[3];
    { char* xs=sm+8192+w*3072+voff;
      f32x4 a0=acc[0],a1=acc[1],a2=acc[2],a3=acc[3]; asm volatile("" : "+v"(a0),"+v"(a1),"+v"(a2),"+v"(a3));
      keep[0]=hf?a2:a0; keep[1]=hf?a3:a1; keep[2]=acc[4];
      *(f32x4*)(xs)=hf?a0:a2; *(f32x4*)(xs+1024)=hf?a1:a3; *(f32x4*)(xs+2048)=acc[4]; }
    const f32x4 m4=__builtin_bit_cast(f32x4,mu4);
    const float m=__uint_as_float(mxu)+sum_g((m4[0]+m4[1])+(m4[2]+m4[3]));
    LBAR();
    { const char* xr=sm+8192+(w^4)*3072+voff;
      const rsrc_t rc=mkrs(ws+O_C+(size_t)t3*18*1024,18*1024), rq=mkrs(ws+O_SSQ+(size_t)t3*16*64,1024);
      float qs=0; unsigned cp[6];
      #pragma unroll
      for(int i=0;i<3;i++){
        const f32x4 v=keep[i]+*(const f32x4*)(xr+i*1024);
        const float c0=(lo16(xv[i][0])+v[0])-m,c1=(hi16(xv[i][0])+v[1])-m,c2=(lo16(xv[i][1])+v[2])-m,c3=(hi16(xv[i][1])+v[3])-m;
        const float qq=(c0*c0+c1*c1)+(c2*c2+c3*c3);
        qs+= (i<2 || (hf && g<2))? qq:0.f;
        cp[2*i]=pk2(c0,c1); cp[2*i+1]=pk2(c2,c3); }
      ST16(((u32x4){cp[0],cp[1],cp[2],cp[3]}),rc,voff,(2*ct3+hf)*1024);
      if(hf && g<2) ST8(((u32x2){cp[4],cp[5]}),rc,((2*(ct3&1)+g)*16+c16)*16+((ct3>>1)&1)*8,(16+(ct3>>2))*1024);
      qs=sum_g(qs);
      if(lane<16){ ST4(qs,rq,lane*64+(ct3*2+hf)*4,0); if(ct3==0 && hf==0) ST4(m,mkrs(ws+O_MH+(size_t)t3*64,64),lane*4,0); }
    }
    publish(ws,O_FH+(r3>>2)*256+((r3&3)*8+ct3)*8,tag,w);
    if(b==0 && tid==64) __hip_atomic_store((GAS u64*)(ws+O_EP),tag,__ATOMIC_RELAXED,__HIP_MEMORY_SCOPE_AGENT);
  }
  STAMP(11);
  STAMP(12);
  {
    using namespace g1;
    const int t1=r1*16+w*2; const int whi=w>>2;
    const char* abase=ws+O_C+(size_t)t1*18*1024;
    if(w<4) sfor<2,8>([&](auto R){ constexpr int r=R.v; glds16(wrow(krow(r),kstep8(r)),vA,wdst+r*4096); });
    if(w==0) poll<32>(ws,O_FH+r1*256,tag,lane);
    waitv<0>();
    BAR();
    STAMP(13);
    bf16x8 af[18][2];
    f32x4 sq[2];
    { const char* sp=ws+O_SSQ+(size_t)t1*16*64; const unsigned vo=c16*64+g*16; gload16c<0>(sq[0],sp,vo); gload16c<1024>(sq[1],sp,vo); }
    auto loadA=[&](auto Sx,auto T){ constexpr int s=Sx.v,t=T.v; gload16c<(s&3)*1024>(af[s][t], abase+t*18*1024+(s>>2)*4096, voff); };
    sfor<0,LAK>([&](auto Sx){ loadA(Sx,IC<0>{}); loadA(Sx,IC<1>{}); });
    f32x4 acc[4][2][2];
    const unsigned lb0=smb+(4*g+q)*128+((((g&1)<<1)|(q>>1))*32)+pp*8;
    unsigned ab[4][2];
    bf16x8 wf[PF];
    float c1[2], oo[2], ea, pa;
    u32x4 hold[2];
    const rsrc_t ors=mkrs(ws+O_ACT+(size_t)t1*144*1024,2*144*1024);
    const int obase=((fs*9+odd)>>1)*1024;
    const int olast=((fs*9+(odd?0:8))>>1)*1024+(odd?8:0);
    auto rdW=[&](auto I){ constexpr int i=I.v, sl=slotOf(pT(i)), s=pS(i), mat=i&1;
      if constexpr(s==0){ ab[sl][mat]=(lb0^(sl*32))+mat*73728; asm volatile("" : "+v"(ab[sl][mat])); }
      if constexpr(s==9){ ab[sl][mat]+=36864; asm volatile("" : "+v"(ab[sl][mat])); }
      wf[i%PF]=cat(trread(ab[sl][mat]+(s%9)*4096),trread(ab[sl][mat]+(s%9)*4096+2048)); };
    sfor<0,PF-1>(rdW);
    auto epi=[&](auto E,auto K){ constexpr int e=E.v,kk=K.v; constexpr int ET=e>>3, t=(e>>2)&1, r=e&3;
      if constexpr(kk==0){ float gg=acc[ET&3][0][t][r], u=acc[ET&3][1][t][r]; ea=gg*c1[t]; pa=gg*u; asm volatile("" : "+v"(ea),"+v"(pa)); }
      if constexpr(kk==1){ ea=__builtin_amdgcn_exp2f(ea); asm volatile("" : "+v"(ea)); }
      if constexpr(kk==2){ ea=1.f+ea; asm volatile("" : "+v"(ea)); }
      if constexpr(kk==3){ ea=__builtin_amdgcn_rcpf(ea); asm volatile("" : "+v"(ea)); }
      if constexpr(kk==4){
        oo[r&1]=pa*ea;
        if constexpr((r&1)==0) asm volatile("" : "+v"(oo[0]));
        else {
          constexpr int hi=(ET==8||(ET&1)==0)?0:2;
          hold[t][hi+(r>>1)]=pk2(oo[0],oo[1]);
          asm volatile("" : "+v"(hold[t][hi+(r>>1)]));
          if constexpr(r==3){
            if constexpr(ET==8){ ST8(((u32x2){hold[t][0],hold[t][1]}),ors,voff,olast+t*144*1024); }
            else if constexpr((ET&1)==1){ ST16(hold[t],ors,voff,obase+t*144*1024+(ET>>1)*1024); }
          }
        }
      } };
    auto slot=[&](auto M){ constexpr int m=M.v-216-2;
      if constexpr(m>=0){ constexpr int e=(m*4+3)/27; constexpr int kk=m-(e*27)/4;
        if constexpr(e<64 && kk>=0 && kk<5) epi(IC<e>{},IC<kk>{}); } };
    sfor<0,324>([&](auto I){ constexpr int i=I.v; constexpr int T=pT(i), s=pS(i), mat=i&1, j=i-108;
      if constexpr(i==108) STAMP(14);
      if constexpr(i<108 && i%6==0){
        constexpr int iss=(s+LAK<18)?s+LAK:18;
        asm volatile("s_waitcnt vmcnt(%2)" : "+v"(af[s][0]),"+v"(af[s][1]) : "i"(2*(iss-s-1)) : "memory");
        if constexpr(i==0){ asm volatile("" : "+v"(sq[0]),"+v"(sq[1]));
          #pragma unroll
          for(int t=0;t<2;t++){ f32x4 s4=sq[t]; float ss=sum_g((s4[0]+s4[1])+(s4[2]+s4[3])); c1[t]=-1.44269504089f*__builtin_amdgcn_rsqf(ss*(1.f/576.f)+1e-5f); }
          asm volatile("" : "+v"(c1[0]),"+v"(c1[1])); } }
      if constexpr(j==2||j==74) BAR();
      if constexpr(j==31){ waitv<5>(); BAR(); }
      if constexpr(j==49||j==139){ waitv<0>(); BAR(); }
      if constexpr(i+PF-1<324) rdW(IC<i+PF-1>{});
      SB();
      if constexpr(s==0) acc[T&3][mat][0]=MF16(wf[i%PF],af[s][0],(f32x4){}); else acc[T&3][mat][0]=MF16(wf[i%PF],af[s][0],acc[T&3][mat][0]);
      SB();
      slot(IC<2*i>{});
      SB();
      if constexpr(s==0) acc[T&3][mat][1]=MF16(wf[i%PF],af[s][1],(f32x4){}); else acc[T&3][mat][1]=MF16(wf[i%PF],af[s][1],acc[T&3][mat][1]);
      SB();
      slot(IC<2*i+1>{});
      if constexpr(i<108 && (i%6==1||i%6==4) && s+LAK<18) loadA(IC<s+LAK>{},IC<(i%6)/3>{});
      if constexpr(j>=2 && j<=37){ constexpr int r=(j-2)/2; glds16ms(wrow(krow(r),kstep8(r))+128,vA,wdst+r*4096,mB,(j&1)? whi^1:whi); }
      if constexpr(j>=75 && j<=127 && (j-75)%3<2){ constexpr int r=(j-75)/3; glds16ms(wrow(krow(r),kstep8(r))-32,vC,wdst+r*4096,mC,((j-75)%3)? whi^1:whi); }
      SB();
    });
    STAMP(15);
    sfor<64,72>([&](auto E){ sfor<0,5>([&](auto K){ epi(E,K); }); });
    publish(ws,O_FA+r1*256+fs*8,tag,w);
  }
  STAMP(16);
  {
    using namespace g2;
    const int lane=fresh_lane(); const unsigned voff=lane*16; const int g=lane>>4, q=(lane>>2)&3, pp=lane&3, c16=lane&15;
    const int sp=xcd>>1, r2t=(xcd&1)*8+(idx>>2), ct=idx&3;
    const int tq=w&3, kh=w>>2;
    const int P0=sp*36+kh*18;
    const unsigned rb=smb+kh*RING*1024;
    const char* wbase=(const char*)p.wd+(size_t)P0*32*1152+ct*288;
    unsigned wv[9];
    #pragma unroll
    for(int i=0;i<9;i++){ const int pc=(4*i+tq); const int id=pc*64+lane; const int row=id/18; wv[i]=row*1152+(id-row*18)*16; }
    const char* abase=ws+O_ACT+((size_t)(r2t*8+tq*2)*144+P0)*1024;
    auto loadW=[&](auto R){ constexpr int r=R.v;
      if constexpr(r==NR-1){ if(tq<2) glds16(wbase+(size_t)(r/9)*128*1152, wv[r%9], rb+((4*r)%RING+tq)*1024); else glds16(wbase+(size_t)(r/9)*128*1152, wv[(r-1)%9], rb+((4*(r-1))%RING+tq)*1024); }
      else glds16(wbase+(size_t)(r/9)*128*1152, wv[r%9], rb+((4*r)%RING+tq)*1024); };
    sfor<0,PRE/4>(loadW);
    if(w==0) poll<8>(ws,O_FA+(r2t>>1)*256+sp*64,tag,lane);
    waitv<0>();
    BAR();
    STAMP(17);
    constexpr int AR=LAK+1;
    bf16x8 af[AR][2];
    auto loadA=[&](auto J,auto T){ constexpr int j=J.v,t=T.v; gload16c<0>(af[j%AR][t], abase+t*144*1024+j*1024, voff); };
    sfor<0,LAK>([&](auto J){ loadA(J,IC<0>{}); loadA(J,IC<1>{}); });
    f32x4 acc[9][2];
    unsigned lwb[2];
    #pragma unroll
    for(int i=0;i<2;i++){ lwb[i]=rb+(4*g+q)*288+pp*8+i*36864; asm volatile("" : "+v"(lwb[i])); }
    bf16x8 wf[PF];
    #define LDW2(i) cat(trread(lwb[(((i)/9)%8)/4]+((((i)/9)%8)%4)*9216+((i)%9)*32),trread(lwb[(((i)/9)%8)/4]+((((i)/9)%8)%4)*9216+((i)%9)*32+16*288))
    sfor<0,PF-1>([&](auto I){ wf[I.v]=LDW2(I.v); });
    sfor<0,NP>([&](auto I){ constexpr int i=I.v; constexpr int j=i/9, nt=i%9;
      if constexpr((i+PF)%18==0 && i+PF<NP){ if constexpr(i+PF+18>PRE) waitv<vmc(i,-1,i+PF+18)>(); BAR(); }
      if constexpr(nt==0) asm volatile("s_waitcnt vmcnt(%2)" : "+v"(af[j%AR][0]),"+v"(af[j%AR][1]) : "i"(vmc(i,j,0)) : "memory");
      if constexpr(i+PF-1<NP) wf[(i+PF-1)%PF]=LDW2(i+PF-1);
      SB();
      if constexpr(j==0) acc[nt][0]=MF16(wf[i%PF],af[j%AR][0],(f32x4){}); else acc[nt][0]=MF16(wf[i%PF],af[j%AR][0],acc[nt][0]);
      if constexpr(j==0) acc[nt][1]=MF16(wf[i%PF],af[j%AR][1],(f32x4){}); else acc[nt][1]=MF16(wf[i%PF],af[j%AR][1],acc[nt][1]);
      SB();
      if constexpr(nt==0 && j+LAK<18) loadA(IC<j+LAK>{},IC<0>{});
      if constexpr(nt==4 && j+LAK<18) loadA(IC<j+LAK>{},IC<1>{});
      if constexpr((i+LWP)%4==0 && (i+LWP)/4<NR && (i+LWP)/4>=PRE/4) loadW(IC<(i+LWP)/4>{});
      SB();
    });
    STAMP(18);
    BAR();
    { char* xs=sm+(w*9)*1024+voff;
      #pragma unroll
      for(int n=0;n<9;n++){ f32x4 v= kh? acc[n][0]:acc[n][1]; *(f32x4*)(xs+n*1024)=v; } }
    LBAR();
    f32x4 part[10];
    { const char* xr=sm+((w^4)*9)*1024+voff;
      #pragma unroll
      for(int n=0;n<9;n++){ f32x4 mine= kh? acc[n][1]:acc[n][0]; part[n]=mine+*(const f32x4*)(xr+n*1024); }
      part[9]=part[8]; }
    const int grp=r2t*4+ct;
    const rsrc_t xr=mkrs(ws+O_X+(size_t)grp*160*1024,160*1024);
    if(tq!=sp){
      #pragma unroll
      for(int n=0;n<5;n++) ST16(((u32x4){pk2(part[2*n][0],part[2*n][1]),pk2(part[2*n][2],part[2*n][3]),pk2(part[2*n+1][0],part[2*n+1][1]),pk2(part[2*n+1][2],part[2*n+1][3])}),xr,voff,((tq*4+sp)*10+kh*5+n)*1024);
    } else {
      char* xs=sm+(72+kh*10)*1024+voff;
      #pragma unroll
      for(int n=0;n<10;n++) *(f32x4*)(xs+n*1024)=part[n];
    }
    publish(ws,O_FX+grp*128+sp*8,tag,w);
    STAMP(19);
    const int tk0=r2t*128+sp*32;
    const rsrc_t rh=mkrs(ws+O_C+(size_t)(tk0>>4)*18432,2*18432), rq=mkrs(ws+O_SSQ+(size_t)tk0*64,2048), rmh=mkrs(ws+O_MH+(size_t)tk0*4,128);
    u32x2 hv[2][2]; float r2[2], mh[2]; int go[2][2];
    #pragma unroll
    for(int x=0;x<2;x++){ int u=w+8*x; if(u>9) u=9; const int tl=u>=5, pr=u-5*tl;
      #pragma unroll
      for(int y=0;y<2;y++){ int nt=2*pr+y; if(nt>8) nt=8; const int nl=nt*16+g*4;
        go[x][y]=(tl*16+c16)*1152+(ct*144+nl)*2;
        const int c3=ct*2+(nl>=72), o72=nl>=72? nl-72:nl;
        int kc,gp,hfh; if(o72<64){ kc=2*c3+(o72>>5); hfh=(o72>>4)&1; gp=(o72>>2)&3; } else { kc=16+(c3>>2); gp=2*(c3&1)+((o72-64)>>2); hfh=(c3>>1)&1; }
        hv[x][y]=LD8(rh,(tl*18+kc)*1024+(gp*16+c16)*16+hfh*8,0); }
      const f32x4 s4=__builtin_bit_cast(f32x4,LD16(rq,(tl*16+c16)*64+g*16,0));
      mh[x]=__uint_as_float(__builtin_amdgcn_raw_buffer_load_b32(rmh,(tl*16+c16)*4,0,16));
      r2[x]=__builtin_amdgcn_rcpf(sum_g((s4[0]+s4[1])+(s4[2]+s4[3]))*(1.f/576.f)+1e-5f); }
    if(w==0) poll<4>(ws,O_FX+grp*128,tag,lane);
    BAR();
    STAMP(20);
    u32x4 rv[2][3];
    #pragma unroll
    for(int x=0;x<2;x++){ int u=w+8*x; if(u>9) u=9;
      #pragma unroll
      for(int y=0;y<3;y++){ int src=(sp+1+y)&3; rv[x][y]=LD16(xr,voff,((sp*4+src)*10+u)*1024); } }
    char* const ob=(char*)p.out+(size_t)(r2t*128+sp*32)*1152;
    #pragma unroll
    for(int x=0;x<2;x++){ if(x==0 || w<2){ const int u=w+8*x; const int tl=u>=5, pr=u-5*tl;
      #pragma unroll
      for(int y=0;y<2;y++){ if(y==0 || pr<4){
        f32x4 v=*(const f32x4*)(sm+(72+tl*10+2*pr+y)*1024+voff);
        #pragma unroll
        for(int z=0;z<3;z++){ v[0]+=lo16(rv[x][z][2*y]); v[1]+=hi16(rv[x][z][2*y]); v[2]+=lo16(rv[x][z][2*y+1]); v[3]+=hi16(rv[x][z][2*y+1]); }
        u32x2 ov; ov[0]=pk2(v[0]*r2[x]+(lo16(hv[x][y][0])+mh[x]),v[1]*r2[x]+(hi16(hv[x][y][0])+mh[x])); ov[1]=pk2(v[2]*r2[x]+(lo16(hv[x][y][1])+mh[x]),v[3]*r2[x]+(hi16(hv[x][y][1])+mh[x]));
        *(u32x2*)(ob+go[x][y])=ov; } } } }
  }
  STAMP(21);
  STAMP_OUT();
}
}

extern "C" void kernel_launch(void* const* d_in, const int* in_sizes, int n_in,
                              void* d_out, int out_size, void* d_ws, size_t ws_size,
                              hipStream_t stream) {
  using namespace k;
  Params p;
  p.x=(const u16*)d_in[0]; p.wqkv=(const u16*)d_in[1]; p.wo=(const u16*)d_in[2];
  p.wg=(const u16*)d_in[4]; p.wu=(const u16*)d_in[5]; p.wd=(const u16*)d_in[6];
  p.out=(u16*)d_out; p.ws=(char*)d_ws;
  fused_k<<<256,512,0,stream>>>(p);
}
```

```cpp
#include <hip/hip_runtime.h>
#include <hip/hip_bf16.h>
typedef unsigned short u16;
typedef unsigned long long u64;
typedef short s16x4 __attribute__((ext_vector_type(4)));
typedef short s16x8 __attribute__((ext_vector_type(8)));
typedef __bf16 bf16x8 __attribute__((ext_vector_type(8)));
typedef __bf16 bf16x2 __attribute__((ext_vector_type(2)));
typedef float f32x4 __attribute__((ext_vector_type(4)));
typedef unsigned u32x4 __attribute__((ext_vector_type(4)));
typedef unsigned u32x2 __attribute__((ext_vector_type(2)));
#define LAS __attribute__((address_space(3)))
#define GAS __attribute__((address_space(1)))
#define DI __device__ __forceinline__

#define STAMP(i)
#define STAMP_OUT()

#define WARM 21
#define WARM_SLEEP 20
#define STAG 1
#define PRETOUCH 1
#define CODE_KB 48
#define WARM_MODE 1
#define TOUCH_AT 121
#define F0_UNITS 0
#define TOUCH_D 50
#define F1_LA 6
namespace k {
constexpr int S=2048, D=576, F=4608;
constexpr int STAMP_LDS=156*1024, TAG_LDS=155*1024;
DI int fresh_lane(){ int l; asm volatile("v_mbcnt_lo_u32_b32 %0, -1, 0\n\tv_mbcnt_hi_u32_b32 %0, -1, %0" : "=v"(l)); return l; }
DI u16 f2bf(float f){ __bf16 b=(__bf16)f; return __builtin_bit_cast(u16,b); }
DI unsigned pk2(float lo,float hi){ bf16x2 v; v[0]=(__bf16)lo; v[1]=(__bf16)hi; return __builtin_bit_cast(unsigned,v); }
DI float lo16(unsigned v){ return __uint_as_float(v<<16); }
DI float hi16(unsigned v){ return __uint_as_float(v&0xffff0000u); }
DI float phi(float t){ return (t>0.f? t+1.f: __builtin_amdgcn_exp2f(t*1.44269504089f))+1e-6f; }
DI float bcast_hi(float x){ unsigned a=__float_as_uint(x); auto r=__builtin_amdgcn_permlane32_swap(a,a,false,false); return __uint_as_float(r[1]); }
DI float sum_g(float x){
  unsigned a=__float_as_uint(x); auto r=__builtin_amdgcn_permlane32_swap(a,a,false,false); x=__uint_as_float(r[0])+__uint_as_float(r[1]);
  a=__float_as_uint(x); auto r2=__builtin_amdgcn_permlane16_swap(a,a,false,false); return __uint_as_float(r2[0])+__uint_as_float(r2[1]); }
DI float dot2(unsigned a,unsigned b,float c){ return __builtin_amdgcn_fdot2_f32_bf16(__builtin_bit_cast(bf16x2,a),__builtin_bit_cast(bf16x2,b),c,false); }
DI s16x4 trread(unsigned a){ return __builtin_amdgcn_ds_read_tr16_b64_v4i16((LAS s16x4*)(size_t)a); }
DI bf16x8 cat(s16x4 a,s16x4 b){ return __builtin_bit_cast(bf16x8,__builtin_shufflevector(a,b,0,1,2,3,4,5,6,7)); }
DI bf16x8 cat2(u32x2 a,u32x2 b){ return __builtin_bit_cast(bf16x8,(u32x4){a[0],a[1],b[0],b[1]}); }
#define BAR() do{ asm volatile("":::"memory"); __builtin_amdgcn_s_barrier(); asm volatile("":::"memory"); }while(0)
#define LBAR() do{ asm volatile("s_waitcnt lgkmcnt(0)":::"memory"); __builtin_amdgcn_s_barrier(); asm volatile("":::"memory"); }while(0)
#define MF16(a,bb,c) __builtin_amdgcn_mfma_f32_16x16x32_bf16(a,bb,c,0,0,0)
#define SB() __builtin_amdgcn_sched_barrier(0)
#define WAITV0() __builtin_amdgcn_s_waitcnt(0x0F70)
template<int I> struct IC{ static constexpr int v=I; };
template<int B,int E,class Fn> DI void sfor(Fn&& f){ if constexpr(B<E){ f(IC<B>{}); sfor<B+1,E>(f); } }
DI void glds16(const void* sbase, unsigned voff, unsigned lds){
  unsigned keep;
  asm volatile("s_mov_b32 %0, m0\n\ts_mov_b32 m0, %3\n\ts_nop 0\n\tglobal_load_lds_dwordx4 %1, %2\n\ts_mov_b32 m0, %0" : "=&s"(keep) : "v"(voff), "s"(sbase), "s"(lds) : "memory");
}
DI void glds16c(const void* sbase, unsigned voff, unsigned lds){
  unsigned keep;
  asm volatile("s_mov_b32 %0, m0\n\ts_mov_b32 m0, %3\n\ts_nop 0\n\tglobal_load_lds_dwordx4 %1, %2 sc1\n\ts_mov_b32 m0, %0" : "=&s"(keep) : "v"(voff), "s"(sbase), "s"(lds) : "memory");
}
DI void glds16m(const void* sbase, unsigned voff, unsigned lds, u64 mask){
  unsigned keep;
  asm volatile("s_mov_b32 %0, m0\n\ts_mov_b32 m0, %3\n\ts_mov_b64 exec, %4\n\tglobal_load_lds_dwordx4 %1, %2\n\ts_mov_b64 exec, -1\n\ts_mov_b32 m0, %0" : "=&s"(keep) : "v"(voff), "s"(sbase), "s"(lds), "s"(mask) : "memory");
}
DI void glds16ms(const void* sbase, unsigned voff, unsigned lds, u64 mask, int skip){
  unsigned keep;
  asm volatile("s_cmp_lg_u32 %5, 0\n\ts_cbranch_scc1 1f\n\ts_mov_b32 %0, m0\n\ts_mov_b32 m0, %3\n\ts_mov_b64 exec, %4\n\tglobal_load_lds_dwordx4 %1, %2\n\ts_mov_b64 exec, -1\n\ts_mov_b32 m0, %0\n1:" : "=&s"(keep) : "v"(voff), "s"(sbase), "s"(lds), "s"(mask), "s"(skip) : "memory","scc");
}
template<int OFF,class T> DI void gload16(T& r,const void* sbase,unsigned voff){ asm volatile("global_load_dwordx4 %0, %1, %2 offset:%3" : "=v"(r) : "v"(voff), "s"(sbase), "i"(OFF) : "memory"); }
template<int OFF,class T> DI void gload16c(T& r,const void* sbase,unsigned voff){ asm volatile("global_load_dwordx4 %0, %1, %2 offset:%3 sc1" : "=v"(r) : "v"(voff), "s"(sbase), "i"(OFF) : "memory"); }
DI void gtouch(unsigned& r,const void* sbase,unsigned voff){ asm volatile("global_load_dword %0, %1, %2" : "=v"(r) : "v"(voff), "s"(sbase) : "memory"); }
DI void asm_use(unsigned r){ asm volatile("" :: "v"(r)); }
template<int N> DI void waitv(){ asm volatile("s_waitcnt vmcnt(%0)" :: "i"(N) : "memory"); }
typedef __amdgpu_buffer_rsrc_t rsrc_t;
DI rsrc_t mkrs(const void* p,unsigned bytes){ return __builtin_amdgcn_make_buffer_rsrc((void*)p,0,bytes,0x00020000); }
#define ST16(v,rs,vo,so) __builtin_amdgcn_raw_buffer_store_b128(__builtin_bit_cast(u32x4,v),rs,vo,so,16)
#define ST8(v,rs,vo,so)  __builtin_amdgcn_raw_buffer_store_b64(__builtin_bit_cast(u32x2,v),rs,vo,so,16)
#define ST4(v,rs,vo,so)  __builtin_amdgcn_raw_buffer_store_b32(__float_as_uint(v),rs,vo,so,16)
#define LD16(rs,vo,so) __builtin_amdgcn_raw_buffer_load_b128(rs,vo,so,16)
#define LD8(rs,vo,so)  __builtin_amdgcn_raw_buffer_load_b64(rs,vo,so,16)

constexpr size_t O_EP=0;
constexpr size_t O_F0 =256;
constexpr size_t O_FW =O_F0+2048;
constexpr size_t O_FWO=O_FW+2048;
constexpr size_t O_FKV=O_FWO+1024;
constexpr size_t O_FO =O_FKV+16*256;
constexpr size_t O_FH =O_FO+16*256;
constexpr size_t O_FA =O_FH+8*256;
constexpr size_t O_FX =O_FA+8*256;
constexpr size_t O_KVP=32768;
constexpr size_t O_XN =O_KVP+(size_t)256*8192;
constexpr size_t O_MX =O_XN+(size_t)S*D*2;
constexpr size_t O_WB =O_MX+(size_t)S*4;
constexpr size_t O_WQ =O_WB+4096;
constexpr size_t O_WO =O_WQ+(size_t)16*144*1024;
constexpr size_t O_O  =O_WO+(size_t)8*120*1024;
constexpr size_t O_MU =O_O+(size_t)128*16*1536;
constexpr size_t O_MH =O_MU+(size_t)S*64;
constexpr size_t O_C  =O_MH+(size_t)S*4;
constexpr size_t O_SSQ=O_C+(size_t)S*D*2;
constexpr size_t O_ACT=O_SSQ+(size_t)S*64;
constexpr size_t O_X  =O_ACT+(size_t)S*F*2;
constexpr size_t O_END=O_X+(size_t)64*160*1024;
struct Params { const u16 *x,*wqkv,*wo,*wg,*wu,*wd; u16* out; char* ws; };

DI void publish(char* ws,size_t off,u64 tag,int w){
  asm volatile("s_waitcnt vmcnt(0) lgkmcnt(0)":::"memory");
  BAR();
  if(w==0 && fresh_lane()==0) __hip_atomic_store((GAS u64*)(ws+off),tag,__ATOMIC_RELAXED,__HIP_MEMORY_SCOPE_AGENT);
}
DI void pollp(const GAS u64* p,u64 tag){
  for(unsigned spins=0; spins<(1u<<20); ++spins){
    u64 v=__hip_atomic_load(p,__ATOMIC_RELAXED,__HIP_MEMORY_SCOPE_AGENT);
    if(__all(v==tag)) break;
    __builtin_amdgcn_s_sleep(1);
  }
  __builtin_amdgcn_fence(__ATOMIC_ACQUIRE,"wavefront");
}
template<int N> DI void poll(char* ws,size_t off,u64 tag,int lane){ pollp((const GAS u64*)(ws+off)+(lane<N?lane:0),tag); }

namespace g1 {
constexpr int PF=4, LAK=5;
constexpr int pT(int i){ return i<108? (i%6)/2 : 3+(i-108)/36; }
constexpr int pS(int i){ return i<108? i/6 : ((i-108)%36)/2; }
constexpr int slotOf(int T){ return T<4? T : (T<8? T-4 : 0); }
constexpr int krow(int kc){ return kc<16? 72*(kc>>1)+32*(kc&1) : 288*(kc-16)+64; }
constexpr int kstep8(int kc){ return kc<16? 8:72; }
}
namespace g2 {
constexpr int LWP=44, PRE=72, LAK=5, PF=5, NP=162, RING=72, NR=41;
constexpr int vmc(int iw,int ja,int pw){
  int cnt=0;
  for(int j=0;j<LAK;j++){ if(j<=ja) cnt=0; else cnt+=2; }
  for(int i=0;i<iw;i++){
    if((i%9==0||i%9==4) && i/9+LAK<18){ if(i/9+LAK<=ja) cnt=0; else cnt++; }
    if((i+LWP)%4==0 && (i+LWP)/4<NR && (i+LWP)/4>=PRE/4){ if(i+LWP<pw) cnt=0; else cnt++; }
  }
  return cnt;
}
}

__global__ __launch_bounds__(512,2) void fused_k(Params p){
  __shared__ __attribute__((aligned(16))) char sm[160*1024];
  const int tid=threadIdx.x, lane=tid&63, w=__builtin_amdgcn_readfirstlane(tid>>6);
  const int b=blockIdx.x, xcd=b&7, idx=b>>3;
  const unsigned smb=(unsigned)(size_t)(LAS char*)sm;
  const unsigned voff=lane*16;
  const int g=lane>>4, q=(lane>>2)&3, pp=lane&3, c16=lane&15;
  char* const ws=p.ws;
  STAMP(0);
  u64 tag;
  const int hp=(xcd&1)*8+(idx>>2), rt=(xcd>>1)*4+(idx&3);
  unsigned wm[2]={0,0};
  auto touch_gu=[&](int slot,int unit,int lo){
    int ln=unit*64+lane; if(ln>323) ln=323; const int mt=ln>=162; ln-=mt*162;
    const char* src=(const char*)(mt? p.wu : p.wg)+(size_t)b*20736+ln*128;
    asm volatile("s_cmp_lt_u32 %2, %3\n\ts_cbranch_scc1 1f\n\tglobal_load_dword %0, %1, off\n1:" : "+v"(wm[slot]) : "v"(src), "s"(w), "s"(lo) : "memory","scc"); };
  {
    char* const smw=sm+w*16384;
    const unsigned smwb=smb+w*16384;
    if(w<2){
      u32x2 ep; { const unsigned z=0; asm volatile("global_load_dwordx2 %0, %1, %2 sc1" : "=v"(ep) : "v"(z), "s"(ws+(w?(size_t)O_XN:0)) : "memory"); }
      u32x4 v[5];
      { const char* xb=(const char*)p.x+(size_t)(b*8+w*4)*1152; const unsigned v0=g*1152+c16*16, v4=g*1152+(64+(c16&7))*16;
        gload16<0>(v[0],xb,v0); gload16<256>(v[1],xb,v0); gload16<512>(v[2],xb,v0); gload16<768>(v[3],xb,v0); gload16<0>(v[4],xb,v4); }
      asm volatile("s_waitcnt vmcnt(0)" : "+v"(ep),"+v"(v[0]),"+v"(v[1]),"+v"(v[2]),"+v"(v[3]),"+v"(v[4]) :: "memory");
      if(tid==0) *(u32x2*)(sm+TAG_LDS)=ep;
      float s1=0;
      #pragma unroll
      for(int i=0;i<5;i++){ const unsigned one=(i<4||c16<8)?0x3f803f80u:0u;
        #pragma unroll
        for(int j=0;j<4;j++) s1=dot2(v[i][j],one,s1); }
      s1+=__shfl_xor(s1,1); s1+=__shfl_xor(s1,2); s1+=__shfl_xor(s1,4); s1+=__shfl_xor(s1,8);
      const float m=s1*(1.f/576.f);
      float f[5][8]; float s2=0,s2t=0;
      #pragma unroll
      for(int i=0;i<5;i++)
        #pragma unroll
        for(int j=0;j<4;j++){ f[i][2*j]=lo16(v[i][j])-m; f[i][2*j+1]=hi16(v[i][j])-m;
          if(i<4){ s2=fmaf(f[i][2*j],f[i][2*j],s2); s2=fmaf(f[i][2*j+1],f[i][2*j+1],s2); } else { s2t=fmaf(f[i][2*j],f[i][2*j],s2t); s2t=fmaf(f[i][2*j+1],f[i][2*j+1],s2t); } }
      s2+= c16<8? s2t:0.f;
      s2+=__shfl_xor(s2,1); s2+=__shfl_xor(s2,2); s2+=__shfl_xor(s2,4); s2+=__shfl_xor(s2,8);
      const float r=__builtin_amdgcn_rsqf(s2*(1.f/576.f)+1e-5f);
      #pragma unroll
      for(int i=0;i<5;i++){ u32x4 o;
        #pragma unroll
        for(int j=0;j<4;j++) o[j]=pk2(f[i][2*j]*r,f[i][2*j+1]*r);
        *(u32x4*)(smw+(i<4? c16+16*i: 64+(c16&7))*80+g*16)=o; }
      asm volatile("s_waitcnt lgkmcnt(0)":::"memory");
      const rsrc_t rx=mkrs(ws+O_XN+(size_t)(b>>1)*18432+(b&1)*128+w*64,18432);
      #pragma unroll
      for(int i=0;i<5;i++){ const int ch=(lane>>2)+16*i; if(ch<72) ST16(*(const u32x4*)(smw+ch*80+pp*16),rx,ch*256+pp*16,0); }
      const rsrc_t rm=mkrs(ws+O_MX+(size_t)b*32+w*16,16);
      if(c16==0) ST4(m,rm,g*4,0);
    } else if(w<4){
      if(b<144){
        const int s=b>>3, hq=b&7, h2=w-2;
        u32x4 v[8];
        const char* src=(const char*)p.wqkv+(size_t)s*32*3456+hq*144+h2*64;
        int lo_[8];
        #pragma unroll
        for(int i=0;i<8;i++){ const int r=g+4*i; int c=c16<15?c16:14; const int part=c/5, cc=c-part*5; lo_[i]=r*272+c*16; gload16<0>(v[i],src,r*3456+part*1152+cc*16); }
        asm volatile("s_waitcnt vmcnt(0)" : "+v"(v[0]),"+v"(v[1]),"+v"(v[2]),"+v"(v[3]),"+v"(v[4]),"+v"(v[5]),"+v"(v[6]),"+v"(v[7]) :: "memory");
        #pragma unroll
        for(int i=0;i<8;i++) *(u32x4*)(smw+lo_[i])=v[i];
        asm volatile("s_waitcnt lgkmcnt(0)":::"memory");
        const rsrc_t rw=mkrs(ws+O_WQ+(size_t)(hq*2+h2)*144*1024,144*1024);
        #pragma unroll
        for(int t=0;t<8;t++){
          const int vc=(t<3? 16*t:16*(t-3))+4*pp;
          const int col= t<3? (vc<36? vc:0) : (vc<36? 40+vc : (vc<72? 80+vc-36 : 0));
          const unsigned ba=smwb+(8*g+q)*272+(h2*4+col)*2;
          u32x4 o=__builtin_bit_cast(u32x4,cat(trread(ba),trread(ba+4*272)));
          if((t<3? 16*t:16*(t-3))+c16>=(t<3?36:72)) o=(u32x4){0,0,0,0};
          ST16(o,rw,voff,(s*8+t)*1024); }
      } else if(b<240){
        const int jb=b-144, kap=jb>>2, ct=(jb&3)*2+(w-2);
        const int hf=kap/12, kl=kap%12;
        u32x4 v[5];
        const char* src=(const char*)p.wo+ct*144;
        bool okr[5]; int lo_[5];
        #pragma unroll
        for(int i=0;i<5;i++){ int e=lane+64*i; if(e>287) e=287; const int r=e/9, c=e-r*9, row=r&15, hi=r>>4;
          int kr; if(kl<8){ kr=(8*hf+kl)*36+(hi? 12+row: row-4); okr[i]= hi|| row>=4; } else { kr=(8*hf+2*(kl-8)+hi)*36+28+row; okr[i]=row<8; }
          lo_[i]=r*160+c*16; gload16<0>(v[i],src,(okr[i]?kr:0)*1152+c*16); }
        asm volatile("s_waitcnt vmcnt(0)" : "+v"(v[0]),"+v"(v[1]),"+v"(v[2]),"+v"(v[3]),"+v"(v[4]) :: "memory");
        #pragma unroll
        for(int i=0;i<5;i++) *(u32x4*)(smw+lo_[i])= okr[i]? v[i]:(u32x4){0,0,0,0};
        asm volatile("s_waitcnt lgkmcnt(0)":::"memory");
        const rsrc_t rw=mkrs(ws+O_WO+(size_t)ct*120*1024,120*1024);
        #pragma unroll
        for(int nt=0;nt<5;nt++){
          const int col=nt*16+4*((nt==4)?(pp&1):pp);
          const unsigned ba=smwb+(4*g+q)*160+col*2;
          ST16(cat(trread(ba),trread(ba+16*160)),rw,voff,(kap*5+nt)*1024); }
      }
    } else if(w==4 && rt<9){
      u32x4 v[5];
      const char* rp=(const char*)p.wo+(size_t)(hp*36+rt*4)*1152;
      const unsigned v0=g*1152+c16*16, v4=g*1152+(64+(c16&7))*16;
      gload16<0>(v[0],rp,v0); gload16<256>(v[1],rp,v0); gload16<512>(v[2],rp,v0); gload16<768>(v[3],rp,v0); gload16<0>(v[4],rp,v4);
      asm volatile("s_waitcnt vmcnt(0)" : "+v"(v[0]),"+v"(v[1]),"+v"(v[2]),"+v"(v[3]),"+v"(v[4]) :: "memory");
      float sacc=0;
      #pragma unroll
      for(int i=0;i<5;i++){ const unsigned one=(i<4||c16<8)?0x3f803f80u:0u;
        #pragma unroll
        for(int j=0;j<4;j++) sacc=dot2(v[i][j],one,sacc); }
      sacc+=__shfl_xor(sacc,1); sacc+=__shfl_xor(sacc,2); sacc+=__shfl_xor(sacc,4); sacc+=__shfl_xor(sacc,8);
      if(c16==0) ST4(sacc*(1.f/576.f),mkrs(ws+O_WB+(size_t)(hp*36+rt*4)*4,16),g*4,0);
    }
    if(w>=5){ __builtin_amdgcn_s_sleep(WARM_SLEEP); touch_gu(1,F0_UNITS? (w-5):(w==5?4:5),5); }
    STAMP(1);
    if(w<5) asm volatile("s_waitcnt vmcnt(0) lgkmcnt(0)":::"memory");
    BAR();
    { const u32x2 e=*(const u32x2*)(sm+TAG_LDS); unsigned lo=__builtin_amdgcn_readfirstlane(e[0]), hi=__builtin_amdgcn_readfirstlane(e[1]); tag=(((u64)hi<<32)|lo)+1ull; }
    if(tid<2 && (tid==0 || b<240)){
      const size_t fo= tid==0? O_F0+b*8 : (b<144? O_FW+(b&7)*256+(b>>3)*8 : O_FWO+((b-144)&3)*256+((b-144)>>2)*8);
      __hip_atomic_store((GAS u64*)(ws+fo),tag,__ATOMIC_RELAXED,__HIP_MEMORY_SCOPE_AGENT); }
    STAMP(2);
    if(w==0) pollp((const GAS u64*)(ws+(lane<16? O_F0+rt*128+lane*8 : O_FW+(hp>>1)*256+(lane<34?lane-16:0)*8)),tag);
    BAR();
  }
  STAMP(3);
  const int tt=rt*8+w;
  bf16x8 q01; u32x2 q2;
  {
    constexpr int LA=F1_LA, PF=6;
    const char* wbase=ws+O_WQ+(size_t)hp*144*1024+w*1024;
    const char* abase=ws+O_XN+(size_t)tt*18*1024;
    bf16x8 af[18];
    auto ld=[&](auto Sx){ constexpr int s=Sx.v; glds16c(wbase+s*8192,voff,smb+(s*8+w)*1024); gload16c<(s&3)*1024>(af[s],abase+(s>>2)*4096,voff); };
    sfor<0,LA>(ld);
    f32x4 acc[8];
    unsigned lwb[3];
    #pragma unroll
    for(int i=0;i<3;i++){ lwb[i]=smb+voff+i*49152; asm volatile("" : "+v"(lwb[i])); }
    bf16x8 wf[PF];
    #define LDW1(i) (*(const LAS bf16x8*)((const LAS char*)(size_t)lwb[(i)/48]+((i)%48)*1024))
    #define WAITP(m,n) asm volatile("s_waitcnt vmcnt(%2)" : "+v"(af[2*(m)]),"+v"(af[2*(m)+1]) : "i"(n) : "memory")
    WAITP(0,2*(LA-2)); BAR();
    STAMP(4);
    sfor<0,PF-1>([&](auto I){ wf[I.v]=LDW1(I.v); });
    sfor<0,144>([&](auto I){ constexpr int i=I.v; constexpr int s=i>>3,t=i&7;
      if constexpr((i&15)==8 && (i>>4)<8){ constexpr int m=(i>>4)+1; constexpr int iss=(LA+2*m-1)<18?(LA+2*m-1):18;
        WAITP(m,2*(iss-(2*m+2))); BAR(); }
      if constexpr(i+PF-1<144) wf[(i+PF-1)%PF]=LDW1(i+PF-1);
      SB();
      if constexpr(t<3){ if constexpr(s==0) acc[t]=MF16(wf[i%PF],af[s],(f32x4){}); else acc[t]=MF16(wf[i%PF],af[s],acc[t]); }
      else { if constexpr(s==0) acc[t]=MF16(af[s],wf[i%PF],(f32x4){}); else acc[t]=MF16(af[s],wf[i%PF],acc[t]); }
      SB();
      if constexpr(t==1 && s+LA<18) ld(IC<s+LA>{});
      if constexpr(i==TOUCH_AT) touch_gu(0,F0_UNITS? (w-2):(w-4),F0_UNITS?5:4);
      SB();
    });
    STAMP(5);
    u32x2 kvb[5];
    #pragma unroll
    for(int t=0;t<5;t++){ float x[4];
      #pragma unroll
      for(int r4=0;r4<4;r4++){ float a=acc[3+t][r4];
        if(t<2) a=phi(a); else if(t==2) a= c16<4? phi(a):a; else if(t==4) a= c16<8? a : 1.f;
        x[r4]=a; }
      kvb[t][0]=pk2(x[0],x[1]); kvb[t][1]=pk2(x[2],x[3]); }
    BAR();
    { char* xs=sm+w*8192+voff;
      #define KVM(idx,kt,vt) *(f32x4*)(xs+(idx)*1024)=__builtin_amdgcn_mfma_f32_16x16x16bf16_1k(__builtin_bit_cast(s16x4,kvb[kt]),__builtin_bit_cast(s16x4,kvb[vt]),(f32x4){},0,0,0)
      KVM(0,0,2); KVM(1,1,2); KVM(2,0,3); KVM(3,1,3); KVM(4,2,3); KVM(5,0,4); KVM(6,1,4); KVM(7,2,4); }
    LBAR();
    { u32x4 v; v[0]=pk2(phi(acc[0][0]),phi(acc[0][1])); v[1]=pk2(phi(acc[0][2]),phi(acc[0][3])); v[2]=pk2(phi(acc[1][0]),phi(acc[1][1])); v[3]=pk2(phi(acc[1][2]),phi(acc[1][3]));
      q01=__builtin_bit_cast(bf16x8,v);
      q2[0]=pk2(phi(acc[2][0]),phi(acc[2][1])); q2[1]=pk2(phi(acc[2][2]),phi(acc[2][3])); }
  }
  bool okv[2][4]; bool anyok[2]={false,false};
  #pragma unroll
  for(int x=0;x<2;x++){ const int tl=(w&3)+4*x;
    const int kt=(tl==0||tl==2||tl==5)?0:((tl==1||tl==3||tl==6)?1:2), vt= tl<2?2:(tl<5?3:4);
    #pragma unroll
    for(int r4=0;r4<4;r4++){ int d=16*kt+4*g+r4, v=16*vt+c16;
      bool ok; if(v<72) ok= v>=36 && d<36 && ((d<18)==(v-36<18)); else ok=(c16==8||c16==12)? d<18 : ((c16==9||c16==13)? (d>=18&&d<36):false);
      okv[x][r4]=ok; anyok[x]|=ok; } }
  if(w<4){
    const rsrc_t rkv=mkrs(ws+O_KVP+(size_t)hp*16*8192,16*8192);
    #pragma unroll
    for(int x=0;x<2;x++){ const int tl=w+4*x;
      f32x4 sacc=*(const f32x4*)(sm+tl*1024+voff);
      #pragma unroll
      for(int ww=1;ww<8;ww++) sacc+=*(const f32x4*)(sm+ww*8192+tl*1024+voff);
      if(anyok[x]) ST16(sacc,rkv,voff,rt*8192+tl*1024); }
    asm volatile("s_waitcnt vmcnt(0) lgkmcnt(0)":::"memory");
  }
  BAR();
  if(tid==0) __hip_atomic_store((GAS u64*)(ws+O_FKV+hp*256+rt*8),tag,__ATOMIC_RELAXED,__HIP_MEMORY_SCOPE_AGENT);
  STAMP(6);
  const int ct3=idx&7, r3=xcd*4+(idx>>3);
  constexpr int WOL=36*1024;
  if(w==0) pollp((const GAS u64*)(ws+(lane<16? O_FKV+hp*256+lane*8 : O_FWO+(ct3>>1)*256+(lane<40?lane-16:0)*8)),tag);
  BAR();
  if(w<4){
    u32x4 pv[2][16]; u32x4 wb4;
    { const char* pb=ws+O_KVP+(size_t)hp*16*8192+w*1024;
      #pragma unroll
      for(int x=0;x<2;x++){ const unsigned vo=(anyok[x]?voff:0u)+x*4096;
        #pragma unroll
        for(int i=0;i<16;i++) gload16c<0>(pv[x][i],pb+i*8192,vo); }
      gload16c<0>(wb4,ws+O_WB+hp*144,(lane<9?lane:0)*16); }
    #pragma unroll
    for(int x=0;x<2;x++){
      if(x==0) waitv<17>(); else waitv<0>();
      asm volatile("" : "+v"(pv[x][0]),"+v"(pv[x][1]),"+v"(pv[x][2]),"+v"(pv[x][3]),"+v"(pv[x][4]),"+v"(pv[x][5]),"+v"(pv[x][6]),"+v"(pv[x][7]),
         "+v"(pv[x][8]),"+v"(pv[x][9]),"+v"(pv[x][10]),"+v"(pv[x][11]),"+v"(pv[x][12]),"+v"(pv[x][13]),"+v"(pv[x][14]),"+v"(pv[x][15]) :: "memory");
      f32x4 sacc=__builtin_bit_cast(f32x4,pv[x][0]);
      #pragma unroll
      for(int i=1;i<16;i++) sacc+=__builtin_bit_cast(f32x4,pv[x][i]);
      u32x2 pk; pk[0]=pk2(okv[x][0]?sacc[0]:0.f,okv[x][1]?sacc[1]:0.f); pk[1]=pk2(okv[x][2]?sacc[2]:0.f,okv[x][3]?sacc[3]:0.f);
      *(u32x2*)(sm+(w+4*x)*512+lane*8)=pk; }
    asm volatile("" : "+v"(wb4));
    if(w==0 && lane<9) *(u32x4*)(sm+4096+lane*16)=wb4;
  }
  LBAR();
  STAMP(7);
  { const char* kk=sm+lane*8; const u32x2 z2={0,0};
    #define KF(i) (*(const u32x2*)(kk+(i)*512))
    f32x4 A=MF16(cat2(KF(0),KF(1)),q01,(f32x4){});
    f32x4 B=MF16(cat2(KF(2),KF(3)),q01,(f32x4){}); B=MF16(cat2(KF(4),z2),cat2(q2,z2),B);
    f32x4 C=MF16(cat2(KF(5),KF(6)),q01,(f32x4){}); C=MF16(cat2(KF(7),z2),cat2(q2,z2),C);
    const float ra=__builtin_amdgcn_rcpf(bcast_hi(C[0])+1e-6f), rb=__builtin_amdgcn_rcpf(bcast_hi(C[1])+1e-6f);
    const float b01= g<2? ra:rb, b23= g<1? ra:rb, cc= g<2? rb:0.f;
    A*=ra; B[0]*=b01; B[1]*=b01; B[2]*=b23; B[3]*=b23; C*=cc;
    const char* wbl=sm+4096;
    f32x4 t4=A*(*(const f32x4*)(wbl+(g>0?g-1:0)*16))+B*(*(const f32x4*)(wbl+48+g*16))+C*(*(const f32x4*)(wbl+112+(g<2?g:0)*16)); const float mu=sum_g((t4[0]+t4[1])+(t4[2]+t4[3]));
    const rsrc_t ro=mkrs(ws+O_O+((size_t)tt*16+hp)*1536,1536);
    ST16(((u32x4){pk2(A[0],A[1]),pk2(A[2],A[3]),pk2(B[0],B[1]),pk2(B[2],B[3])}),ro,voff,0);
    ST8(((u32x2){pk2(C[0],C[1]),pk2(C[2],C[3])}),ro,lane*8,1024);
    const rsrc_t rm=mkrs(ws+O_MU+(size_t)tt*16*64,1024);
    if(lane<16) ST4(mu,rm,lane*64+hp*4,0);
  }
  publish(ws,O_FO+rt*256+hp*8,tag,w);
  asm volatile("" :: "v"(wm[0]),"v"(wm[1]));
  STAMP(8);
  const int r1=(xcd&1)*4+(idx&3), fs=(xcd>>1)*8+(idx>>2);
  const int odd=fs&1;
  const char* wsrc=(const char*)((w>>2)? p.wu:p.wg)+(size_t)(fs*9+odd)*32;
  const unsigned wdst=smb+(w>>2)*73728+(w&3)*1024;
  unsigned vA,vC; u64 mB,mC;
  { const int r8=lane>>3, pos=lane&7, sl=(pos>>1)^((r8>>1)&3);
    vA=r8*(F*2)+sl*32+(pos&1)*16;
    vC=r8*(F*2)+(sl==3? 256:(odd?0:288))+(pos&1)*16;
    mB=__ballot(sl<3); mC=__ballot(sl==3||sl==0); }
  const int w3=w&3;
  auto wrow=[&](int kc0,int kst){ return wsrc+(size_t)(kc0+w3*kst)*(F*2); };
  {
    const int tq=w&3, hf=w>>2;
    const int t3=r3*4+tq;
    const int tok=t3*16+c16;
    if(w>0){
      const char* wob=ws+O_WO+(size_t)ct3*120*1024;
      #pragma unroll
      for(int i=0;i<18;i++){ int pc=(w-1)+7*i; if(pc>119) pc=119; glds16c(wob+pc*1024,voff,smb+WOL+pc*1024); }
    } else poll<16>(ws,O_FO+(r3>>1)*256,tag,lane);
    BAR();
    STAMP(9);
    u32x4 oab[8]; u32x2 oc[8]; u32x4 mu4; u32x2 xv[3]; unsigned mxu;
    { const char* ob=ws+O_O+((size_t)t3*16+hf*8)*1536;
      sfor<0,8>([&](auto I){ gload16c<0>(oab[I.v],ob+I.v*1536,voff); });
      #pragma unroll
      for(int i=0;i<8;i++) asm volatile("global_load_dwordx2 %0, %1, %2 offset:1024 sc1" : "=v"(oc[i]) : "v"(lane*8), "s"(ob+i*1536) : "memory");
      gload16c<0>(mu4,ws+O_MU+(size_t)t3*1024,c16*64+g*16);
      asm volatile("global_load_dword %0, %1, %2 sc1" : "=v"(mxu) : "v"(c16*4), "s"(ws+O_MX+(size_t)t3*64) : "memory");
      const char* xb=(const char*)p.x+(size_t)t3*16*1152+ct3*144;
      #pragma unroll
      for(int i=0;i<3;i++){ int nt= hf? 2+i : (i<2? i:1); int n=nt*16+4*g; if(n>=72) n=68;
        asm volatile("global_load_dwordx2 %0, %1, %2" : "=v"(xv[i]) : "v"(c16*1152+n*2), "s"(xb) : "memory"); } }
    waitv<21>();
    BAR();
    const char* rb=sm+WOL+hf*61440+voff;
    bf16x8 wpre[30];
    #pragma unroll
    for(int i=0;i<30;i++) wpre[i]=*(const bf16x8*)(rb+i*1024);
    SB();
    f32x4 acc[5]={};
    bf16x8 wl[30];
    sfor<0,6>([&](auto KL){ constexpr int kl=KL.v;
      asm volatile("s_waitcnt vmcnt(%1)" : "+v"(oab[kl]) : "i"(20-kl) : "memory");
      #pragma unroll
      for(int n=0;n<5;n++){ acc[n]=MF16(wpre[kl*5+n],__builtin_bit_cast(bf16x8,oab[kl]),acc[n]); wl[kl*5+n]=*(const bf16x8*)(rb+(30+kl*5+n)*1024); SB(); } });
    asm volatile("s_waitcnt vmcnt(0)" : "+v"(oab[6]),"+v"(oab[7]),"+v"(oc[0]),"+v"(oc[1]),"+v"(oc[2]),"+v"(oc[3]),"+v"(oc[4]),"+v"(oc[5]),"+v"(oc[6]),"+v"(oc[7]),"+v"(mu4),"+v"(mxu),"+v"(xv[0]),"+v"(xv[1]),"+v"(xv[2]) :: "memory");
    LBAR();
    sfor<0,18>([&](auto R){ constexpr int r=R.v;
      if constexpr(r>=2&&r<8){ if(w>=4) glds16(wrow(g1::krow(r),g1::kstep8(r)),vA,wdst+r*4096); } else glds16(wrow(g1::krow(r),g1::kstep8(r)),vA,wdst+r*4096); });
    #pragma unroll
    for(int kl=6;kl<12;kl++){ const bf16x8 ob= kl<8? __builtin_bit_cast(bf16x8,oab[kl<8?kl:0]) : cat2(oc[kl<8?0:2*(kl-8)],oc[kl<8?0:2*(kl-8)+1]);
      #pragma unroll
      for(int n=0;n<5;n++) acc[n]=MF16(wl[(kl-6)*5+n],ob,acc[n]); }
    STAMP(10);
    f32x4 keep[3];
    { char* xs=sm+8192+w*3072+voff;
      f32x4 a0=acc[0],a1=acc[1],a2=acc[2],a3=acc[3]; asm volatile("" : "+v"(a0),"+v"(a1),"+v"(a2),"+v"(a3));
      keep[0]=hf?a2:a0; keep[1]=hf?a3:a1; keep[2]=acc[4];
      *(f32x4*)(xs)=hf?a0:a2; *(f32x4*)(xs+1024)=hf?a1:a3; *(f32x4*)(xs+2048)=acc[4]; }
    const f32x4 m4=__builtin_bit_cast(f32x4,mu4);
    const float m=__uint_as_float(mxu)+sum_g((m4[0]+m4[1])+(m4[2]+m4[3]));
    LBAR();
    { const char* xr=sm+8192+(w^4)*3072+voff;
      const rsrc_t rc=mkrs(ws+O_C+(size_t)t3*18*1024,18*1024), rq=mkrs(ws+O_SSQ+(size_t)t3*16*64,1024);
      float qs=0; unsigned cp[6];
      #pragma unroll
      for(int i=0;i<3;i++){
        const f32x4 v=keep[i]+*(const f32x4*)(xr+i*1024);
        const float c0=(lo16(xv[i][0])+v[0])-m,c1=(hi16(xv[i][0])+v[1])-m,c2=(lo16(xv[i][1])+v[2])-m,c3=(hi16(xv[i][1])+v[3])-m;
        const float qq=(c0*c0+c1*c1)+(c2*c2+c3*c3);
        qs+= (i<2 || (hf && g<2))? qq:0.f;
        cp[2*i]=pk2(c0,c1); cp[2*i+1]=pk2(c2,c3); }
      ST16(((u32x4){cp[0],cp[1],cp[2],cp[3]}),rc,voff,(2*ct3+hf)*1024);
      if(hf && g<2) ST8(((u32x2){cp[4],cp[5]}),rc,((2*(ct3&1)+g)*16+c16)*16+((ct3>>1)&1)*8,(16+(ct3>>2))*1024);
      qs=sum_g(qs);
      if(lane<16){ ST4(qs,rq,lane*64+(ct3*2+hf)*4,0); if(ct3==0 && hf==0) ST4(m,mkrs(ws+O_MH+(size_t)t3*64,64),lane*4,0); }
    }
    publish(ws,O_FH+(r3>>2)*256+((r3&3)*8+ct3)*8,tag,w);
    if(b==0 && tid==64) __hip_atomic_store((GAS u64*)(ws+O_EP),tag,__ATOMIC_RELAXED,__HIP_MEMORY_SCOPE_AGENT);
  }
  STAMP(11);
  STAMP(12);
  {
    using namespace g1;
    const int t1=r1*16+w*2; const int whi=w>>2;
    const char* abase=ws+O_C+(size_t)t1*18*1024;
    if(w<4) sfor<2,8>([&](auto R){ constexpr int r=R.v; glds16(wrow(krow(r),kstep8(r)),vA,wdst+r*4096); });
    if(w==0) poll<32>(ws,O_FH+r1*256,tag,lane);
    waitv<0>();
    BAR();
    STAMP(13);
    bf16x8 af[18][2];
    f32x4 sq[2];
    { const char* sp=ws+O_SSQ+(size_t)t1*16*64; const unsigned vo=c16*64+g*16; gload16c<0>(sq[0],sp,vo); gload16c<1024>(sq[1],sp,vo); }
    auto loadA=[&](auto Sx,auto T){ constexpr int s=Sx.v,t=T.v; gload16c<(s&3)*1024>(af[s][t], abase+t*18*1024+(s>>2)*4096, voff); };
    sfor<0,LAK>([&](auto Sx){ loadA(Sx,IC<0>{}); loadA(Sx,IC<1>{}); });
    f32x4 acc[4][2][2];
    const unsigned lb0=smb+(4*g+q)*128+((((g&1)<<1)|(q>>1))*32)+pp*8;
    unsigned ab[4][2];
    bf16x8 wf[PF];
    float c1[2], oo[2], ea, pa; unsigned wmd;
    u32x4 hold[2];
    const rsrc_t ors=mkrs(ws+O_ACT+(size_t)t1*144*1024,2*144*1024);
    const int obase=((fs*9+odd)>>1)*1024;
    const int olast=((fs*9+(odd?0:8))>>1)*1024+(odd?8:0);
    auto rdW=[&](auto I){ constexpr int i=I.v, sl=slotOf(pT(i)), s=pS(i), mat=i&1;
      if constexpr(s==0){ ab[sl][mat]=(lb0^(sl*32))+mat*73728; asm volatile("" : "+v"(ab[sl][mat])); }
      if constexpr(s==9){ ab[sl][mat]+=36864; asm volatile("" : "+v"(ab[sl][mat])); }
      wf[i%PF]=cat(trread(ab[sl][mat]+(s%9)*4096),trread(ab[sl][mat]+(s%9)*4096+2048)); };
    sfor<0,PF-1>(rdW);
    auto epi=[&](auto E,auto K){ constexpr int e=E.v,kk=K.v; constexpr int ET=e>>3, t=(e>>2)&1, r=e&3;
      if constexpr(kk==0){ float gg=acc[ET&3][0][t][r], u=acc[ET&3][1][t][r]; ea=gg*c1[t]; pa=gg*u; asm volatile("" : "+v"(ea),"+v"(pa)); }
      if constexpr(kk==1){ ea=__builtin_amdgcn_exp2f(ea); asm volatile("" : "+v"(ea)); }
      if constexpr(kk==2){ ea=1.f+ea; asm volatile("" : "+v"(ea)); }
      if constexpr(kk==3){ ea=__builtin_amdgcn_rcpf(ea); asm volatile("" : "+v"(ea)); }
      if constexpr(kk==4){
        oo[r&1]=pa*ea;
        if constexpr((r&1)==0) asm volatile("" : "+v"(oo[0]));
        else {
          constexpr int hi=(ET==8||(ET&1)==0)?0:2;
          hold[t][hi+(r>>1)]=pk2(oo[0],oo[1]);
          asm volatile("" : "+v"(hold[t][hi+(r>>1)]));
          if constexpr(r==3){
            if constexpr(ET==8){ ST8(((u32x2){hold[t][0],hold[t][1]}),ors,voff,olast+t*144*1024); }
            else if constexpr((ET&1)==1){ ST16(hold[t],ors,voff,obase+t*144*1024+(ET>>1)*1024); }
          }
        }
      } };
    auto slot=[&](auto M){ constexpr int m=M.v-216-2;
      if constexpr(m>=0){ constexpr int e=(m*4+3)/27; constexpr int kk=m-(e*27)/4;
        if constexpr(e<64 && kk>=0 && kk<5) epi(IC<e>{},IC<kk>{}); } };
    sfor<0,324>([&](auto I){ constexpr int i=I.v; constexpr int T=pT(i), s=pS(i), mat=i&1, j=i-108;
      if constexpr(i==108) STAMP(14);
      if constexpr(i<108 && i%6==0){
        constexpr int iss=(s+LAK<18)?s+LAK:18;
        asm volatile("s_waitcnt vmcnt(%2)" : "+v"(af[s][0]),"+v"(af[s][1]) : "i"(2*(iss-s-1)) : "memory");
        if constexpr(i==0){ asm volatile("" : "+v"(sq[0]),"+v"(sq[1]));
          #pragma unroll
          for(int t=0;t<2;t++){ f32x4 s4=sq[t]; float ss=sum_g((s4[0]+s4[1])+(s4[2]+s4[3])); c1[t]=-1.44269504089f*__builtin_amdgcn_rsqf(ss*(1.f/576.f)+1e-5f); }
          asm volatile("" : "+v"(c1[0]),"+v"(c1[1])); } }
      if constexpr(j==2||j==74) BAR();
      if constexpr(j==31){ waitv<5>(); BAR(); }
      if constexpr(j==49||j==139){ waitv<0>(); BAR(); }
      if constexpr(i+PF-1<324) rdW(IC<i+PF-1>{});
      SB();
      if constexpr(s==0) acc[T&3][mat][0]=MF16(wf[i%PF],af[s][0],(f32x4){}); else acc[T&3][mat][0]=MF16(wf[i%PF],af[s][0],acc[T&3][mat][0]);
      SB();
      slot(IC<2*i>{});
      SB();
      if constexpr(s==0) acc[T&3][mat][1]=MF16(wf[i%PF],af[s][1],(f32x4){}); else acc[T&3][mat][1]=MF16(wf[i%PF],af[s][1],acc[T&3][mat][1]);
      SB();
      slot(IC<2*i+1>{});
      if constexpr(j==TOUCH_D){ unsigned o=(w*21+(lane<21?lane:20))*128; if(o>20608) o=20608; gtouch(wmd,(const char*)p.wd+(size_t)b*20736,o); }
      if constexpr(j==140) asm_use(wmd);
      if constexpr(i<108 && (i%6==1||i%6==4) && s+LAK<18) loadA(IC<s+LAK>{},IC<(i%6)/3>{});
      if constexpr(j>=2 && j<=37){ constexpr int r=(j-2)/2; glds16ms(wrow(krow(r),kstep8(r))+128,vA,wdst+r*4096,mB,(j&1)? whi^1:whi); }
      if constexpr(j>=75 && j<=127 && (j-75)%3<2){ constexpr int r=(j-75)/3; glds16ms(wrow(krow(r),kstep8(r))-32,vC,wdst+r*4096,mC,((j-75)%3)? whi^1:whi); }
      SB();
    });
    STAMP(15);
    sfor<64,72>([&](auto E){ sfor<0,5>([&](auto K){ epi(E,K); }); });
    publish(ws,O_FA+r1*256+fs*8,tag,w);
  }
  STAMP(16);
  {
    using namespace g2;
    const int lane=fresh_lane(); const unsigned voff=lane*16; const int g=lane>>4, q=(lane>>2)&3, pp=lane&3, c16=lane&15;
    const int sp=xcd>>1, r2t=(xcd&1)*8+(idx>>2), ct=idx&3;
    const int tq=w&3, kh=w>>2;
    const int P0=sp*36+kh*18;
    const unsigned rb=smb+kh*RING*1024;
    const char* wbase=(const char*)p.wd+(size_t)P0*32*1152+ct*288;
    unsigned wv[9];
    #pragma unroll
    for(int i=0;i<9;i++){ const int pc=(4*i+tq); const int id=pc*64+lane; const int row=id/18; wv[i]=row*1152+(id-row*18)*16; }
    const char* abase=ws+O_ACT+((size_t)(r2t*8+tq*2)*144+P0)*1024;
    auto loadW=[&](auto R){ constexpr int r=R.v;
      if constexpr(r==NR-1){ if(tq<2) glds16(wbase+(size_t)(r/9)*128*1152, wv[r%9], rb+((4*r)%RING+tq)*1024); else glds16(wbase+(size_t)(r/9)*128*1152, wv[(r-1)%9], rb+((4*(r-1))%RING+tq)*1024); }
      else glds16(wbase+(size_t)(r/9)*128*1152, wv[r%9], rb+((4*r)%RING+tq)*1024); };
    sfor<0,PRE/4>(loadW);
    if(w==0) poll<8>(ws,O_FA+(r2t>>1)*256+sp*64,tag,lane);
    waitv<0>();
    BAR();
    STAMP(17);
    constexpr int AR=LAK+1;
    bf16x8 af[AR][2];
    auto loadA=[&](auto J,auto T){ constexpr int j=J.v,t=T.v; gload16c<0>(af[j%AR][t], abase+t*144*1024+j*1024, voff); };
    sfor<0,LAK>([&](auto J){ loadA(J,IC<0>{}); loadA(J,IC<1>{}); });
    f32x4 acc[9][2];
    unsigned lwb[2];
    #pragma unroll
    for(int i=0;i<2;i++){ lwb[i]=rb+(4*g+q)*288+pp*8+i*36864; asm volatile("" : "+v"(lwb[i])); }
    bf16x8 wf[PF];
    #define LDW2(i) cat(trread(lwb[(((i)/9)%8)/4]+((((i)/9)%8)%4)*9216+((i)%9)*32),trread(lwb[(((i)/9)%8)/4]+((((i)/9)%8)%4)*9216+((i)%9)*32+16*288))
    sfor<0,PF-1>([&](auto I){ wf[I.v]=LDW2(I.v); });
    sfor<0,NP>([&](auto I){ constexpr int i=I.v; constexpr int j=i/9, nt=i%9;
      if constexpr((i+PF)%18==0 && i+PF<NP){ if constexpr(i+PF+18>PRE) waitv<vmc(i,-1,i+PF+18)>(); BAR(); }
      if constexpr(nt==0) asm volatile("s_waitcnt vmcnt(%2)" : "+v"(af[j%AR][0]),"+v"(af[j%AR][1]) : "i"(vmc(i,j,0)) : "memory");
      if constexpr(i+PF-1<NP) wf[(i+PF-1)%PF]=LDW2(i+PF-1);
      SB();
      if constexpr(j==0) acc[nt][0]=MF16(wf[i%PF],af[j%AR][0],(f32x4){}); else acc[nt][0]=MF16(wf[i%PF],af[j%AR][0],acc[nt][0]);
      if constexpr(j==0) acc[nt][1]=MF16(wf[i%PF],af[j%AR][1],(f32x4){}); else acc[nt][1]=MF16(wf[i%PF],af[j%AR][1],acc[nt][1]);
      SB();
      if constexpr(nt==0 && j+LAK<18) loadA(IC<j+LAK>{},IC<0>{});
      if constexpr(nt==4 && j+LAK<18) loadA(IC<j+LAK>{},IC<1>{});
      if constexpr((i+LWP)%4==0 && (i+LWP)/4<NR && (i+LWP)/4>=PRE/4) loadW(IC<(i+LWP)/4>{});
      SB();
    });
    STAMP(18);
    BAR();
    { char* xs=sm+(w*9)*1024+voff;
      #pragma unroll
      for(int n=0;n<9;n++){ f32x4 v= kh? acc[n][0]:acc[n][1]; *(f32x4*)(xs+n*1024)=v; } }
    LBAR();
    f32x4 part[10];
    { const char* xr=sm+((w^4)*9)*1024+voff;
      #pragma unroll
      for(int n=0;n<9;n++){ f32x4 mine= kh? acc[n][1]:acc[n][0]; part[n]=mine+*(const f32x4*)(xr+n*1024); }
      part[9]=part[8]; }
    const int grp=r2t*4+ct;
    const rsrc_t xr=mkrs(ws+O_X+(size_t)grp*160*1024,160*1024);
    if(tq!=sp){
      #pragma unroll
      for(int n=0;n<5;n++) ST16(((u32x4){pk2(part[2*n][0],part[2*n][1]),pk2(part[2*n][2],part[2*n][3]),pk2(part[2*n+1][0],part[2*n+1][1]),pk2(part[2*n+1][2],part[2*n+1][3])}),xr,voff,((tq*4+sp)*10+kh*5+n)*1024);
    } else {
      char* xs=sm+(72+kh*10)*1024+voff;
      #pragma unroll
      for(int n=0;n<10;n++) *(f32x4*)(xs+n*1024)=part[n];
    }
    publish(ws,O_FX+grp*128+sp*8,tag,w);
    STAMP(19);
    const int tk0=r2t*128+sp*32;
    const rsrc_t rh=mkrs(ws+O_C+(size_t)(tk0>>4)*18432,2*18432), rq=mkrs(ws+O_SSQ+(size_t)tk0*64,2048), rmh=mkrs(ws+O_MH+(size_t)tk0*4,128);
    u32x2 hv[2][2]; float r2[2], mh[2]; int go[2][2];
    #pragma unroll
    for(int x=0;x<2;x++){ int u=w+8*x; if(u>9) u=9; const int tl=u>=5, pr=u-5*tl;
      #pragma unroll
      for(int y=0;y<2;y++){ int nt=2*pr+y; if(nt>8) nt=8; const int nl=nt*16+g*4;
        go[x][y]=(tl*16+c16)*1152+(ct*144+nl)*2;
        const int c3=ct*2+(nl>=72), o72=nl>=72? nl-72:nl;
        int kc,gp,hfh; if(o72<64){ kc=2*c3+(o72>>5); hfh=(o72>>4)&1; gp=(o72>>2)&3; } else { kc=16+(c3>>2); gp=2*(c3&1)+((o72-64)>>2); hfh=(c3>>1)&1; }
        hv[x][y]=LD8(rh,(tl*18+kc)*1024+(gp*16+c16)*16+hfh*8,0); }
      const f32x4 s4=__builtin_bit_cast(f32x4,LD16(rq,(tl*16+c16)*64+g*16,0));
      mh[x]=__uint_as_float(__builtin_amdgcn_raw_buffer_load_b32(rmh,(tl*16+c16)*4,0,16));
      r2[x]=__builtin_amdgcn_rcpf(sum_g((s4[0]+s4[1])+(s4[2]+s4[3]))*(1.f/576.f)+1e-5f); }
    if(w==0) poll<4>(ws,O_FX+grp*128,tag,lane);
    BAR();
    STAMP(20);
    u32x4 rv[2][3];
    #pragma unroll
    for(int x=0;x<2;x++){ int u=w+8*x; if(u>9) u=9;
      #pragma unroll
      for(int y=0;y<3;y++){ int src=(sp+1+y)&3; rv[x][y]=LD16(xr,voff,((sp*4+src)*10+u)*1024); } }
    char* const ob=(char*)p.out+(size_t)(r2t*128+sp*32)*1152;
    #pragma unroll
    for(int x=0;x<2;x++){ if(x==0 || w<2){ const int u=w+8*x; const int tl=u>=5, pr=u-5*tl;
      #pragma unroll
      for(int y=0;y<2;y++){ if(y==0 || pr<4){
        f32x4 v=*(const f32x4*)(sm+(72+tl*10+2*pr+y)*1024+voff);
        #pragma unroll
        for(int z=0;z<3;z++){ v[0]+=lo16(rv[x][z][2*y]); v[1]+=hi16(rv[x][z][2*y]); v[2]+=lo16(rv[x][z][2*y+1]); v[3]+=hi16(rv[x][z][2*y+1]); }
        u32x2 ov; ov[0]=pk2(v[0]*r2[x]+(lo16(hv[x][y][0])+mh[x]),v[1]*r2[x]+(hi16(hv[x][y][0])+mh[x])); ov[1]=pk2(v[2]*r2[x]+(lo16(hv[x][y][1])+mh[x]),v[3]*r2[x]+(hi16(hv[x][y][1])+mh[x]));
        *(u32x2*)(ob+go[x][y])=ov; } } } }
  }
  STAMP(21);
  STAMP_OUT();
}
}

extern "C" void kernel_launch(void* const* d_in, const int* in_sizes, int n_in,
                              void* d_out, int out_size, void* d_ws, size_t ws_size,
                              hipStream_t stream) {
  using namespace k;
  Params p;
  p.x=(const u16*)d_in[0]; p.wqkv=(const u16*)d_in[1]; p.wo=(const u16*)d_in[2];
  p.wg=(const u16*)d_in[4]; p.wu=(const u16*)d_in[5]; p.wd=(const u16*)d_in[6];
  p.out=(u16*)d_out; p.ws=(char*)d_ws;
  fused_k<<<256,512,0,stream>>>(p);
}
```

```cpp
#include <hip/hip_runtime.h>
#include <hip/hip_bf16.h>
typedef unsigned short u16;
typedef unsigned long long u64;
typedef short s16x4 __attribute__((ext_vector_type(4)));
typedef short s16x8 __attribute__((ext_vector_type(8)));
typedef __bf16 bf16x8 __attribute__((ext_vector_type(8)));
typedef __bf16 bf16x2 __attribute__((ext_vector_type(2)));
typedef float f32x4 __attribute__((ext_vector_type(4)));
typedef unsigned u32x4 __attribute__((ext_vector_type(4)));
typedef unsigned u32x2 __attribute__((ext_vector_type(2)));
#define LAS __attribute__((address_space(3)))
#define GAS __attribute__((address_space(1)))
#define DI __device__ __forceinline__

#define STAMP(i)
#define STAMP_OUT()

#define WARM 21
#define WARM_SLEEP 20
#define STAG 1
#define PRETOUCH 1
#define CODE_KB 48
#define WARM_MODE 1
#define TOUCH_AT 121
#define F0_UNITS 0
#define TOUCH_D 50
#define QSPLIT 24
#define F1_XA 8
#define F1_WA 6
#define P1BAR 36
#define F1_LA 6
namespace k {
constexpr int S=2048, D=576, F=4608;
constexpr int STAMP_LDS=156*1024, TAG_LDS=155*1024;
DI int fresh_lane(){ int l; asm volatile("v_mbcnt_lo_u32_b32 %0, -1, 0\n\tv_mbcnt_hi_u32_b32 %0, -1, %0" : "=v"(l)); return l; }
DI u16 f2bf(float f){ __bf16 b=(__bf16)f; return __builtin_bit_cast(u16,b); }
DI unsigned pk2(float lo,float hi){ bf16x2 v; v[0]=(__bf16)lo; v[1]=(__bf16)hi; return __builtin_bit_cast(unsigned,v); }
DI float lo16(unsigned v){ return __uint_as_float(v<<16); }
DI float hi16(unsigned v){ return __uint_as_float(v&0xffff0000u); }
DI float phi(float t){ return (t>0.f? t+1.f: __builtin_amdgcn_exp2f(t*1.44269504089f))+1e-6f; }
DI float bcast_hi(float x){ unsigned a=__float_as_uint(x); auto r=__builtin_amdgcn_permlane32_swap(a,a,false,false); return __uint_as_float(r[1]); }
DI float sum_g(float x){
  unsigned a=__float_as_uint(x); auto r=__builtin_amdgcn_permlane32_swap(a,a,false,false); x=__uint_as_float(r[0])+__uint_as_float(r[1]);
  a=__float_as_uint(x); auto r2=__builtin_amdgcn_permlane16_swap(a,a,false,false); return __uint_as_float(r2[0])+__uint_as_float(r2[1]); }
DI float ssq_sum(f32x4 v,int lane){
  #pragma unroll
  for(int r=0;r<4;r++){ float x=sum_g(v[r]); x+=__shfl_xor(x,4); x+=__shfl_xor(x,8); v[r]=x; }
  float o[4];
  #pragma unroll
  for(int r=0;r<4;r++) o[r]=__shfl(v[r],(lane>>2)&3);
  const int k=lane&3; return k==0? o[0] : (k==1? o[1] : (k==2? o[2]:o[3])); }
DI float dot2(unsigned a,unsigned b,float c){ return __builtin_amdgcn_fdot2_f32_bf16(__builtin_bit_cast(bf16x2,a),__builtin_bit_cast(bf16x2,b),c,false); }
DI s16x4 trread(unsigned a){ return __builtin_amdgcn_ds_read_tr16_b64_v4i16((LAS s16x4*)(size_t)a); }
DI bf16x8 cat(s16x4 a,s16x4 b){ return __builtin_bit_cast(bf16x8,__builtin_shufflevector(a,b,0,1,2,3,4,5,6,7)); }
DI bf16x8 cat2(u32x2 a,u32x2 b){ return __builtin_bit_cast(bf16x8,(u32x4){a[0],a[1],b[0],b[1]}); }
#define BAR() do{ asm volatile("":::"memory"); __builtin_amdgcn_s_barrier(); asm volatile("":::"memory"); }while(0)
#define LBAR() do{ asm volatile("s_waitcnt lgkmcnt(0)":::"memory"); __builtin_amdgcn_s_barrier(); asm volatile("":::"memory"); }while(0)
#define MF16(a,bb,c) __builtin_amdgcn_mfma_f32_16x16x32_bf16(a,bb,c,0,0,0)
#define SB() __builtin_amdgcn_sched_barrier(0)
#define WAITV0() __builtin_amdgcn_s_waitcnt(0x0F70)
template<int I> struct IC{ static constexpr int v=I; };
template<int B,int E,class Fn> DI void sfor(Fn&& f){ if constexpr(B<E){ f(IC<B>{}); sfor<B+1,E>(f); } }
DI void glds16(const void* sbase, unsigned voff, unsigned lds){
  unsigned keep;
  asm volatile("s_mov_b32 %0, m0\n\ts_mov_b32 m0, %3\n\ts_nop 0\n\tglobal_load_lds_dwordx4 %1, %2\n\ts_mov_b32 m0, %0" : "=&s"(keep) : "v"(voff), "s"(sbase), "s"(lds) : "memory");
}
DI void glds16c(const void* sbase, unsigned voff, unsigned lds){
  unsigned keep;
  asm volatile("s_mov_b32 %0, m0\n\ts_mov_b32 m0, %3\n\ts_nop 0\n\tglobal_load_lds_dwordx4 %1, %2 sc1\n\ts_mov_b32 m0, %0" : "=&s"(keep) : "v"(voff), "s"(sbase), "s"(lds) : "memory");
}
DI void glds16m(const void* sbase, unsigned voff, unsigned lds, u64 mask){
  unsigned keep;
  asm volatile("s_mov_b32 %0, m0\n\ts_mov_b32 m0, %3\n\ts_mov_b64 exec, %4\n\tglobal_load_lds_dwordx4 %1, %2\n\ts_mov_b64 exec, -1\n\ts_mov_b32 m0, %0" : "=&s"(keep) : "v"(voff), "s"(sbase), "s"(lds), "s"(mask) : "memory");
}
DI void glds16ms(const void* sbase, unsigned voff, unsigned lds, u64 mask, int skip){
  unsigned keep;
  asm volatile("s_cmp_lg_u32 %5, 0\n\ts_cbranch_scc1 1f\n\ts_mov_b32 %0, m0\n\ts_mov_b32 m0, %3\n\ts_mov_b64 exec, %4\n\tglobal_load_lds_dwordx4 %1, %2\n\ts_mov_b64 exec, -1\n\ts_mov_b32 m0, %0\n1:" : "=&s"(keep) : "v"(voff), "s"(sbase), "s"(lds), "s"(mask), "s"(skip) : "memory","scc");
}
template<int OFF,class T> DI void gload16(T& r,const void* sbase,unsigned voff){ asm volatile("global_load_dwordx4 %0, %1, %2 offset:%3" : "=v"(r) : "v"(voff), "s"(sbase), "i"(OFF) : "memory"); }
template<int OFF,class T> DI void gload16c(T& r,const void* sbase,unsigned voff){ asm volatile("global_load_dwordx4 %0, %1, %2 offset:%3 sc1" : "=v"(r) : "v"(voff), "s"(sbase), "i"(OFF) : "memory"); }
DI void gtouch(unsigned& r,const void* sbase,unsigned voff){ asm volatile("global_load_dword %0, %1, %2" : "=v"(r) : "v"(voff), "s"(sbase) : "memory"); }
DI void asm_use(unsigned r){ asm volatile("" :: "v"(r)); }
template<int N,class T> DI void gwait(T& a,T& b){ asm volatile("s_waitcnt vmcnt(%2)" : "+v"(a),"+v"(b) : "i"(N) : "memory"); }
template<int N> DI void waitv(){ asm volatile("s_waitcnt vmcnt(%0)" :: "i"(N) : "memory"); }
typedef __amdgpu_buffer_rsrc_t rsrc_t;
DI rsrc_t mkrs(const void* p,unsigned bytes){ return __builtin_amdgcn_make_buffer_rsrc((void*)p,0,bytes,0x00020000); }
#define ST16(v,rs,vo,so) __builtin_amdgcn_raw_buffer_store_b128(__builtin_bit_cast(u32x4,v),rs,vo,so,16)
#define ST8(v,rs,vo,so)  __builtin_amdgcn_raw_buffer_store_b64(__builtin_bit_cast(u32x2,v),rs,vo,so,16)
#define ST4(v,rs,vo,so)  __builtin_amdgcn_raw_buffer_store_b32(__float_as_uint(v),rs,vo,so,16)
#define LD16(rs,vo,so) __builtin_amdgcn_raw_buffer_load_b128(rs,vo,so,16)
#define LD8(rs,vo,so)  __builtin_amdgcn_raw_buffer_load_b64(rs,vo,so,16)

constexpr size_t O_EP=0;
constexpr size_t O_F0 =256;
constexpr size_t O_FW =O_F0+2048;
constexpr size_t O_FWO=O_FW+2048;
constexpr size_t O_FKV=O_FWO+1024;
constexpr size_t O_FO =O_FKV+16*256;
constexpr size_t O_FH =O_FO+16*256;
constexpr size_t O_FA =O_FH+8*256;
constexpr size_t O_FA2=O_FA+8*256;
constexpr size_t O_FX =O_FA2+8*256;
constexpr size_t O_KVP=32768;
constexpr size_t O_XN =O_KVP+(size_t)256*8192;
constexpr size_t O_MX =O_XN+(size_t)S*D*2;
constexpr size_t O_WB =O_MX+(size_t)S*4;
constexpr size_t O_WQ =O_WB+4096;
constexpr size_t O_WO =O_WQ+(size_t)16*144*1024;
constexpr size_t O_O  =O_WO+(size_t)8*120*1024;
constexpr size_t O_MU =O_O+(size_t)128*16*1536;
constexpr size_t O_MH =O_MU+(size_t)S*64;
constexpr size_t O_C  =O_MH+(size_t)S*4;
constexpr size_t O_SSQ=O_C+(size_t)S*D*2;
constexpr size_t O_ACT=O_SSQ+(size_t)S*64;
constexpr size_t O_X  =O_ACT+(size_t)S*F*2;
constexpr size_t O_END=O_X+(size_t)64*160*1024;
struct Params { const u16 *x,*wqkv,*wo,*wg,*wu,*wd; u16* out; char* ws; };

DI void publish(char* ws,size_t off,u64 tag,int w){
  asm volatile("s_waitcnt vmcnt(0) lgkmcnt(0)":::"memory");
  BAR();
  if(w==0 && fresh_lane()==0) __hip_atomic_store((GAS u64*)(ws+off),tag,__ATOMIC_RELAXED,__HIP_MEMORY_SCOPE_AGENT);
}
DI void flag_store(char* ws,size_t off,u64 tag){ if(fresh_lane()==0) __hip_atomic_store((GAS u64*)(ws+off),tag,__ATOMIC_RELAXED,__HIP_MEMORY_SCOPE_AGENT); }
DI void pollp(const GAS u64* p,u64 tag){
  for(unsigned spins=0; spins<(1u<<20); ++spins){
    u64 v=__hip_atomic_load(p,__ATOMIC_RELAXED,__HIP_MEMORY_SCOPE_AGENT);
    if(__all(v==tag)) break;
    __builtin_amdgcn_s_sleep(1);
  }
  __builtin_amdgcn_fence(__ATOMIC_ACQUIRE,"wavefront");
}
template<int N> DI void poll(char* ws,size_t off,u64 tag,int lane){ pollp((const GAS u64*)(ws+off)+(lane<N?lane:0),tag); }

namespace f1 {
constexpr int PF=6, XA=F1_XA, WA=F1_WA;
constexpr int wr(int r){ return r<WA? -1 : (r<12? 5*(r-WA)+3 : 53+5*(r-12)); }
constexpr int roundAt(int i){ for(int r=WA;r<18;r++) if(wr(r)==i) return r; return -1; }
constexpr int vmc(int iw,int xs,int pw){
  int cnt=0;
  for(int k=0;k<XA;k++){ if(k<WA){ if(8*k<pw) cnt=0; else cnt++; } if(k<=xs) cnt=0; else cnt++; }
  for(int i=0;i<iw;i++){
    if(i%5==1 && i/5+XA<18){ if(i/5+XA<=xs) cnt=0; else cnt++; }
    int r=roundAt(i); if(r>=0){ if(8*r<pw) cnt=0; else cnt++; }
  }
  return cnt;
}
constexpr int lpos(int p){ return p<90? p : p+12; }
}
namespace g1 {
constexpr int PF=4, LAK=5;
constexpr int pT(int i){ return i<108? (i%6)/2 : 3+(i-108)/36; }
constexpr int pS(int i){ return i<108? i/6 : ((i-108)%36)/2; }
constexpr int slotOf(int T){ return T<4? T : (T<8? T-4 : 0); }
constexpr int krow(int kc){ return kc<16? 72*(kc>>1)+32*(kc&1) : 288*(kc-16)+64; }
constexpr int kstep8(int kc){ return kc<16? 8:72; }
}
namespace g2 {
constexpr int LW=5, PF=5, NP=162, AR=6;
constexpr int kord(int j){ constexpr int t[18]={0,1,5,6,9,10,14,15, 2,3,4,7,8,11,12,13,16,17}; return t[j]; }
constexpr int actStep(int j,int t){ return j<5? -1 : ((j>=8&&j<=10)? 50+2*(j-8)+t : 9*(j-5)+2+3*t); }
constexpr int vmc(int iw,int ja,int jw){
  int best=63;
  for(int tq=0;tq<4;tq++){
    int cnt=0;
    for(int j=0;j<8;j++){ int n=2+((j%4)==tq); if(j<=jw) cnt=0; else cnt+=n; }
    for(int j=0;j<5;j++){ if(j<=ja) cnt=0; else cnt+=2; }
    for(int i=0;i<iw;i++){
      for(int j=5;j<18;j++) for(int t=0;t<2;t++) if(actStep(j,t)==i){ if(j<=ja) cnt=0; else cnt++; }
      const int j=i/9+LW, nt=i%9;
      if(j>=8 && j<18 && (nt==1||nt==3||(nt==6&&(j%4)==tq))){ if(j<=jw) cnt=0; else cnt++; }
    }
    if(cnt<best) best=cnt;
  }
  return best;
}
constexpr int actAt(int i){ for(int j=5;j<18;j++) for(int t=0;t<2;t++) if(actStep(j,t)==i) return j*2+t; return -1; }
}

__global__ __launch_bounds__(512,2) void fused_k(Params p){
  __shared__ __attribute__((aligned(16))) char sm[160*1024];
  const int tid=threadIdx.x, lane=tid&63, w=__builtin_amdgcn_readfirstlane(tid>>6);
  const int b=blockIdx.x, xcd=b&7, idx=b>>3;
  const unsigned smb=(unsigned)(size_t)(LAS char*)sm;
  const unsigned voff=lane*16;
  const int g=lane>>4, q=(lane>>2)&3, pp=lane&3, c16=lane&15;
  char* const ws=p.ws;
  STAMP(0);
  u64 tag;
  const int hp=(xcd&1)*8+(idx>>2), rt=(xcd>>1)*4+(idx&3);
  unsigned wm[2]={0,0};
  auto touch_gu=[&](int slot,int unit,int lo){
    int ln=unit*64+lane; if(ln>323) ln=323; const int mt=ln>=162; ln-=mt*162;
    const char* src=(const char*)(mt? p.wu : p.wg)+(size_t)b*20736+ln*128;
    asm volatile("s_cmp_lt_u32 %2, %3\n\ts_cbranch_scc1 1f\n\tglobal_load_dword %0, %1, off\n1:" : "+v"(wm[slot]) : "v"(src), "s"(w), "s"(lo) : "memory","scc"); };
  {
    char* const smw=sm+w*16384;
    const unsigned smwb=smb+w*16384;
    if(w<2){
      u32x2 ep; { const unsigned z=0; asm volatile("global_load_dwordx2 %0, %1, %2 sc1" : "=v"(ep) : "v"(z), "s"(ws+(w?(size_t)O_XN:0)) : "memory"); }
      u32x4 v[5];
      { const char* xb=(const char*)p.x+(size_t)(b*8+w*4)*1152; const unsigned v0=g*1152+c16*16, v4=g*1152+(64+(c16&7))*16;
        gload16<0>(v[0],xb,v0); gload16<256>(v[1],xb,v0); gload16<512>(v[2],xb,v0); gload16<768>(v[3],xb,v0); gload16<0>(v[4],xb,v4); }
      asm volatile("s_waitcnt vmcnt(0)" : "+v"(ep),"+v"(v[0]),"+v"(v[1]),"+v"(v[2]),"+v"(v[3]),"+v"(v[4]) :: "memory");
      if(tid==0) *(u32x2*)(sm+TAG_LDS)=ep;
      float s1=0;
      #pragma unroll
      for(int i=0;i<5;i++){ const unsigned one=(i<4||c16<8)?0x3f803f80u:0u;
        #pragma unroll
        for(int j=0;j<4;j++) s1=dot2(v[i][j],one,s1); }
      s1+=__shfl_xor(s1,1); s1+=__shfl_xor(s1,2); s1+=__shfl_xor(s1,4); s1+=__shfl_xor(s1,8);
      const float m=s1*(1.f/576.f);
      float f[5][8]; float s2=0,s2t=0;
      #pragma unroll
      for(int i=0;i<5;i++)
        #pragma unroll
        for(int j=0;j<4;j++){ f[i][2*j]=lo16(v[i][j])-m; f[i][2*j+1]=hi16(v[i][j])-m;
          if(i<4){ s2=fmaf(f[i][2*j],f[i][2*j],s2); s2=fmaf(f[i][2*j+1],f[i][2*j+1],s2); } else { s2t=fmaf(f[i][2*j],f[i][2*j],s2t); s2t=fmaf(f[i][2*j+1],f[i][2*j+1],s2t); } }
      s2+= c16<8? s2t:0.f;
      s2+=__shfl_xor(s2,1); s2+=__shfl_xor(s2,2); s2+=__shfl_xor(s2,4); s2+=__shfl_xor(s2,8);
      const float r=__builtin_amdgcn_rsqf(s2*(1.f/576.f)+1e-5f);
      #pragma unroll
      for(int i=0;i<5;i++){ u32x4 o;
        #pragma unroll
        for(int j=0;j<4;j++) o[j]=pk2(f[i][2*j]*r,f[i][2*j+1]*r);
        *(u32x4*)(smw+(i<4? c16+16*i: 64+(c16&7))*80+g*16)=o; }
      asm volatile("s_waitcnt lgkmcnt(0)":::"memory");
      const rsrc_t rx=mkrs(ws+O_XN+(size_t)(b>>1)*18432+(b&1)*128+w*64,18432);
      #pragma unroll
      for(int i=0;i<5;i++){ const int ch=(lane>>2)+16*i; if(ch<72) ST16(*(const u32x4*)(smw+ch*80+pp*16),rx,ch*256+pp*16,0); }
      const rsrc_t rm=mkrs(ws+O_MX+(size_t)b*32+w*16,16);
      if(c16==0) ST4(m,rm,g*4,0);
    } else if(w<4){
      if(b<144){
        const int s=b>>3, hq=b&7, h2=w-2;
        u32x4 v[8];
        const char* src=(const char*)p.wqkv+(size_t)s*32*3456+hq*144+h2*64;
        int lo_[8];
        #pragma unroll
        for(int i=0;i<8;i++){ const int r=g+4*i; int c=c16<15?c16:14; const int part=c/5, cc=c-part*5; lo_[i]=r*272+c*16; gload16<0>(v[i],src,r*3456+part*1152+cc*16); }
        asm volatile("s_waitcnt vmcnt(0)" : "+v"(v[0]),"+v"(v[1]),"+v"(v[2]),"+v"(v[3]),"+v"(v[4]),"+v"(v[5]),"+v"(v[6]),"+v"(v[7]) :: "memory");
        #pragma unroll
        for(int i=0;i<8;i++) *(u32x4*)(smw+lo_[i])=v[i];
        asm volatile("s_waitcnt lgkmcnt(0)":::"memory");
        const rsrc_t rw=mkrs(ws+O_WQ+(size_t)(hq*2+h2)*144*1024,144*1024);
        #pragma unroll
        for(int t=0;t<8;t++){
          const int vc=(t<3? 16*t:16*(t-3))+4*pp;
          const int col= t<3? (vc<36? vc:0) : (vc<36? 40+vc : (vc<72? 80+vc-36 : 0));
          const unsigned ba=smwb+(8*g+q)*272+(h2*4+col)*2;
          u32x4 o=__builtin_bit_cast(u32x4,cat(trread(ba),trread(ba+4*272)));
          if((t<3? 16*t:16*(t-3))+c16>=(t<3?36:72)) o=(u32x4){0,0,0,0};
          ST16(o,rw,voff,(t<3? 90+s*3+t : s*5+t-3)*1024); }
      } else if(b<240){
        const int jb=b-144, kap=jb>>2, ct=(jb&3)*2+(w-2);
        const int kq=kap/6, kl=kap%6;
        u32x4 v[5];
        const char* src=(const char*)p.wo+ct*144;
        bool okr[5]; int lo_[5];
        #pragma unroll
        for(int i=0;i<5;i++){ int e=lane+64*i; if(e>287) e=287; const int r=e/9, c=e-r*9, row=r&15, hi=r>>4;
          int kr; if(kl<4){ kr=(4*kq+kl)*36+(hi? 12+row: row-4); okr[i]= hi|| row>=4; } else { kr=(4*kq+2*(kl-4)+hi)*36+28+row; okr[i]=row<8; }
          lo_[i]=r*160+c*16; gload16<0>(v[i],src,(okr[i]?kr:0)*1152+c*16); }
        asm volatile("s_waitcnt vmcnt(0)" : "+v"(v[0]),"+v"(v[1]),"+v"(v[2]),"+v"(v[3]),"+v"(v[4]) :: "memory");
        #pragma unroll
        for(int i=0;i<5;i++) *(u32x4*)(smw+lo_[i])= okr[i]? v[i]:(u32x4){0,0,0,0};
        asm volatile("s_waitcnt lgkmcnt(0)":::"memory");
        const rsrc_t rw=mkrs(ws+O_WO+(size_t)ct*120*1024,120*1024);
        #pragma unroll
        for(int nt=0;nt<5;nt++){
          const int col=nt*16+4*((nt==4)?(pp&1):pp);
          const unsigned ba=smwb+(4*g+q)*160+col*2;
          u32x4 o=__builtin_bit_cast(u32x4,cat(trread(ba),trread(ba+16*160)));
          if(nt==4 && c16>=8) o=(u32x4){ (kl<4 && g==0 && (c16&3)==0)? 0x3f803f80u:0u ,0u,0u,0u};
          ST16(o,rw,voff,(kap*5+nt)*1024); }
      }
    } else if(w==4 && rt<9){
      u32x4 v[5];
      const char* rp=(const char*)p.wo+(size_t)(hp*36+rt*4)*1152;
      const unsigned v0=g*1152+c16*16, v4=g*1152+(64+(c16&7))*16;
      gload16<0>(v[0],rp,v0); gload16<256>(v[1],rp,v0); gload16<512>(v[2],rp,v0); gload16<768>(v[3],rp,v0); gload16<0>(v[4],rp,v4);
      asm volatile("s_waitcnt vmcnt(0)" : "+v"(v[0]),"+v"(v[1]),"+v"(v[2]),"+v"(v[3]),"+v"(v[4]) :: "memory");
      float sacc=0;
      #pragma unroll
      for(int i=0;i<5;i++){ const unsigned one=(i<4||c16<8)?0x3f803f80u:0u;
        #pragma unroll
        for(int j=0;j<4;j++) sacc=dot2(v[i][j],one,sacc); }
      sacc+=__shfl_xor(sacc,1); sacc+=__shfl_xor(sacc,2); sacc+=__shfl_xor(sacc,4); sacc+=__shfl_xor(sacc,8);
      if(c16==0) ST4(sacc*(1.f/576.f),mkrs(ws+O_WB+(size_t)(hp*36+rt*4)*4,16),g*4,0);
    }
    if(w>=5){ __builtin_amdgcn_s_sleep(WARM_SLEEP); touch_gu(1,F0_UNITS? (w-5):(w==5?4:5),5); }
    STAMP(1);
    if(w<5) asm volatile("s_waitcnt vmcnt(0) lgkmcnt(0)":::"memory");
    BAR();
    { const u32x2 e=*(const u32x2*)(sm+TAG_LDS); unsigned lo=__builtin_amdgcn_readfirstlane(e[0]), hi=__builtin_amdgcn_readfirstlane(e[1]); tag=(((u64)hi<<32)|lo)+1ull; }
    if(tid<2 && (tid==0 || b<240)){
      const size_t fo= tid==0? O_F0+b*8 : (b<144? O_FW+(b&7)*256+(b>>3)*8 : O_FWO+((b-144)&3)*256+((b-144)>>2)*8);
      __hip_atomic_store((GAS u64*)(ws+fo),tag,__ATOMIC_RELAXED,__HIP_MEMORY_SCOPE_AGENT); }
    STAMP(2);
    if(w==0) pollp((const GAS u64*)(ws+(lane<16? O_F0+rt*128+lane*8 : (lane<34? O_FW+(hp>>1)*256+(lane-16)*8 : O_FWO+((idx&7)>>1)*256+(lane<58?lane-34:0)*8))),tag);
    BAR();
  }
  STAMP(3);
  const int tt=rt*8+w;
  const int ct3=idx&7, r3=xcd*4+(idx>>3);
  constexpr int WOL=8*1024;
  bf16x8 q01; u32x2 q2;
  bool okv[2][4]; bool anyok[2]={false,false};
  {
    using namespace f1;
    const char* wbase=ws+O_WQ+(size_t)hp*144*1024+w*1024;
    const char* abase=ws+O_XN+(size_t)tt*18*1024;
    bf16x8 af[18];
    auto ldW=[&](auto R){ constexpr int r=R.v; const int pc=r*8+w; glds16c(wbase+r*8192,voff,smb+((r<11||(r==11&&0))? pc : (pc<90? pc:pc+12))*1024); };
    auto ldX=[&](auto Sx){ constexpr int s=Sx.v; gload16c<(s&3)*1024>(af[s],abase+(s>>2)*4096,voff); };
    sfor<0,XA>([&](auto K){ if constexpr(K.v<WA) ldW(K); ldX(K); });
    f32x4 acc[8];
    unsigned lwb[4];
    #pragma unroll
    for(int i=0;i<4;i++){ lwb[i]=smb+voff+i*49152; asm volatile("" : "+v"(lwb[i])); }
    bf16x8 wf[PF];
    #define LDW1(i) (*(const LAS bf16x8*)((const LAS char*)(size_t)lwb[lpos(i)/48]+(lpos(i)%48)*1024))
    #define WAITP(m,n) asm volatile("s_waitcnt vmcnt(%2)" : "+v"(af[2*(m)]),"+v"(af[2*(m)+1]) : "i"(n) : "memory")
    WAITP(0,vmc(0,1,10)); BAR();
    STAMP(4);
    sfor<0,PF-1>([&](auto I){ wf[I.v]=LDW1(I.v); });
    sfor<0,90>([&](auto I){ constexpr int i=I.v; constexpr int s=i/5,t=i%5;
      if constexpr(i%10==4 && i/10<8){ constexpr int m=i/10+1; WAITP(m,vmc(i,2*m+1,10*(m+1))); BAR(); }
      if constexpr(i+PF-1<90) wf[(i+PF-1)%PF]=LDW1(i+PF-1);
      SB();
      if constexpr(s==0) acc[3+t]=MF16(af[s],wf[i%PF],(f32x4){}); else acc[3+t]=MF16(af[s],wf[i%PF],acc[3+t]);
      SB();
      if constexpr(t==1 && s+XA<18) ldX(IC<s+XA>{});
      if constexpr(roundAt(i)>=0) ldW(IC<roundAt(i)>{});
      SB();
    });
    STAMP(5);
    u32x2 kvb[5];
    #pragma unroll
    for(int t=0;t<5;t++){ float x[4];
      #pragma unroll
      for(int r4=0;r4<4;r4++){ float a=acc[3+t][r4];
        if(t<2) a=phi(a); else if(t==2) a= c16<4? phi(a):a; else if(t==4) a= c16<8? a : 1.f;
        x[r4]=a; }
      kvb[t][0]=pk2(x[0],x[1]); kvb[t][1]=pk2(x[2],x[3]); }
    BAR();
    { char* xs=sm+w*8192+voff;
      #define KVM(idx,kt,vt) *(f32x4*)(xs+(idx)*1024)=__builtin_amdgcn_mfma_f32_16x16x16bf16_1k(__builtin_bit_cast(s16x4,kvb[kt]),__builtin_bit_cast(s16x4,kvb[vt]),(f32x4){},0,0,0)
      KVM(0,0,2); KVM(1,1,2); KVM(2,0,3); KVM(3,1,3); KVM(4,2,3); KVM(5,0,4); KVM(6,1,4); KVM(7,2,4); }
    waitv<0>();
    LBAR();
    touch_gu(0,w-4,4);
    #pragma unroll
    for(int x=0;x<2;x++){ const int tl=(w&3)+4*x;
      const int kt=(tl==0||tl==2||tl==5)?0:((tl==1||tl==3||tl==6)?1:2), vt= tl<2?2:(tl<5?3:4);
      #pragma unroll
      for(int r4=0;r4<4;r4++){ int d=16*kt+4*g+r4, v=16*vt+c16;
        bool ok; if(v<72) ok= v>=36 && d<36 && ((d<18)==(v-36<18)); else ok=(c16==8||c16==12)? d<18 : ((c16==9||c16==13)? (d>=18&&d<36):false);
        okv[x][r4]=ok; anyok[x]|=ok; } }
    if(w<4){
      const rsrc_t rkv=mkrs(ws+O_KVP+(size_t)hp*16*8192,16*8192);
      #pragma unroll
      for(int x=0;x<2;x++){ const int tl=w+4*x;
        f32x4 sacc=*(const f32x4*)(sm+tl*1024+voff);
        #pragma unroll
        for(int ww=1;ww<8;ww++) sacc+=*(const f32x4*)(sm+ww*8192+tl*1024+voff);
        if(anyok[x]) ST16(sacc,rkv,voff,rt*8192+tl*1024); }
    }
    auto qsteps=[&](auto B,auto E){ constexpr int b0=B.v,e0=E.v;
      sfor<b0,(b0+PF-1<e0?b0+PF-1:e0)>([&](auto I){ wf[I.v%PF]=LDW1(I.v); });
      sfor<b0,e0>([&](auto I){ constexpr int i=I.v; constexpr int s=(i-90)/3,t=(i-90)%3;
        if constexpr(i+PF-1<e0) wf[(i+PF-1)%PF]=LDW1(i+PF-1);
        SB();
        if constexpr(s==0) acc[t]=MF16(wf[i%PF],af[s],(f32x4){}); else acc[t]=MF16(wf[i%PF],af[s],acc[t]);
        SB(); }); };
    qsteps(IC<90>{},IC<90+QSPLIT>{});
    if(w<4) waitv<0>();
    BAR();
    if(tid==0) __hip_atomic_store((GAS u64*)(ws+O_FKV+hp*256+rt*8),tag,__ATOMIC_RELAXED,__HIP_MEMORY_SCOPE_AGENT);
    STAMP(6);
    if(w>=4){
      const char* wob=ws+O_WO+(size_t)ct3*120*1024;
      #pragma unroll
      for(int i=0;i<24;i++){ int pc=(w-4)+4*i; if(pc>93) pc=93; glds16c(wob+pc*1024,voff,smb+WOL+pc*1024); } }
    qsteps(IC<90+QSPLIT>{},IC<144>{});
    { u32x4 v; v[0]=pk2(phi(acc[0][0]),phi(acc[0][1])); v[1]=pk2(phi(acc[0][2]),phi(acc[0][3])); v[2]=pk2(phi(acc[1][0]),phi(acc[1][1])); v[3]=pk2(phi(acc[1][2]),phi(acc[1][3]));
      q01=__builtin_bit_cast(bf16x8,v);
      q2[0]=pk2(phi(acc[2][0]),phi(acc[2][1])); q2[1]=pk2(phi(acc[2][2]),phi(acc[2][3])); }
  }
  if(w==0) poll<16>(ws,O_FKV+hp*256,tag,lane);
  BAR();
  STAMP(22);
  if(w<4){
    u32x4 pv[2][16]; u32x4 wb4;
    { const char* pb=ws+O_KVP+(size_t)hp*16*8192+w*1024;
      #pragma unroll
      for(int x=0;x<2;x++){ const unsigned vo=(anyok[x]?voff:0u)+x*4096;
        #pragma unroll
        for(int i=0;i<16;i++) gload16c<0>(pv[x][i],pb+i*8192,vo); }
      gload16c<0>(wb4,ws+O_WB+hp*144,(lane<9?lane:0)*16); }
    #pragma unroll
    for(int x=0;x<2;x++){
      if(x==0) waitv<17>(); else waitv<0>();
      asm volatile("" : "+v"(pv[x][0]),"+v"(pv[x][1]),"+v"(pv[x][2]),"+v"(pv[x][3]),"+v"(pv[x][4]),"+v"(pv[x][5]),"+v"(pv[x][6]),"+v"(pv[x][7]),
         "+v"(pv[x][8]),"+v"(pv[x][9]),"+v"(pv[x][10]),"+v"(pv[x][11]),"+v"(pv[x][12]),"+v"(pv[x][13]),"+v"(pv[x][14]),"+v"(pv[x][15]) :: "memory");
      f32x4 sacc=__builtin_bit_cast(f32x4,pv[x][0]);
      #pragma unroll
      for(int i=1;i<16;i++) sacc+=__builtin_bit_cast(f32x4,pv[x][i]);
      u32x2 pk; pk[0]=pk2(okv[x][0]?sacc[0]:0.f,okv[x][1]?sacc[1]:0.f); pk[1]=pk2(okv[x][2]?sacc[2]:0.f,okv[x][3]?sacc[3]:0.f);
      *(u32x2*)(sm+(w+4*x)*512+lane*8)=pk; }
    asm volatile("" : "+v"(wb4));
    if(w==0 && lane<9) *(u32x4*)(sm+4096+lane*16)=wb4;
  } else {
    __builtin_amdgcn_s_sleep(8);
    const char* wob=ws+O_WO+(size_t)ct3*120*1024;
    #pragma unroll
    for(int i=0;i<7;i++){ int pc=94+(w-4)+4*i; if(pc>119) pc=119; glds16c(wob+pc*1024,voff,smb+WOL+pc*1024); }
  }
  LBAR();
  STAMP(7);
  { const char* kk=sm+lane*8; const u32x2 z2={0,0};
    #define KF(i) (*(const u32x2*)(kk+(i)*512))
    f32x4 A=MF16(cat2(KF(0),KF(1)),q01,(f32x4){});
    f32x4 B=MF16(cat2(KF(2),KF(3)),q01,(f32x4){}); B=MF16(cat2(KF(4),z2),cat2(q2,z2),B);
    f32x4 C=MF16(cat2(KF(5),KF(6)),q01,(f32x4){}); C=MF16(cat2(KF(7),z2),cat2(q2,z2),C);
    const float ra=__builtin_amdgcn_rcpf(bcast_hi(C[0])+1e-6f), rb=__builtin_amdgcn_rcpf(bcast_hi(C[1])+1e-6f);
    const float b01= g<2? ra:rb, b23= g<1? ra:rb, cc= g<2? rb:0.f;
    A*=ra; B[0]*=b01; B[1]*=b01; B[2]*=b23; B[3]*=b23; C*=cc;
    const char* wbl=sm+4096;
    const f32x4 t4=A*(*(const f32x4*)(wbl+(g>0?g-1:0)*16))+B*(*(const f32x4*)(wbl+48+g*16))+C*(*(const f32x4*)(wbl+112+(g<2?g:0)*16));
    const float mu=sum_g((t4[0]+t4[1])+(t4[2]+t4[3]));
    const float muh=lo16((unsigned)f2bf(mu));
    if(g==0){ A[0]=muh; A[1]=mu-muh; }
    const rsrc_t ro=mkrs(ws+O_O+((size_t)tt*16+hp)*1536,1536);
    ST16(((u32x4){pk2(A[0],A[1]),pk2(A[2],A[3]),pk2(B[0],B[1]),pk2(B[2],B[3])}),ro,voff,0);
    ST8(((u32x2){pk2(C[0],C[1]),pk2(C[2],C[3])}),ro,lane*8,1024);
  }
  publish(ws,O_FO+rt*256+hp*8,tag,w);
  asm volatile("" :: "v"(wm[0]),"v"(wm[1]));
  STAMP(8);
  const int r1=(xcd&1)*4+(idx&3), fs=(xcd>>1)*8+(idx>>2);
  const int odd=fs&1;
  const char* wsrc=(const char*)((w>>2)? p.wu:p.wg)+(size_t)(fs*9+odd)*32;
  const unsigned wdst=smb+(w>>2)*73728+(w&3)*1024;
  unsigned vA,vC; u64 mB,mC;
  { const int r8=lane>>3, pos=lane&7, sl=(pos>>1)^((r8>>1)&3);
    vA=r8*(F*2)+sl*32+(pos&1)*16;
    vC=r8*(F*2)+(sl==3? 256:(odd?0:288))+(pos&1)*16;
    mB=__ballot(sl<3); mC=__ballot(sl==3||sl==0); }
  const int w3=w&3;
  auto wrow=[&](int kc0,int kst){ return wsrc+(size_t)(kc0+w3*kst)*(F*2); };
  {
    const int th=w&1, kq=w>>1;
    const int tq=th*2+(kq>>1), hf=kq&1;
    const int t3=r3*4+tq;
    constexpr int XS=116*1024;
    bf16x8 wpre[30];
    { const char* rb=sm+WOL+kq*30720+voff;
      #pragma unroll
      for(int i=0;i<30;i++) wpre[i]=*(const bf16x8*)(rb+i*1024); }
    u32x2 xv[3]; unsigned mxu;
    { const char* xb=(const char*)p.x+(size_t)t3*16*1152+ct3*144;
      #pragma unroll
      for(int i=0;i<3;i++){ int nt= hf? 2+i : (i<2? i:1); int n=nt*16+4*g; if(n>=72) n=68;
        asm volatile("global_load_dwordx2 %0, %1, %2" : "=v"(xv[i]) : "v"(c16*1152+n*2), "s"(xb) : "memory"); } }
    LBAR();
    #pragma unroll
    for(int i=0;i<30;i++) asm volatile("" : "+v"(wpre[i]));
    if(w==7){
      poll<16>(ws,O_FO+(r3>>1)*256,tag,lane);
    } else {
      sfor<0,18>([&](auto R){ constexpr int r=R.v;
        if constexpr(r>=11){ if(w<4) glds16(wrow(g1::krow(r),g1::kstep8(r)),vA,wdst+r*4096); } else glds16(wrow(g1::krow(r),g1::kstep8(r)),vA,wdst+r*4096); });
    }
    BAR();
    STAMP(9);
    u32x4 oab[2][4]; u32x2 oc[2][4];
    { const char* ob=ws+O_O+((size_t)(r3*4+th*2)*16+kq*4)*1536;
      #pragma unroll
      for(int i=0;i<4;i++){ gload16c<0>(oab[0][i],ob+i*1536,voff); gload16c<0>(oab[1][i],ob+24576+i*1536,voff); }
      #pragma unroll
      for(int i=0;i<4;i++){ asm volatile("global_load_dwordx2 %0, %1, %2 offset:1024 sc1" : "=v"(oc[0][i]) : "v"(lane*8), "s"(ob+i*1536) : "memory");
                            asm volatile("global_load_dwordx2 %0, %1, %2 offset:1024 sc1" : "=v"(oc[1][i]) : "v"(lane*8), "s"(ob+24576+i*1536) : "memory"); }
      asm volatile("global_load_dword %0, %1, %2 sc1" : "=v"(mxu) : "v"(c16*4), "s"(ws+O_MX+(size_t)t3*64) : "memory"); }
    f32x4 acc[2][5]={};
    sfor<0,4>([&](auto KL){ constexpr int kl=KL.v;
      gwait<15-2*kl>(oab[0][kl],oab[1][kl]);
      #pragma unroll
      for(int t=0;t<2;t++)
        #pragma unroll
        for(int n=0;n<5;n++) acc[t][n]=MF16(wpre[kl*5+n],__builtin_bit_cast(bf16x8,oab[t][kl]),acc[t][n]); });
    asm volatile("s_waitcnt vmcnt(0)" : "+v"(oc[0][0]),"+v"(oc[0][1]),"+v"(oc[0][2]),"+v"(oc[0][3]),"+v"(oc[1][0]),"+v"(oc[1][1]),"+v"(oc[1][2]),"+v"(oc[1][3]),"+v"(mxu),"+v"(xv[0]),"+v"(xv[1]),"+v"(xv[2]) :: "memory");
    #pragma unroll
    for(int kl=0;kl<2;kl++)
      #pragma unroll
      for(int t=0;t<2;t++)
        #pragma unroll
        for(int n=0;n<5;n++) acc[t][n]=MF16(wpre[(4+kl)*5+n],cat2(oc[t][2*kl],oc[t][2*kl+1]),acc[t][n]);
    STAMP(10);
    f32x4 a[5];
    { char* xs=sm+XS+w*5120+voff; const bool hi=kq>>1;
      #pragma unroll
      for(int n=0;n<5;n++){ f32x4 u0=acc[0][n],u1=acc[1][n]; asm volatile("" : "+v"(u0),"+v"(u1)); a[n]=hi?u1:u0; *(f32x4*)(xs+n*1024)=hi?u0:u1; } }
    LBAR();
    { const char* xr=sm+XS+(w^4)*5120+voff;
      #pragma unroll
      for(int n=0;n<5;n++) a[n]+=*(const f32x4*)(xr+n*1024); }
    LBAR();
    f32x4 keep[3];
    { char* xs=sm+XS+w*3072+voff;
      keep[0]=hf?a[2]:a[0]; keep[1]=hf?a[3]:a[1]; keep[2]=a[4];
      *(f32x4*)(xs)=hf?a[0]:a[2]; *(f32x4*)(xs+1024)=hf?a[1]:a[3]; *(f32x4*)(xs+2048)=a[4]; }
    LBAR();
    { const char* xr=sm+XS+(w^2)*3072+voff;
      const rsrc_t rc=mkrs(ws+O_C+(size_t)t3*18*1024,18*1024), rq=mkrs(ws+O_SSQ+(size_t)t3*1024,1024);
      #pragma unroll
      for(int i=0;i<3;i++) keep[i]+=*(const f32x4*)(xr+i*1024);
      const float m=__uint_as_float(mxu)+bcast_hi(keep[2][0]);
      float qs=0; unsigned cp[6];
      #pragma unroll
      for(int i=0;i<3;i++){
        const f32x4 v=keep[i];
        const float c0=(lo16(xv[i][0])+v[0])-m,c1=(hi16(xv[i][0])+v[1])-m,c2=(lo16(xv[i][1])+v[2])-m,c3=(hi16(xv[i][1])+v[3])-m;
        const float qq=(c0*c0+c1*c1)+(c2*c2+c3*c3);
        qs+= (i<2 || (hf && g<2))? qq:0.f;
        cp[2*i]=pk2(c0,c1); cp[2*i+1]=pk2(c2,c3); }
      ST16(((u32x4){cp[0],cp[1],cp[2],cp[3]}),rc,voff,(2*ct3+hf)*1024);
      if(hf && g<2) ST8(((u32x2){cp[4],cp[5]}),rc,((2*(ct3&1)+g)*16+c16)*16+((ct3>>1)&1)*8,(16+(ct3>>2))*1024);
      qs=sum_g(qs);
      if(lane<16){ ST4(qs,rq,lane*4,(ct3*2+hf)*64); if(ct3==0 && hf==0) ST4(m,mkrs(ws+O_MH+(size_t)t3*64,64),lane*4,0); }
    }
    publish(ws,O_FH+(r3>>2)*256+((r3&3)*8+ct3)*8,tag,w);
    if(b==0 && tid==64) __hip_atomic_store((GAS u64*)(ws+O_EP),tag,__ATOMIC_RELAXED,__HIP_MEMORY_SCOPE_AGENT);
  }
  STAMP(11);
  STAMP(12);
  {
    using namespace g1;
    const int t1=r1*16+w*2; const int whi=w>>2;
    const char* abase=ws+O_C+(size_t)t1*18*1024;
    if(w==7) sfor<0,11>([&](auto R){ constexpr int r=R.v; glds16(wrow(krow(r),kstep8(r)),vA,wdst+r*4096); });
    if(w>=4) sfor<11,18>([&](auto R){ constexpr int r=R.v; glds16(wrow(krow(r),kstep8(r)),vA,wdst+r*4096); });
    if(w==0) poll<32>(ws,O_FH+r1*256,tag,lane);
    waitv<0>();
    BAR();
    STAMP(13);
    bf16x8 af[18][2];
    f32x4 sq[2];
    { const char* sp=ws+O_SSQ+(size_t)t1*1024; gload16c<0>(sq[0],sp,voff); gload16c<1024>(sq[1],sp,voff); }
    auto loadA=[&](auto Sx,auto T){ constexpr int s=Sx.v,t=T.v; gload16c<(s&3)*1024>(af[s][t], abase+t*18*1024+(s>>2)*4096, voff); };
    sfor<0,LAK>([&](auto Sx){ loadA(Sx,IC<0>{}); loadA(Sx,IC<1>{}); });
    f32x4 acc[4][2][2];
    const unsigned lb0=smb+(4*g+q)*128+((((g&1)<<1)|(q>>1))*32)+pp*8;
    unsigned ab[4][2];
    bf16x8 wf[PF];
    float c1[2], oo[2], ea, pa; unsigned wmd;
    u32x4 hold[2];
    const rsrc_t ors=mkrs(ws+O_ACT+(size_t)t1*144*1024,2*144*1024);
    const int obase=((fs*9+odd)>>1)*1024;
    const int olast=((fs*9+(odd?0:8))>>1)*1024+(odd?8:0);
    auto rdW=[&](auto I){ constexpr int i=I.v, sl=slotOf(pT(i)), s=pS(i), mat=i&1;
      if constexpr(s==0){ ab[sl][mat]=(lb0^(sl*32))+mat*73728; asm volatile("" : "+v"(ab[sl][mat])); }
      if constexpr(s==9){ ab[sl][mat]+=36864; asm volatile("" : "+v"(ab[sl][mat])); }
      wf[i%PF]=cat(trread(ab[sl][mat]+(s%9)*4096),trread(ab[sl][mat]+(s%9)*4096+2048)); };
    sfor<0,PF-1>(rdW);
    auto epi=[&](auto E,auto K){ constexpr int e=E.v,kk=K.v; constexpr int ET=e>>3, t=(e>>2)&1, r=e&3;
      if constexpr(kk==0){ float gg=acc[ET&3][0][t][r], u=acc[ET&3][1][t][r]; ea=gg*c1[t]; pa=gg*u; asm volatile("" : "+v"(ea),"+v"(pa)); }
      if constexpr(kk==1){ ea=__builtin_amdgcn_exp2f(ea); asm volatile("" : "+v"(ea)); }
      if constexpr(kk==2){ ea=1.f+ea; asm volatile("" : "+v"(ea)); }
      if constexpr(kk==3){ ea=__builtin_amdgcn_rcpf(ea); asm volatile("" : "+v"(ea)); }
      if constexpr(kk==4){
        oo[r&1]=pa*ea;
        if constexpr((r&1)==0) asm volatile("" : "+v"(oo[0]));
        else {
          constexpr int hi=(ET==8||(ET&1)==0)?0:2;
          hold[t][hi+(r>>1)]=pk2(oo[0],oo[1]);
          asm volatile("" : "+v"(hold[t][hi+(r>>1)]));
          if constexpr(r==3){
            if constexpr(ET==8){ ST8(((u32x2){hold[t][0],hold[t][1]}),ors,voff,olast+t*144*1024); }
            else if constexpr((ET&1)==1){ ST16(hold[t],ors,voff,obase+t*144*1024+(ET>>1)*1024); }
          }
        }
      } };
    auto slot=[&](auto M){ constexpr int m=M.v-216-2;
      if constexpr(m>=0){ constexpr int e=(m*4+3)/27; constexpr int kk=m-(e*27)/4;
        if constexpr(e<64 && kk>=0 && kk<5) epi(IC<e>{},IC<kk>{}); } };
    sfor<0,324>([&](auto I){ constexpr int i=I.v; constexpr int T=pT(i), s=pS(i), mat=i&1, j=i-108;
      if constexpr(i==108) STAMP(14);
      if constexpr(i<108 && i%6==0){
        constexpr int iss=(s+LAK<18)?s+LAK:18;
        asm volatile("s_waitcnt vmcnt(%2)" : "+v"(af[s][0]),"+v"(af[s][1]) : "i"(2*(iss-s-1)) : "memory");
        if constexpr(i==0){ asm volatile("" : "+v"(sq[0]),"+v"(sq[1]));
          #pragma unroll
          for(int t=0;t<2;t++){ const float ss=ssq_sum(sq[t],lane); c1[t]=-1.44269504089f*__builtin_amdgcn_rsqf(ss*(1.f/576.f)+1e-5f); }
          asm volatile("" : "+v"(c1[0]),"+v"(c1[1])); } }
      if constexpr(j==2||j==74) BAR();
      if constexpr(j==31){ waitv<5>(); BAR(); }
      if constexpr(j==49||j==139){ waitv<0>(); BAR(); }
      if constexpr(j==139){ if(w==0) flag_store(ws,O_FA+r1*256+fs*8,tag); }
      if constexpr(i<108 && i>0 && i%P1BAR==0) BAR();
      if constexpr(i+PF-1<324) rdW(IC<i+PF-1>{});
      SB();
      if constexpr(s==0) acc[T&3][mat][0]=MF16(wf[i%PF],af[s][0],(f32x4){}); else acc[T&3][mat][0]=MF16(wf[i%PF],af[s][0],acc[T&3][mat][0]);
      SB();
      slot(IC<2*i>{});
      SB();
      if constexpr(s==0) acc[T&3][mat][1]=MF16(wf[i%PF],af[s][1],(f32x4){}); else acc[T&3][mat][1]=MF16(wf[i%PF],af[s][1],acc[T&3][mat][1]);
      SB();
      slot(IC<2*i+1>{});
      if constexpr(j==TOUCH_D){ unsigned o=(w*21+(lane<21?lane:20))*128; if(o>20608) o=20608; gtouch(wmd,(const char*)p.wd+(size_t)b*20736,o); }
      if constexpr(j==140) asm_use(wmd);
      if constexpr(i<108 && (i%6==1||i%6==4) && s+LAK<18) loadA(IC<s+LAK>{},IC<(i%6)/3>{});
      if constexpr(j>=2 && j<=37){ constexpr int r=(j-2)/2; glds16ms(wrow(krow(r),kstep8(r))+128,vA,wdst+r*4096,mB,(j&1)? whi^1:whi); }
      if constexpr(j>=75 && j<=127 && (j-75)%3<2){ constexpr int r=(j-75)/3; glds16ms(wrow(krow(r),kstep8(r))-32,vC,wdst+r*4096,mC,((j-75)%3)? whi^1:whi); }
      SB();
    });
    STAMP(15);
    sfor<64,72>([&](auto E){ sfor<0,5>([&](auto K){ epi(E,K); }); });
    publish(ws,O_FA2+r1*256+fs*8,tag,w);
  }
  STAMP(16);
  {
    using namespace g2;
    const int lane=fresh_lane(); const unsigned voff=lane*16; const int g=lane>>4, q=(lane>>2)&3, pp=lane&3, c16=lane&15;
    const int sp=xcd>>1, r2t=(xcd&1)*8+(idx>>2), ct=idx&3;
    const int tq=w&3, kh=w>>2;
    const int P0=sp*36+kh*18;
    const unsigned rb=smb+kh*73728;
    const char* wbase=(const char*)p.wd+(size_t)P0*32*1152+ct*288;
    const char* abase=ws+O_ACT+((size_t)(r2t*8+tq*2)*144+P0)*1024;
    unsigned wv[3];
    #pragma unroll
    for(int i=0;i<3;i++){ const int sub= i<2? tq+4*i:8; const int id=sub*64+lane; const int row=id/18; wv[i]=row*1152+(id-row*18)*16; }
    auto loadW=[&](auto J,auto X){ constexpr int j=J.v,x=X.v;
      if constexpr(x<2) glds16(wbase+(size_t)kord(j)*32*1152, wv[x], rb+(j%8)*9216+(tq+4*x)*1024);
      else glds16ms(wbase+(size_t)kord(j)*32*1152, wv[2], rb+(j%8)*9216+8192, ~0ull, tq^(j%4)); };
    sfor<0,8>([&](auto J){ loadW(J,IC<0>{}); loadW(J,IC<1>{}); loadW(J,IC<2>{}); });
    if(w==0) poll<8>(ws,O_FA+(r2t>>1)*256+sp*64,tag,lane);
    BAR();
    STAMP(17);
    bf16x8 af[AR][2];
    auto loadA=[&](auto J,auto T){ constexpr int j=J.v,t=T.v; gload16c<0>(af[j%AR][t], abase+t*144*1024+kord(j)*1024, voff); };
    sfor<0,5>([&](auto J){ loadA(J,IC<0>{}); loadA(J,IC<1>{}); });
    f32x4 acc[9][2];
    unsigned lwb[2];
    #pragma unroll
    for(int i=0;i<2;i++){ lwb[i]=rb+(4*g+q)*288+pp*8+i*36864; asm volatile("" : "+v"(lwb[i])); }
    bf16x8 wf[PF];
    #define LDW2(i) cat(trread(lwb[(((i)/9)%8)/4]+((((i)/9)%8)%4)*9216+((i)%9)*32),trread(lwb[(((i)/9)%8)/4]+((((i)/9)%8)%4)*9216+((i)%9)*32+16*288))
    waitv<10>(); BAR();
    sfor<0,PF-1>([&](auto I){ wf[I.v]=LDW2(I.v); });
    sfor<0,NP>([&](auto I){ constexpr int i=I.v; constexpr int j=i/9, nt=i%9;
      if constexpr((i+PF)%18==0 && i+PF<NP){
        if constexpr(i==49){ if(w==0) poll<8>(ws,O_FA2+(r2t>>1)*256+sp*64,tag,lane); }
        if constexpr((i+PF)/9+1>=8) waitv<vmc(i,-1,(i+PF)/9+1)>();
        BAR(); }
      if constexpr(nt==0) gwait<vmc(i,j,-1)>(af[j%AR][0],af[j%AR][1]);
      if constexpr(i+PF-1<NP) wf[(i+PF-1)%PF]=LDW2(i+PF-1);
      SB();
      if constexpr(j==0) acc[nt][0]=MF16(wf[i%PF],af[j%AR][0],(f32x4){}); else acc[nt][0]=MF16(wf[i%PF],af[j%AR][0],acc[nt][0]);
      if constexpr(j==0) acc[nt][1]=MF16(wf[i%PF],af[j%AR][1],(f32x4){}); else acc[nt][1]=MF16(wf[i%PF],af[j%AR][1],acc[nt][1]);
      SB();
      if constexpr(actAt(i)>=0) loadA(IC<actAt(i)/2>{},IC<actAt(i)%2>{});
      if constexpr(j+LW>=8 && j+LW<18){
        if constexpr(nt==1) loadW(IC<j+LW>{},IC<0>{});
        if constexpr(nt==3) loadW(IC<j+LW>{},IC<1>{});
        if constexpr(nt==6) loadW(IC<j+LW>{},IC<2>{}); }
      SB();
    });
    STAMP(18);
    BAR();
    { char* xs=sm+(w*9)*1024+voff;
      #pragma unroll
      for(int n=0;n<9;n++){ f32x4 v= kh? acc[n][0]:acc[n][1]; *(f32x4*)(xs+n*1024)=v; } }
    LBAR();
    f32x4 part[10];
    { const char* xr=sm+((w^4)*9)*1024+voff;
      #pragma unroll
      for(int n=0;n<9;n++){ f32x4 mine= kh? acc[n][1]:acc[n][0]; part[n]=mine+*(const f32x4*)(xr+n*1024); }
      part[9]=part[8]; }
    const int grp=r2t*4+ct;
    const rsrc_t xr=mkrs(ws+O_X+(size_t)grp*160*1024,160*1024);
    if(tq!=sp){
      #pragma unroll
      for(int n=0;n<5;n++) ST16(((u32x4){pk2(part[2*n][0],part[2*n][1]),pk2(part[2*n][2],part[2*n][3]),pk2(part[2*n+1][0],part[2*n+1][1]),pk2(part[2*n+1][2],part[2*n+1][3])}),xr,voff,((tq*4+sp)*10+kh*5+n)*1024);
    } else {
      char* xs=sm+(72+kh*10)*1024+voff;
      #pragma unroll
      for(int n=0;n<10;n++) *(f32x4*)(xs+n*1024)=part[n];
    }
    publish(ws,O_FX+grp*128+sp*8,tag,w);
    STAMP(19);
    const int tk0=r2t*128+sp*32;
    const rsrc_t rh=mkrs(ws+O_C+(size_t)(tk0>>4)*18432,2*18432), rq=mkrs(ws+O_SSQ+(size_t)tk0*64,2048), rmh=mkrs(ws+O_MH+(size_t)tk0*4,128);
    u32x2 hv[2][2]; float r2[2], mh[2]; int go[2][2];
    #pragma unroll
    for(int x=0;x<2;x++){ int u=w+8*x; if(u>9) u=9; const int tl=u>=5, pr=u-5*tl;
      #pragma unroll
      for(int y=0;y<2;y++){ int nt=2*pr+y; if(nt>8) nt=8; const int nl=nt*16+g*4;
        go[x][y]=(tl*16+c16)*1152+(ct*144+nl)*2;
        const int c3=ct*2+(nl>=72), o72=nl>=72? nl-72:nl;
        int kc,gp,hfh; if(o72<64){ kc=2*c3+(o72>>5); hfh=(o72>>4)&1; gp=(o72>>2)&3; } else { kc=16+(c3>>2); gp=2*(c3&1)+((o72-64)>>2); hfh=(c3>>1)&1; }
        hv[x][y]=LD8(rh,(tl*18+kc)*1024+(gp*16+c16)*16+hfh*8,0); }
      const f32x4 s4=__builtin_bit_cast(f32x4,LD16(rq,tl*1024+voff,0));
      mh[x]=__uint_as_float(__builtin_amdgcn_raw_buffer_load_b32(rmh,(tl*16+c16)*4,0,16));
      r2[x]=__builtin_amdgcn_rcpf(ssq_sum(s4,lane)*(1.f/576.f)+1e-5f); }
    if(w==0) poll<4>(ws,O_FX+grp*128,tag,lane);
    BAR();
    STAMP(20);
    u32x4 rv[2][3];
    #pragma unroll
    for(int x=0;x<2;x++){ int u=w+8*x; if(u>9) u=9;
      #pragma unroll
      for(int y=0;y<3;y++){ int src=(sp+1+y)&3; rv[x][y]=LD16(xr,voff,((sp*4+src)*10+u)*1024); } }
    char* const ob=(char*)p.out+(size_t)(r2t*128+sp*32)*1152;
    #pragma unroll
    for(int x=0;x<2;x++){ if(x==0 || w<2){ const int u=w+8*x; const int tl=u>=5, pr=u-5*tl;
      #pragma unroll
      for(int y=0;y<2;y++){ if(y==0 || pr<4){
        f32x4 v=*(const f32x4*)(sm+(72+tl*10+2*pr+y)*1024+voff);
        #pragma unroll
        for(int z=0;z<3;z++){ v[0]+=lo16(rv[x][z][2*y]); v[1]+=hi16(rv[x][z][2*y]); v[2]+=lo16(rv[x][z][2*y+1]); v[3]+=hi16(rv[x][z][2*y+1]); }
        u32x2 ov; ov[0]=pk2(v[0]*r2[x]+(lo16(hv[x][y][0])+mh[x]),v[1]*r2[x]+(hi16(hv[x][y][0])+mh[x])); ov[1]=pk2(v[2]*r2[x]+(lo16(hv[x][y][1])+mh[x]),v[3]*r2[x]+(hi16(hv[x][y][1])+mh[x]));
        *(u32x2*)(ob+go[x][y])=ov; } } } }
  }
  STAMP(21);
  STAMP_OUT();
}
}

extern "C" void kernel_launch(void* const* d_in, const int* in_sizes, int n_in,
                              void* d_out, int out_size, void* d_ws, size_t ws_size,
                              hipStream_t stream) {
  using namespace k;
  Params p;
  p.x=(const u16*)d_in[0]; p.wqkv=(const u16*)d_in[1]; p.wo=(const u16*)d_in[2];
  p.wg=(const u16*)d_in[4]; p.wu=(const u16*)d_in[5]; p.wd=(const u16*)d_in[6];
  p.out=(u16*)d_out; p.ws=(char*)d_ws;
  fused_k<<<256,512,0,stream>>>(p);
}
```

```cpp
#include <hip/hip_runtime.h>
#include <hip/hip_bf16.h>
typedef unsigned short u16;
typedef unsigned long long u64;
typedef short s16x4 __attribute__((ext_vector_type(4)));
typedef short s16x8 __attribute__((ext_vector_type(8)));
typedef __bf16 bf16x8 __attribute__((ext_vector_type(8)));
typedef __bf16 bf16x2 __attribute__((ext_vector_type(2)));
typedef float f32x4 __attribute__((ext_vector_type(4)));
typedef unsigned u32x4 __attribute__((ext_vector_type(4)));
typedef unsigned u32x2 __attribute__((ext_vector_type(2)));
#define LAS __attribute__((address_space(3)))
#define GAS __attribute__((address_space(1)))
#define DI __device__ __forceinline__

#define STAMP(i)
#define STAMP_OUT()

#define WARM 21
#define WARM_SLEEP 20
#define STAG 1
#define PRETOUCH 1
#define CODE_KB 48
#define WARM_MODE 1
#define TOUCH_AT 121
#define F0_UNITS 0
#define TOUCH_D 50
#define QSPLIT 24
#define F1_XA 8
#define F1_WA 6
#define P1BAR 36
#define G2_PF 5
#define G2_PRIO 0
#define G2_PRO 4
#define F1_LA 6
namespace k {
constexpr int S=2048, D=576, F=4608;
constexpr int STAMP_LDS=156*1024, TAG_LDS=155*1024;
DI int fresh_lane(){ int l; asm volatile("v_mbcnt_lo_u32_b32 %0, -1, 0\n\tv_mbcnt_hi_u32_b32 %0, -1, %0" : "=v"(l)); return l; }
DI u16 f2bf(float f){ __bf16 b=(__bf16)f; return __builtin_bit_cast(u16,b); }
DI unsigned pk2(float lo,float hi){ bf16x2 v; v[0]=(__bf16)lo; v[1]=(__bf16)hi; return __builtin_bit_cast(unsigned,v); }
DI float lo16(unsigned v){ return __uint_as_float(v<<16); }
DI float hi16(unsigned v){ return __uint_as_float(v&0xffff0000u); }
DI float phi(float t){ return (t>0.f? t+1.f: __builtin_amdgcn_exp2f(t*1.44269504089f))+1e-6f; }
DI float bcast_hi(float x){ unsigned a=__float_as_uint(x); auto r=__builtin_amdgcn_permlane32_swap(a,a,false,false); return __uint_as_float(r[1]); }
DI float sum_g(float x){
  unsigned a=__float_as_uint(x); auto r=__builtin_amdgcn_permlane32_swap(a,a,false,false); x=__uint_as_float(r[0])+__uint_as_float(r[1]);
  a=__float_as_uint(x); auto r2=__builtin_amdgcn_permlane16_swap(a,a,false,false); return __uint_as_float(r2[0])+__uint_as_float(r2[1]); }
DI float ssq_sum(f32x4 v,int lane){
  #pragma unroll
  for(int r=0;r<4;r++){ float x=sum_g(v[r]); x+=__shfl_xor(x,4); x+=__shfl_xor(x,8); v[r]=x; }
  float o[4];
  #pragma unroll
  for(int r=0;r<4;r++) o[r]=__shfl(v[r],(lane>>2)&3);
  const int k=lane&3; return k==0? o[0] : (k==1? o[1] : (k==2? o[2]:o[3])); }
DI float dot2(unsigned a,unsigned b,float c){ return __builtin_amdgcn_fdot2_f32_bf16(__builtin_bit_cast(bf16x2,a),__builtin_bit_cast(bf16x2,b),c,false); }
DI s16x4 trread(unsigned a){ return __builtin_amdgcn_ds_read_tr16_b64_v4i16((LAS s16x4*)(size_t)a); }
DI bf16x8 cat(s16x4 a,s16x4 b){ return __builtin_bit_cast(bf16x8,__builtin_shufflevector(a,b,0,1,2,3,4,5,6,7)); }
DI bf16x8 cat2(u32x2 a,u32x2 b){ return __builtin_bit_cast(bf16x8,(u32x4){a[0],a[1],b[0],b[1]}); }
#define BAR() do{ asm volatile("":::"memory"); __builtin_amdgcn_s_barrier(); asm volatile("":::"memory"); }while(0)
#define LBAR() do{ asm volatile("s_waitcnt lgkmcnt(0)":::"memory"); __builtin_amdgcn_s_barrier(); asm volatile("":::"memory"); }while(0)
#define MF16(a,bb,c) __builtin_amdgcn_mfma_f32_16x16x32_bf16(a,bb,c,0,0,0)
#define SB() __builtin_amdgcn_sched_barrier(0)
#define WAITV0() __builtin_amdgcn_s_waitcnt(0x0F70)
template<int I> struct IC{ static constexpr int v=I; };
template<int B,int E,class Fn> DI void sfor(Fn&& f){ if constexpr(B<E){ f(IC<B>{}); sfor<B+1,E>(f); } }
DI void glds16(const void* sbase, unsigned voff, unsigned lds){
  unsigned keep;
  asm volatile("s_mov_b32 %0, m0\n\ts_mov_b32 m0, %3\n\ts_nop 0\n\tglobal_load_lds_dwordx4 %1, %2\n\ts_mov_b32 m0, %0" : "=&s"(keep) : "v"(voff), "s"(sbase), "s"(lds) : "memory");
}
DI void glds16c(const void* sbase, unsigned voff, unsigned lds){
  unsigned keep;
  asm volatile("s_mov_b32 %0, m0\n\ts_mov_b32 m0, %3\n\ts_nop 0\n\tglobal_load_lds_dwordx4 %1, %2 sc1\n\ts_mov_b32 m0, %0" : "=&s"(keep) : "v"(voff), "s"(sbase), "s"(lds) : "memory");
}
DI void glds16m(const void* sbase, unsigned voff, unsigned lds, u64 mask){
  unsigned keep;
  asm volatile("s_mov_b32 %0, m0\n\ts_mov_b32 m0, %3\n\ts_mov_b64 exec, %4\n\tglobal_load_lds_dwordx4 %1, %2\n\ts_mov_b64 exec, -1\n\ts_mov_b32 m0, %0" : "=&s"(keep) : "v"(voff), "s"(sbase), "s"(lds), "s"(mask) : "memory");
}
DI void glds16ms(const void* sbase, unsigned voff, unsigned lds, u64 mask, int skip){
  unsigned keep;
  asm volatile("s_cmp_lg_u32 %5, 0\n\ts_cbranch_scc1 1f\n\ts_mov_b32 %0, m0\n\ts_mov_b32 m0, %3\n\ts_mov_b64 exec, %4\n\tglobal_load_lds_dwordx4 %1, %2\n\ts_mov_b64 exec, -1\n\ts_mov_b32 m0, %0\n1:" : "=&s"(keep) : "v"(voff), "s"(sbase), "s"(lds), "s"(mask), "s"(skip) : "memory","scc");
}
template<int OFF,class T> DI void gload16(T& r,const void* sbase,unsigned voff){ asm volatile("global_load_dwordx4 %0, %1, %2 offset:%3" : "=v"(r) : "v"(voff), "s"(sbase), "i"(OFF) : "memory"); }
template<int OFF,class T> DI void gload16c(T& r,const void* sbase,unsigned voff){ asm volatile("global_load_dwordx4 %0, %1, %2 offset:%3 sc1" : "=v"(r) : "v"(voff), "s"(sbase), "i"(OFF) : "memory"); }
DI void gtouch(unsigned& r,const void* sbase,unsigned voff){ asm volatile("global_load_dword %0, %1, %2" : "=v"(r) : "v"(voff), "s"(sbase) : "memory"); }
DI void asm_use(unsigned r){ asm volatile("" :: "v"(r)); }
template<int N,class T> DI void gwait(T& a,T& b){ asm volatile("s_waitcnt vmcnt(%2)" : "+v"(a),"+v"(b) : "i"(N) : "memory"); }
template<int N> DI void waitv(){ asm volatile("s_waitcnt vmcnt(%0)" :: "i"(N) : "memory"); }
typedef __amdgpu_buffer_rsrc_t rsrc_t;
DI rsrc_t mkrs(const void* p,unsigned bytes){ return __builtin_amdgcn_make_buffer_rsrc((void*)p,0,bytes,0x00020000); }
#define ST16(v,rs,vo,so) __builtin_amdgcn_raw_buffer_store_b128(__builtin_bit_cast(u32x4,v),rs,vo,so,16)
#define ST8(v,rs,vo,so)  __builtin_amdgcn_raw_buffer_store_b64(__builtin_bit_cast(u32x2,v),rs,vo,so,16)
#define ST4(v,rs,vo,so)  __builtin_amdgcn_raw_buffer_store_b32(__float_as_uint(v),rs,vo,so,16)
#define LD16(rs,vo,so) __builtin_amdgcn_raw_buffer_load_b128(rs,vo,so,16)
#define LD8(rs,vo,so)  __builtin_amdgcn_raw_buffer_load_b64(rs,vo,so,16)

constexpr size_t O_EP=0;
constexpr size_t O_F0 =256;
constexpr size_t O_FW =O_F0+2048;
constexpr size_t O_FWO=O_FW+2048;
constexpr size_t O_FKV=O_FWO+1024;
constexpr size_t O_FO =O_FKV+16*256;
constexpr size_t O_FH =O_FO+16*256;
constexpr size_t O_FA =O_FH+8*256;
constexpr size_t O_FA2=O_FA+8*256;
constexpr size_t O_FX =O_FA2+8*256;
constexpr size_t O_KVP=32768;
constexpr size_t O_XN =O_KVP+(size_t)256*8192;
constexpr size_t O_MX =O_XN+(size_t)S*D*2;
constexpr size_t O_WB =O_MX+(size_t)S*4;
constexpr size_t O_WQ =O_WB+4096;
constexpr size_t O_WO =O_WQ+(size_t)16*144*1024;
constexpr size_t O_O  =O_WO+(size_t)8*120*1024;
constexpr size_t O_MU =O_O+(size_t)128*16*1536;
constexpr size_t O_MH =O_MU+(size_t)S*64;
constexpr size_t O_C  =O_MH+(size_t)S*4;
constexpr size_t O_SSQ=O_C+(size_t)S*D*2;
constexpr size_t O_ACT=O_SSQ+(size_t)S*64;
constexpr size_t O_X  =O_ACT+(size_t)S*F*2;
constexpr size_t O_END=O_X+(size_t)64*160*1024;
struct Params { const u16 *x,*wqkv,*wo,*wg,*wu,*wd; u16* out; char* ws; };

DI void publish(char* ws,size_t off,u64 tag,int w){
  asm volatile("s_waitcnt vmcnt(0) lgkmcnt(0)":::"memory");
  BAR();
  if(w==0 && fresh_lane()==0) __hip_atomic_store((GAS u64*)(ws+off),tag,__ATOMIC_RELAXED,__HIP_MEMORY_SCOPE_AGENT);
}
DI void flag_store(char* ws,size_t off,u64 tag){ if(fresh_lane()==0) __hip_atomic_store((GAS u64*)(ws+off),tag,__ATOMIC_RELAXED,__HIP_MEMORY_SCOPE_AGENT); }
DI void pollp(const GAS u64* p,u64 tag){
  for(unsigned spins=0; spins<(1u<<20); ++spins){
    u64 v=__hip_atomic_load(p,__ATOMIC_RELAXED,__HIP_MEMORY_SCOPE_AGENT);
    if(__all(v==tag)) break;
    __builtin_amdgcn_s_sleep(1);
  }
  __builtin_amdgcn_fence(__ATOMIC_ACQUIRE,"wavefront");
}
template<int N> DI void poll(char* ws,size_t off,u64 tag,int lane){ pollp((const GAS u64*)(ws+off)+(lane<N?lane:0),tag); }

namespace f1 {
constexpr int PF=6, XA=F1_XA, WA=F1_WA;
constexpr int wr(int r){ return r<WA? -1 : (r<12? 5*(r-WA)+3 : 53+5*(r-12)); }
constexpr int roundAt(int i){ for(int r=WA;r<18;r++) if(wr(r)==i) return r; return -1; }
constexpr int vmc(int iw,int xs,int pw){
  int cnt=0;
  for(int k=0;k<XA;k++){ if(k<WA){ if(8*k<pw) cnt=0; else cnt++; } if(k<=xs) cnt=0; else cnt++; }
  for(int i=0;i<iw;i++){
    if(i%5==1 && i/5+XA<18){ if(i/5+XA<=xs) cnt=0; else cnt++; }
    int r=roundAt(i); if(r>=0){ if(8*r<pw) cnt=0; else cnt++; }
  }
  return cnt;
}
constexpr int lpos(int p){ return p<90? p : p+12; }
}
namespace g1 {
constexpr int PF=4, LAK=5;
constexpr int pT(int i){ return i<108? (i%6)/2 : 3+(i-108)/36; }
constexpr int pS(int i){ return i<108? i/6 : ((i-108)%36)/2; }
constexpr int slotOf(int T){ return T<4? T : (T<8? T-4 : 0); }
constexpr int krow(int kc){ return kc<16? 72*(kc>>1)+32*(kc&1) : 288*(kc-16)+64; }
constexpr int kstep8(int kc){ return kc<16? 8:72; }
}
namespace g2 {
constexpr int LW=5, PF=G2_PF, BO=8, NP=162, AR=6;
constexpr int kord(int j){ constexpr int t[18]={0,1,5,6,9,10,14,15, 2,3,4,7,8,11,12,13,16,17}; return t[j]; }
constexpr int PRO=G2_PRO;
constexpr int actStep(int j,int t){ return j<PRO? -1 : (j<6? 5*(j-PRO)+2+2*t : ((j>=8&&j<=10)? 50+2*(j-8)+t : (j<8? 9*(j-4)+2+3*t : 9*(j-5)+2+3*t))); }
constexpr int wStep(int j,int x){ return j<PRO? -1 : (j<8? 5*(j-PRO)+(x==2?3:x) : 9*(j-LW)+(x==0?1:(x==1?3:6))); }
constexpr int vmc(int iw,int ja,int jw){
  int best=63;
  for(int tq=0;tq<4;tq++){
    int cnt=0;
    for(int j=0;j<PRO;j++){ int n=2+((j%4)==tq); if(j<=jw) cnt=0; else cnt+=n; if(j<=ja) cnt=0; else cnt+=2; }
    for(int i=0;i<iw;i++){
      for(int j=PRO;j<18;j++) for(int t=0;t<2;t++) if(actStep(j,t)==i){ if(j<=ja) cnt=0; else cnt++; }
      for(int j=PRO;j<18;j++) for(int x=0;x<3;x++) if(wStep(j,x)==i && (x<2||(j%4)==tq)){ if(j<=jw) cnt=0; else cnt++; }
    }
    if(cnt<best) best=cnt;
  }
  return best;
}
constexpr int wAt(int i){ for(int j=PRO;j<18;j++) for(int x=0;x<3;x++) if(wStep(j,x)==i) return j*3+x; return -1; }
constexpr int actAt(int i){ for(int j=PRO;j<18;j++) for(int t=0;t<2;t++) if(actStep(j,t)==i) return j*2+t; return -1; }
}

__global__ __launch_bounds__(512,2) void fused_k(Params p){
  __shared__ __attribute__((aligned(16))) char sm[160*1024];
  const int tid=threadIdx.x, lane=tid&63, w=__builtin_amdgcn_readfirstlane(tid>>6);
  const int b=blockIdx.x, xcd=b&7, idx=b>>3;
  const unsigned smb=(unsigned)(size_t)(LAS char*)sm;
  const unsigned voff=lane*16;
  const int g=lane>>4, q=(lane>>2)&3, pp=lane&3, c16=lane&15;
  char* const ws=p.ws;
  STAMP(0);
  u64 tag;
  const int hp=(xcd&1)*8+(idx>>2), rt=(xcd>>1)*4+(idx&3);
  unsigned wm[2]={0,0};
  auto touch_gu=[&](int slot,int unit,int lo){
    int ln=unit*64+lane; if(ln>323) ln=323; const int mt=ln>=162; ln-=mt*162;
    const char* src=(const char*)(mt? p.wu : p.wg)+(size_t)b*20736+ln*128;
    asm volatile("s_cmp_lt_u32 %2, %3\n\ts_cbranch_scc1 1f\n\tglobal_load_dword %0, %1, off\n1:" : "+v"(wm[slot]) : "v"(src), "s"(w), "s"(lo) : "memory","scc"); };
  {
    char* const smw=sm+w*16384;
    const unsigned smwb=smb+w*16384;
    if(w<2){
      u32x2 ep; { const unsigned z=0; asm volatile("global_load_dwordx2 %0, %1, %2 sc1" : "=v"(ep) : "v"(z), "s"(ws+(w?(size_t)O_XN:0)) : "memory"); }
      u32x4 v[5];
      { const char* xb=(const char*)p.x+(size_t)(b*8+w*4)*1152; const unsigned v0=g*1152+c16*16, v4=g*1152+(64+(c16&7))*16;
        gload16<0>(v[0],xb,v0); gload16<256>(v[1],xb,v0); gload16<512>(v[2],xb,v0); gload16<768>(v[3],xb,v0); gload16<0>(v[4],xb,v4); }
      asm volatile("s_waitcnt vmcnt(0)" : "+v"(ep),"+v"(v[0]),"+v"(v[1]),"+v"(v[2]),"+v"(v[3]),"+v"(v[4]) :: "memory");
      if(tid==0) *(u32x2*)(sm+TAG_LDS)=ep;
      float s1=0;
      #pragma unroll
      for(int i=0;i<5;i++){ const unsigned one=(i<4||c16<8)?0x3f803f80u:0u;
        #pragma unroll
        for(int j=0;j<4;j++) s1=dot2(v[i][j],one,s1); }
      s1+=__shfl_xor(s1,1); s1+=__shfl_xor(s1,2); s1+=__shfl_xor(s1,4); s1+=__shfl_xor(s1,8);
      const float m=s1*(1.f/576.f);
      float f[5][8]; float s2=0,s2t=0;
      #pragma unroll
      for(int i=0;i<5;i++)
        #pragma unroll
        for(int j=0;j<4;j++){ f[i][2*j]=lo16(v[i][j])-m; f[i][2*j+1]=hi16(v[i][j])-m;
          if(i<4){ s2=fmaf(f[i][2*j],f[i][2*j],s2); s2=fmaf(f[i][2*j+1],f[i][2*j+1],s2); } else { s2t=fmaf(f[i][2*j],f[i][2*j],s2t); s2t=fmaf(f[i][2*j+1],f[i][2*j+1],s2t); } }
      s2+= c16<8? s2t:0.f;
      s2+=__shfl_xor(s2,1); s2+=__shfl_xor(s2,2); s2+=__shfl_xor(s2,4); s2+=__shfl_xor(s2,8);
      const float r=__builtin_amdgcn_rsqf(s2*(1.f/576.f)+1e-5f);
      #pragma unroll
      for(int i=0;i<5;i++){ u32x4 o;
        #pragma unroll
        for(int j=0;j<4;j++) o[j]=pk2(f[i][2*j]*r,f[i][2*j+1]*r);
        *(u32x4*)(smw+(i<4? c16+16*i: 64+(c16&7))*80+g*16)=o; }
      asm volatile("s_waitcnt lgkmcnt(0)":::"memory");
      const rsrc_t rx=mkrs(ws+O_XN+(size_t)(b>>1)*18432+(b&1)*128+w*64,18432);
      #pragma unroll
      for(int i=0;i<5;i++){ const int ch=(lane>>2)+16*i; if(ch<72) ST16(*(const u32x4*)(smw+ch*80+pp*16),rx,ch*256+pp*16,0); }
      const rsrc_t rm=mkrs(ws+O_MX+(size_t)b*32+w*16,16);
      if(c16==0) ST4(m,rm,g*4,0);
    } else if(w<4){
      if(b<144){
        const int s=b>>3, hq=b&7, h2=w-2;
        u32x4 v[8];
        const char* src=(const char*)p.wqkv+(size_t)s*32*3456+hq*144+h2*64;
        int lo_[8];
        #pragma unroll
        for(int i=0;i<8;i++){ const int r=g+4*i; int c=c16<15?c16:14; const int part=c/5, cc=c-part*5; lo_[i]=r*272+c*16; gload16<0>(v[i],src,r*3456+part*1152+cc*16); }
        asm volatile("s_waitcnt vmcnt(0)" : "+v"(v[0]),"+v"(v[1]),"+v"(v[2]),"+v"(v[3]),"+v"(v[4]),"+v"(v[5]),"+v"(v[6]),"+v"(v[7]) :: "memory");
        #pragma unroll
        for(int i=0;i<8;i++) *(u32x4*)(smw+lo_[i])=v[i];
        asm volatile("s_waitcnt lgkmcnt(0)":::"memory");
        const rsrc_t rw=mkrs(ws+O_WQ+(size_t)(hq*2+h2)*144*1024,144*1024);
        #pragma unroll
        for(int t=0;t<8;t++){
          const int vc=(t<3? 16*t:16*(t-3))+4*pp;
          const int col= t<3? (vc<36? vc:0) : (vc<36? 40+vc : (vc<72? 80+vc-36 : 0));
          const unsigned ba=smwb+(8*g+q)*272+(h2*4+col)*2;
          u32x4 o=__builtin_bit_cast(u32x4,cat(trread(ba),trread(ba+4*272)));
          if((t<3? 16*t:16*(t-3))+c16>=(t<3?36:72)) o=(u32x4){0,0,0,0};
          ST16(o,rw,voff,(t<3? 90+s*3+t : s*5+t-3)*1024); }
      } else if(b<240){
        const int jb=b-144, kap=jb>>2, ct=(jb&3)*2+(w-2);
        const int kq=kap/6, kl=kap%6;
        u32x4 v[5];
        const char* src=(const char*)p.wo+ct*144;
        bool okr[5]; int lo_[5];
        #pragma unroll
        for(int i=0;i<5;i++){ int e=lane+64*i; if(e>287) e=287; const int r=e/9, c=e-r*9, row=r&15, hi=r>>4;
          int kr; if(kl<4){ kr=(4*kq+kl)*36+(hi? 12+row: row-4); okr[i]= hi|| row>=4; } else { kr=(4*kq+2*(kl-4)+hi)*36+28+row; okr[i]=row<8; }
          lo_[i]=r*160+c*16; gload16<0>(v[i],src,(okr[i]?kr:0)*1152+c*16); }
        asm volatile("s_waitcnt vmcnt(0)" : "+v"(v[0]),"+v"(v[1]),"+v"(v[2]),"+v"(v[3]),"+v"(v[4]) :: "memory");
        #pragma unroll
        for(int i=0;i<5;i++) *(u32x4*)(smw+lo_[i])= okr[i]? v[i]:(u32x4){0,0,0,0};
        asm volatile("s_waitcnt lgkmcnt(0)":::"memory");
        const rsrc_t rw=mkrs(ws+O_WO+(size_t)ct*120*1024,120*1024);
        #pragma unroll
        for(int nt=0;nt<5;nt++){
          const int col=nt*16+4*((nt==4)?(pp&1):pp);
          const unsigned ba=smwb+(4*g+q)*160+col*2;
          u32x4 o=__builtin_bit_cast(u32x4,cat(trread(ba),trread(ba+16*160)));
          if(nt==4 && c16>=8) o=(u32x4){ (kl<4 && g==0 && (c16&3)==0)? 0x3f803f80u:0u ,0u,0u,0u};
          ST16(o,rw,voff,(kap*5+nt)*1024); }
      }
    } else if(w==4 && rt<9){
      u32x4 v[5];
      const char* rp=(const char*)p.wo+(size_t)(hp*36+rt*4)*1152;
      const unsigned v0=g*1152+c16*16, v4=g*1152+(64+(c16&7))*16;
      gload16<0>(v[0],rp,v0); gload16<256>(v[1],rp,v0); gload16<512>(v[2],rp,v0); gload16<768>(v[3],rp,v0); gload16<0>(v[4],rp,v4);
      asm volatile("s_waitcnt vmcnt(0)" : "+v"(v[0]),"+v"(v[1]),"+v"(v[2]),"+v"(v[3]),"+v"(v[4]) :: "memory");
      float sacc=0;
      #pragma unroll
      for(int i=0;i<5;i++){ const unsigned one=(i<4||c16<8)?0x3f803f80u:0u;
        #pragma unroll
        for(int j=0;j<4;j++) sacc=dot2(v[i][j],one,sacc); }
      sacc+=__shfl_xor(sacc,1); sacc+=__shfl_xor(sacc,2); sacc+=__shfl_xor(sacc,4); sacc+=__shfl_xor(sacc,8);
      if(c16==0) ST4(sacc*(1.f/576.f),mkrs(ws+O_WB+(size_t)(hp*36+rt*4)*4,16),g*4,0);
    }
    if(w>=5){ __builtin_amdgcn_s_sleep(WARM_SLEEP); touch_gu(1,F0_UNITS? (w-5):(w==5?4:5),5); }
    STAMP(1);
    if(w<5) asm volatile("s_waitcnt vmcnt(0) lgkmcnt(0)":::"memory");
    BAR();
    { const u32x2 e=*(const u32x2*)(sm+TAG_LDS); unsigned lo=__builtin_amdgcn_readfirstlane(e[0]), hi=__builtin_amdgcn_readfirstlane(e[1]); tag=(((u64)hi<<32)|lo)+1ull; }
    if(tid<2 && (tid==0 || b<240)){
      const size_t fo= tid==0? O_F0+b*8 : (b<144? O_FW+(b&7)*256+(b>>3)*8 : O_FWO+((b-144)&3)*256+((b-144)>>2)*8);
      __hip_atomic_store((GAS u64*)(ws+fo),tag,__ATOMIC_RELAXED,__HIP_MEMORY_SCOPE_AGENT); }
    STAMP(2);
    if(w==0) pollp((const GAS u64*)(ws+(lane<16? O_F0+rt*128+lane*8 : (lane<34? O_FW+(hp>>1)*256+(lane-16)*8 : O_FWO+((idx&7)>>1)*256+(lane<58?lane-34:0)*8))),tag);
    BAR();
  }
  STAMP(3);
  const int tt=rt*8+w;
  const int ct3=idx&7, r3=xcd*4+(idx>>3);
  constexpr int WOL=8*1024;
  bf16x8 q01; u32x2 q2;
  bool okv[2][4]; bool anyok[2]={false,false};
  {
    using namespace f1;
    const char* wbase=ws+O_WQ+(size_t)hp*144*1024+w*1024;
    const char* abase=ws+O_XN+(size_t)tt*18*1024;
    bf16x8 af[18];
    auto ldW=[&](auto R){ constexpr int r=R.v; const int pc=r*8+w; glds16c(wbase+r*8192,voff,smb+((r<11||(r==11&&0))? pc : (pc<90? pc:pc+12))*1024); };
    auto ldX=[&](auto Sx){ constexpr int s=Sx.v; gload16c<(s&3)*1024>(af[s],abase+(s>>2)*4096,voff); };
    sfor<0,XA>([&](auto K){ if constexpr(K.v<WA) ldW(K); ldX(K); });
    f32x4 acc[8];
    unsigned lwb[4];
    #pragma unroll
    for(int i=0;i<4;i++){ lwb[i]=smb+voff+i*49152; asm volatile("" : "+v"(lwb[i])); }
    bf16x8 wf[PF];
    #define LDW1(i) (*(const LAS bf16x8*)((const LAS char*)(size_t)lwb[lpos(i)/48]+(lpos(i)%48)*1024))
    #define WAITP(m,n) asm volatile("s_waitcnt vmcnt(%2)" : "+v"(af[2*(m)]),"+v"(af[2*(m)+1]) : "i"(n) : "memory")
    WAITP(0,vmc(0,1,10)); BAR();
    STAMP(4);
    sfor<0,PF-1>([&](auto I){ wf[I.v]=LDW1(I.v); });
    sfor<0,90>([&](auto I){ constexpr int i=I.v; constexpr int s=i/5,t=i%5;
      if constexpr(i%10==4 && i/10<8){ constexpr int m=i/10+1; WAITP(m,vmc(i,2*m+1,10*(m+1))); BAR(); }
      if constexpr(i+PF-1<90) wf[(i+PF-1)%PF]=LDW1(i+PF-1);
      SB();
      if constexpr(s==0) acc[3+t]=MF16(af[s],wf[i%PF],(f32x4){}); else acc[3+t]=MF16(af[s],wf[i%PF],acc[3+t]);
      SB();
      if constexpr(t==1 && s+XA<18) ldX(IC<s+XA>{});
      if constexpr(roundAt(i)>=0) ldW(IC<roundAt(i)>{});
      SB();
    });
    STAMP(5);
    u32x2 kvb[5];
    #pragma unroll
    for(int t=0;t<5;t++){ float x[4];
      #pragma unroll
      for(int r4=0;r4<4;r4++){ float a=acc[3+t][r4];
        if(t<2) a=phi(a); else if(t==2) a= c16<4? phi(a):a; else if(t==4) a= c16<8? a : 1.f;
        x[r4]=a; }
      kvb[t][0]=pk2(x[0],x[1]); kvb[t][1]=pk2(x[2],x[3]); }
    BAR();
    { char* xs=sm+w*8192+voff;
      #define KVM(idx,kt,vt) *(f32x4*)(xs+(idx)*1024)=__builtin_amdgcn_mfma_f32_16x16x16bf16_1k(__builtin_bit_cast(s16x4,kvb[kt]),__builtin_bit_cast(s16x4,kvb[vt]),(f32x4){},0,0,0)
      KVM(0,0,2); KVM(1,1,2); KVM(2,0,3); KVM(3,1,3); KVM(4,2,3); KVM(5,0,4); KVM(6,1,4); KVM(7,2,4); }
    waitv<0>();
    LBAR();
    touch_gu(0,w-4,4);
    #pragma unroll
    for(int x=0;x<2;x++){ const int tl=(w&3)+4*x;
      const int kt=(tl==0||tl==2||tl==5)?0:((tl==1||tl==3||tl==6)?1:2), vt= tl<2?2:(tl<5?3:4);
      #pragma unroll
      for(int r4=0;r4<4;r4++){ int d=16*kt+4*g+r4, v=16*vt+c16;
        bool ok; if(v<72) ok= v>=36 && d<36 && ((d<18)==(v-36<18)); else ok=(c16==8||c16==12)? d<18 : ((c16==9||c16==13)? (d>=18&&d<36):false);
        okv[x][r4]=ok; anyok[x]|=ok; } }
    if(w<4){
      const rsrc_t rkv=mkrs(ws+O_KVP+(size_t)hp*16*8192,16*8192);
      #pragma unroll
      for(int x=0;x<2;x++){ const int tl=w+4*x;
        f32x4 sacc=*(const f32x4*)(sm+tl*1024+voff);
        #pragma unroll
        for(int ww=1;ww<8;ww++) sacc+=*(const f32x4*)(sm+ww*8192+tl*1024+voff);
        if(anyok[x]) ST16(sacc,rkv,voff,rt*8192+tl*1024); }
    }
    auto qsteps=[&](auto B,auto E){ constexpr int b0=B.v,e0=E.v;
      sfor<b0,(b0+PF-1<e0?b0+PF-1:e0)>([&](auto I){ wf[I.v%PF]=LDW1(I.v); });
      sfor<b0,e0>([&](auto I){ constexpr int i=I.v; constexpr int s=(i-90)/3,t=(i-90)%3;
        if constexpr(i+PF-1<e0) wf[(i+PF-1)%PF]=LDW1(i+PF-1);
        SB();
        if constexpr(s==0) acc[t]=MF16(wf[i%PF],af[s],(f32x4){}); else acc[t]=MF16(wf[i%PF],af[s],acc[t]);
        SB(); }); };
    qsteps(IC<90>{},IC<90+QSPLIT>{});
    if(w<4) waitv<0>();
    BAR();
    if(tid==0) __hip_atomic_store((GAS u64*)(ws+O_FKV+hp*256+rt*8),tag,__ATOMIC_RELAXED,__HIP_MEMORY_SCOPE_AGENT);
    STAMP(6);
    if(w>=4){
      const char* wob=ws+O_WO+(size_t)ct3*120*1024;
      #pragma unroll
      for(int i=0;i<24;i++){ int pc=(w-4)+4*i; if(pc>93) pc=93; glds16c(wob+pc*1024,voff,smb+WOL+pc*1024); } }
    qsteps(IC<90+QSPLIT>{},IC<144>{});
    { u32x4 v; v[0]=pk2(phi(acc[0][0]),phi(acc[0][1])); v[1]=pk2(phi(acc[0][2]),phi(acc[0][3])); v[2]=pk2(phi(acc[1][0]),phi(acc[1][1])); v[3]=pk2(phi(acc[1][2]),phi(acc[1][3]));
      q01=__builtin_bit_cast(bf16x8,v);
      q2[0]=pk2(phi(acc[2][0]),phi(acc[2][1])); q2[1]=pk2(phi(acc[2][2]),phi(acc[2][3])); }
  }
  if(w==0) poll<16>(ws,O_FKV+hp*256,tag,lane);
  BAR();
  STAMP(22);
  if(w<4){
    u32x4 pv[2][16]; u32x4 wb4;
    { const char* pb=ws+O_KVP+(size_t)hp*16*8192+w*1024;
      #pragma unroll
      for(int x=0;x<2;x++){ const unsigned vo=(anyok[x]?voff:0u)+x*4096;
        #pragma unroll
        for(int i=0;i<16;i++) gload16c<0>(pv[x][i],pb+i*8192,vo); }
      gload16c<0>(wb4,ws+O_WB+hp*144,(lane<9?lane:0)*16); }
    #pragma unroll
    for(int x=0;x<2;x++){
      if(x==0) waitv<17>(); else waitv<0>();
      asm volatile("" : "+v"(pv[x][0]),"+v"(pv[x][1]),"+v"(pv[x][2]),"+v"(pv[x][3]),"+v"(pv[x][4]),"+v"(pv[x][5]),"+v"(pv[x][6]),"+v"(pv[x][7]),
         "+v"(pv[x][8]),"+v"(pv[x][9]),"+v"(pv[x][10]),"+v"(pv[x][11]),"+v"(pv[x][12]),"+v"(pv[x][13]),"+v"(pv[x][14]),"+v"(pv[x][15]) :: "memory");
      f32x4 sacc=__builtin_bit_cast(f32x4,pv[x][0]);
      #pragma unroll
      for(int i=1;i<16;i++) sacc+=__builtin_bit_cast(f32x4,pv[x][i]);
      u32x2 pk; pk[0]=pk2(okv[x][0]?sacc[0]:0.f,okv[x][1]?sacc[1]:0.f); pk[1]=pk2(okv[x][2]?sacc[2]:0.f,okv[x][3]?sacc[3]:0.f);
      *(u32x2*)(sm+(w+4*x)*512+lane*8)=pk; }
    asm volatile("" : "+v"(wb4));
    if(w==0 && lane<9) *(u32x4*)(sm+4096+lane*16)=wb4;
  } else {
    __builtin_amdgcn_s_sleep(8);
    const char* wob=ws+O_WO+(size_t)ct3*120*1024;
    #pragma unroll
    for(int i=0;i<7;i++){ int pc=94+(w-4)+4*i; if(pc>119) pc=119; glds16c(wob+pc*1024,voff,smb+WOL+pc*1024); }
  }
  LBAR();
  STAMP(7);
  { const char* kk=sm+lane*8; const u32x2 z2={0,0};
    #define KF(i) (*(const u32x2*)(kk+(i)*512))
    f32x4 A=MF16(cat2(KF(0),KF(1)),q01,(f32x4){});
    f32x4 B=MF16(cat2(KF(2),KF(3)),q01,(f32x4){}); B=MF16(cat2(KF(4),z2),cat2(q2,z2),B);
    f32x4 C=MF16(cat2(KF(5),KF(6)),q01,(f32x4){}); C=MF16(cat2(KF(7),z2),cat2(q2,z2),C);
    const float ra=__builtin_amdgcn_rcpf(bcast_hi(C[0])+1e-6f), rb=__builtin_amdgcn_rcpf(bcast_hi(C[1])+1e-6f);
    const float b01= g<2? ra:rb, b23= g<1? ra:rb, cc= g<2? rb:0.f;
    A*=ra; B[0]*=b01; B[1]*=b01; B[2]*=b23; B[3]*=b23; C*=cc;
    const char* wbl=sm+4096;
    const f32x4 t4=A*(*(const f32x4*)(wbl+(g>0?g-1:0)*16))+B*(*(const f32x4*)(wbl+48+g*16))+C*(*(const f32x4*)(wbl+112+(g<2?g:0)*16));
    const float mu=sum_g((t4[0]+t4[1])+(t4[2]+t4[3]));
    const float muh=lo16((unsigned)f2bf(mu));
    if(g==0){ A[0]=muh; A[1]=mu-muh; }
    const rsrc_t ro=mkrs(ws+O_O+((size_t)tt*16+hp)*1536,1536);
    ST16(((u32x4){pk2(A[0],A[1]),pk2(A[2],A[3]),pk2(B[0],B[1]),pk2(B[2],B[3])}),ro,voff,0);
    ST8(((u32x2){pk2(C[0],C[1]),pk2(C[2],C[3])}),ro,lane*8,1024);
  }
  publish(ws,O_FO+rt*256+hp*8,tag,w);
  asm volatile("" :: "v"(wm[0]),"v"(wm[1]));
  STAMP(8);
  const int r1=(xcd&1)*4+(idx&3), fs=(xcd>>1)*8+(idx>>2);
  const int odd=fs&1;
  const char* wsrc=(const char*)((w>>2)? p.wu:p.wg)+(size_t)(fs*9+odd)*32;
  const unsigned wdst=smb+(w>>2)*73728+(w&3)*1024;
  unsigned vA,vC; u64 mB,mC;
  { const int r8=lane>>3, pos=lane&7, sl=(pos>>1)^((r8>>1)&3);
    vA=r8*(F*2)+sl*32+(pos&1)*16;
    vC=r8*(F*2)+(sl==3? 256:(odd?0:288))+(pos&1)*16;
    mB=__ballot(sl<3); mC=__ballot(sl==3||sl==0); }
  const int w3=w&3;
  auto wrow=[&](int kc0,int kst){ return wsrc+(size_t)(kc0+w3*kst)*(F*2); };
  {
    const int th=w&1, kq=w>>1;
    const int tq=th*2+(kq>>1), hf=kq&1;
    const int t3=r3*4+tq;
    constexpr int XS=116*1024;
    bf16x8 wpre[30];
    { const char* rb=sm+WOL+kq*30720+voff;
      #pragma unroll
      for(int i=0;i<30;i++) wpre[i]=*(const bf16x8*)(rb+i*1024); }
    u32x2 xv[3]; unsigned mxu;
    { const char* xb=(const char*)p.x+(size_t)t3*16*1152+ct3*144;
      #pragma unroll
      for(int i=0;i<3;i++){ int nt= hf? 2+i : (i<2? i:1); int n=nt*16+4*g; if(n>=72) n=68;
        asm volatile("global_load_dwordx2 %0, %1, %2" : "=v"(xv[i]) : "v"(c16*1152+n*2), "s"(xb) : "memory"); } }
    LBAR();
    #pragma unroll
    for(int i=0;i<30;i++) asm volatile("" : "+v"(wpre[i]));
    if(w==7){
      poll<16>(ws,O_FO+(r3>>1)*256,tag,lane);
    } else {
      sfor<0,18>([&](auto R){ constexpr int r=R.v;
        if constexpr(r>=11){ if(w<4) glds16(wrow(g1::krow(r),g1::kstep8(r)),vA,wdst+r*4096); } else glds16(wrow(g1::krow(r),g1::kstep8(r)),vA,wdst+r*4096); });
    }
    BAR();
    STAMP(9);
    u32x4 oab[2][4]; u32x2 oc[2][4];
    { const char* ob=ws+O_O+((size_t)(r3*4+th*2)*16+kq*4)*1536;
      #pragma unroll
      for(int i=0;i<4;i++){ gload16c<0>(oab[0][i],ob+i*1536,voff); gload16c<0>(oab[1][i],ob+24576+i*1536,voff); }
      #pragma unroll
      for(int i=0;i<4;i++){ asm volatile("global_load_dwordx2 %0, %1, %2 offset:1024 sc1" : "=v"(oc[0][i]) : "v"(lane*8), "s"(ob+i*1536) : "memory");
                            asm volatile("global_load_dwordx2 %0, %1, %2 offset:1024 sc1" : "=v"(oc[1][i]) : "v"(lane*8), "s"(ob+24576+i*1536) : "memory"); }
      asm volatile("global_load_dword %0, %1, %2 sc1" : "=v"(mxu) : "v"(c16*4), "s"(ws+O_MX+(size_t)t3*64) : "memory"); }
    f32x4 acc[2][5]={};
    sfor<0,4>([&](auto KL){ constexpr int kl=KL.v;
      gwait<15-2*kl>(oab[0][kl],oab[1][kl]);
      #pragma unroll
      for(int t=0;t<2;t++)
        #pragma unroll
        for(int n=0;n<5;n++) acc[t][n]=MF16(wpre[kl*5+n],__builtin_bit_cast(bf16x8,oab[t][kl]),acc[t][n]); });
    asm volatile("s_waitcnt vmcnt(0)" : "+v"(oc[0][0]),"+v"(oc[0][1]),"+v"(oc[0][2]),"+v"(oc[0][3]),"+v"(oc[1][0]),"+v"(oc[1][1]),"+v"(oc[1][2]),"+v"(oc[1][3]),"+v"(mxu),"+v"(xv[0]),"+v"(xv[1]),"+v"(xv[2]) :: "memory");
    #pragma unroll
    for(int kl=0;kl<2;kl++)
      #pragma unroll
      for(int t=0;t<2;t++)
        #pragma unroll
        for(int n=0;n<5;n++) acc[t][n]=MF16(wpre[(4+kl)*5+n],cat2(oc[t][2*kl],oc[t][2*kl+1]),acc[t][n]);
    STAMP(10);
    f32x4 a[5];
    { char* xs=sm+XS+w*5120+voff; const bool hi=kq>>1;
      #pragma unroll
      for(int n=0;n<5;n++){ f32x4 u0=acc[0][n],u1=acc[1][n]; asm volatile("" : "+v"(u0),"+v"(u1)); a[n]=hi?u1:u0; *(f32x4*)(xs+n*1024)=hi?u0:u1; } }
    LBAR();
    { const char* xr=sm+XS+(w^4)*5120+voff;
      #pragma unroll
      for(int n=0;n<5;n++) a[n]+=*(const f32x4*)(xr+n*1024); }
    LBAR();
    f32x4 keep[3];
    { char* xs=sm+XS+w*3072+voff;
      keep[0]=hf?a[2]:a[0]; keep[1]=hf?a[3]:a[1]; keep[2]=a[4];
      *(f32x4*)(xs)=hf?a[0]:a[2]; *(f32x4*)(xs+1024)=hf?a[1]:a[3]; *(f32x4*)(xs+2048)=a[4]; }
    LBAR();
    { const char* xr=sm+XS+(w^2)*3072+voff;
      const rsrc_t rc=mkrs(ws+O_C+(size_t)t3*18*1024,18*1024), rq=mkrs(ws+O_SSQ+(size_t)t3*1024,1024);
      #pragma unroll
      for(int i=0;i<3;i++) keep[i]+=*(const f32x4*)(xr+i*1024);
      const float m=__uint_as_float(mxu)+bcast_hi(keep[2][0]);
      float qs=0; unsigned cp[6];
      #pragma unroll
      for(int i=0;i<3;i++){
        const f32x4 v=keep[i];
        const float c0=(lo16(xv[i][0])+v[0])-m,c1=(hi16(xv[i][0])+v[1])-m,c2=(lo16(xv[i][1])+v[2])-m,c3=(hi16(xv[i][1])+v[3])-m;
        const float qq=(c0*c0+c1*c1)+(c2*c2+c3*c3);
        qs+= (i<2 || (hf && g<2))? qq:0.f;
        cp[2*i]=pk2(c0,c1); cp[2*i+1]=pk2(c2,c3); }
      ST16(((u32x4){cp[0],cp[1],cp[2],cp[3]}),rc,voff,(2*ct3+hf)*1024);
      if(hf && g<2) ST8(((u32x2){cp[4],cp[5]}),rc,((2*(ct3&1)+g)*16+c16)*16+((ct3>>1)&1)*8,(16+(ct3>>2))*1024);
      qs=sum_g(qs);
      if(lane<16){ ST4(qs,rq,lane*4,(ct3*2+hf)*64); if(ct3==0 && hf==0) ST4(m,mkrs(ws+O_MH+(size_t)t3*64,64),lane*4,0); }
    }
    publish(ws,O_FH+(r3>>2)*256+((r3&3)*8+ct3)*8,tag,w);
    if(b==0 && tid==64) __hip_atomic_store((GAS u64*)(ws+O_EP),tag,__ATOMIC_RELAXED,__HIP_MEMORY_SCOPE_AGENT);
  }
  STAMP(11);
  STAMP(12);
  const int sp=xcd>>1, r2t=(xcd&1)*8+(idx>>2), ct=idx&3;
  const int tq=w&3, kh=w>>2;
  const unsigned rb=smb+kh*73728;
  const char* wbase2=(const char*)p.wd+(size_t)(sp*36+kh*18)*32*1152+ct*288;
  const char* abase2=ws+O_ACT+((size_t)(r2t*8+tq*2)*144+sp*36+kh*18)*1024;
  unsigned wv[3], voff2;
  bf16x8 a2[g2::AR][2];
  auto loadW=[&](auto J,auto X_){ constexpr int j=J.v,x=X_.v;
    if constexpr(x<2) glds16(wbase2+(size_t)g2::kord(j)*32*1152, wv[x], rb+(j%8)*9216+(tq+4*x)*1024);
    else glds16ms(wbase2+(size_t)g2::kord(j)*32*1152, wv[2], rb+(j%8)*9216+8192, ~0ull, tq^(j%4)); };
  auto loadA2=[&](auto J,auto T){ constexpr int j=J.v,t=T.v; gload16c<0>(a2[j%g2::AR][t], abase2+t*144*1024+g2::kord(j)*1024, voff2); };
  auto g2_prologue=[&](){
    const int l2=fresh_lane(); voff2=l2*16;
    #pragma unroll
    for(int i=0;i<3;i++){ const int sub= i<2? tq+4*i:8; const int id=sub*64+l2; const int row=id/18; wv[i]=row*1152+(id-row*18)*16; }
    sfor<0,g2::PRO>([&](auto J){ loadW(J,IC<0>{}); loadW(J,IC<1>{}); loadW(J,IC<2>{}); loadA2(J,IC<0>{}); loadA2(J,IC<1>{}); }); };
  {
    using namespace g1;
    const int t1=r1*16+w*2; const int whi=w>>2;
    const char* abase=ws+O_C+(size_t)t1*18*1024;
    if(w==7) sfor<0,11>([&](auto R){ constexpr int r=R.v; glds16(wrow(krow(r),kstep8(r)),vA,wdst+r*4096); });
    if(w>=4) sfor<11,18>([&](auto R){ constexpr int r=R.v; glds16(wrow(krow(r),kstep8(r)),vA,wdst+r*4096); });
    if(w==0) poll<32>(ws,O_FH+r1*256,tag,lane);
    waitv<0>();
    BAR();
    STAMP(13);
    bf16x8 af[18][2];
    f32x4 sq[2];
    { const char* sp=ws+O_SSQ+(size_t)t1*1024; gload16c<0>(sq[0],sp,voff); gload16c<1024>(sq[1],sp,voff); }
    auto loadA=[&](auto Sx,auto T){ constexpr int s=Sx.v,t=T.v; gload16c<(s&3)*1024>(af[s][t], abase+t*18*1024+(s>>2)*4096, voff); };
    sfor<0,LAK>([&](auto Sx){ loadA(Sx,IC<0>{}); loadA(Sx,IC<1>{}); });
    f32x4 acc[4][2][2];
    const unsigned lb0=smb+(4*g+q)*128+((((g&1)<<1)|(q>>1))*32)+pp*8;
    unsigned ab[4][2];
    bf16x8 wf[PF];
    float c1[2], oo[2], ea, pa; unsigned wmd;
    u32x4 hold[2];
    const rsrc_t ors=mkrs(ws+O_ACT+(size_t)t1*144*1024,2*144*1024);
    const int obase=((fs*9+odd)>>1)*1024;
    const int olast=((fs*9+(odd?0:8))>>1)*1024+(odd?8:0);
    auto rdW=[&](auto I){ constexpr int i=I.v, sl=slotOf(pT(i)), s=pS(i), mat=i&1;
      if constexpr(s==0){ ab[sl][mat]=(lb0^(sl*32))+mat*73728; asm volatile("" : "+v"(ab[sl][mat])); }
      if constexpr(s==9){ ab[sl][mat]+=36864; asm volatile("" : "+v"(ab[sl][mat])); }
      wf[i%PF]=cat(trread(ab[sl][mat]+(s%9)*4096),trread(ab[sl][mat]+(s%9)*4096+2048)); };
    sfor<0,PF-1>(rdW);
    auto epi=[&](auto E,auto K){ constexpr int e=E.v,kk=K.v; constexpr int ET=e>>3, t=(e>>2)&1, r=e&3;
      if constexpr(kk==0){ float gg=acc[ET&3][0][t][r], u=acc[ET&3][1][t][r]; ea=gg*c1[t]; pa=gg*u; asm volatile("" : "+v"(ea),"+v"(pa)); }
      if constexpr(kk==1){ ea=__builtin_amdgcn_exp2f(ea); asm volatile("" : "+v"(ea)); }
      if constexpr(kk==2){ ea=1.f+ea; asm volatile("" : "+v"(ea)); }
      if constexpr(kk==3){ ea=__builtin_amdgcn_rcpf(ea); asm volatile("" : "+v"(ea)); }
      if constexpr(kk==4){
        oo[r&1]=pa*ea;
        if constexpr((r&1)==0) asm volatile("" : "+v"(oo[0]));
        else {
          constexpr int hi=(ET==8||(ET&1)==0)?0:2;
          hold[t][hi+(r>>1)]=pk2(oo[0],oo[1]);
          asm volatile("" : "+v"(hold[t][hi+(r>>1)]));
          if constexpr(r==3){
            if constexpr(ET==8){ ST8(((u32x2){hold[t][0],hold[t][1]}),ors,voff,olast+t*144*1024); }
            else if constexpr((ET&1)==1){ ST16(hold[t],ors,voff,obase+t*144*1024+(ET>>1)*1024); }
          }
        }
      } };
    auto slot=[&](auto M){ constexpr int m=M.v-216-2;
      if constexpr(m>=0){ constexpr int e=(m*4+3)/27; constexpr int kk=m-(e*27)/4;
        if constexpr(e<64 && kk>=0 && kk<5) epi(IC<e>{},IC<kk>{}); } };
    sfor<0,324>([&](auto I){ constexpr int i=I.v; constexpr int T=pT(i), s=pS(i), mat=i&1, j=i-108;
      if constexpr(i==108) STAMP(14);
      if constexpr(i<108 && i%6==0){
        constexpr int iss=(s+LAK<18)?s+LAK:18;
        asm volatile("s_waitcnt vmcnt(%2)" : "+v"(af[s][0]),"+v"(af[s][1]) : "i"(2*(iss-s-1)) : "memory");
        if constexpr(i==0){ asm volatile("" : "+v"(sq[0]),"+v"(sq[1]));
          #pragma unroll
          for(int t=0;t<2;t++){ const float ss=ssq_sum(sq[t],lane); c1[t]=-1.44269504089f*__builtin_amdgcn_rsqf(ss*(1.f/576.f)+1e-5f); }
          asm volatile("" : "+v"(c1[0]),"+v"(c1[1])); } }
      if constexpr(j==2||j==74) BAR();
      if constexpr(j==31){ waitv<5>(); BAR(); }
      if constexpr(j==49||j==139){ waitv<0>(); BAR(); }
      if constexpr(j==139){ if(w==0) flag_store(ws,O_FA+r1*256+fs*8,tag); }
      if constexpr(j==204){ if(w==0) poll<8>(ws,O_FA+((xcd&1)*4+(idx>>3))*256+(xcd>>1)*64,tag,lane); }
      if constexpr(i<108 && i>0 && i%P1BAR==0) BAR();
      if constexpr(i+PF-1<324) rdW(IC<i+PF-1>{});
      SB();
      if constexpr(s==0) acc[T&3][mat][0]=MF16(wf[i%PF],af[s][0],(f32x4){}); else acc[T&3][mat][0]=MF16(wf[i%PF],af[s][0],acc[T&3][mat][0]);
      SB();
      slot(IC<2*i>{});
      SB();
      if constexpr(s==0) acc[T&3][mat][1]=MF16(wf[i%PF],af[s][1],(f32x4){}); else acc[T&3][mat][1]=MF16(wf[i%PF],af[s][1],acc[T&3][mat][1]);
      SB();
      slot(IC<2*i+1>{});
      if constexpr(j==TOUCH_D){
        unsigned o=(w*21+(lane<21?lane:20))*128; if(o>20608) o=20608; gtouch(wmd,(const char*)p.wd+(size_t)b*20736,o); }
      if constexpr(j==140) asm_use(wmd);
      if constexpr(i<108 && (i%6==1||i%6==4) && s+LAK<18) loadA(IC<s+LAK>{},IC<(i%6)/3>{});
      if constexpr(j>=2 && j<=37){ constexpr int r=(j-2)/2; glds16ms(wrow(krow(r),kstep8(r))+128,vA,wdst+r*4096,mB,(j&1)? whi^1:whi); }
      if constexpr(j>=75 && j<=127 && (j-75)%3<2){ constexpr int r=(j-75)/3; glds16ms(wrow(krow(r),kstep8(r))-32,vC,wdst+r*4096,mC,((j-75)%3)? whi^1:whi); }
      SB();
    });
    STAMP(15);
    BAR();
    g2_prologue();
    sfor<64,72>([&](auto E){ sfor<0,5>([&](auto K){ epi(E,K); }); });
  }
  STAMP(16);
  {
    using namespace g2;
    const int lane=fresh_lane(); const unsigned voff=lane*16; const int g=lane>>4, q=(lane>>2)&3, pp=lane&3, c16=lane&15;
    f32x4 acc[9][2];
    unsigned lwb[2];
    #pragma unroll
    for(int i=0;i<2;i++){ lwb[i]=rb+(4*g+q)*288+pp*8+i*36864; asm volatile("" : "+v"(lwb[i])); }
    bf16x8 wf[PF];
    #define LDW2(i) cat(trread(lwb[(((i)/9)%8)/4]+((((i)/9)%8)%4)*9216+((i)%9)*32),trread(lwb[(((i)/9)%8)/4]+((((i)/9)%8)%4)*9216+((i)%9)*32+16*288))
    waitv<0>(); BAR();
    if(w==1) flag_store(ws,O_FA2+r1*256+fs*8,tag);
    STAMP(17);
    sfor<0,PF-1>([&](auto I){ wf[I.v]=LDW2(I.v); });
    sfor<0,NP>([&](auto I){ constexpr int i=I.v; constexpr int j=i/9, nt=i%9;
      if constexpr((i+BO)%18==0 && i+BO<NP){
        if constexpr(i+BO==54){ if(w==0) poll<8>(ws,O_FA2+(r2t>>1)*256+sp*64,tag,lane); }
        waitv<vmc(i,-1,(i+BO)/9+1)>();
        BAR(); }
      if constexpr(nt==0) gwait<vmc(i,j,-1)>(a2[j%AR][0],a2[j%AR][1]);
      if constexpr(i+PF-1<NP) wf[(i+PF-1)%PF]=LDW2(i+PF-1);
      SB();
      if constexpr(j==0) acc[nt][0]=MF16(wf[i%PF],a2[j%AR][0],(f32x4){}); else acc[nt][0]=MF16(wf[i%PF],a2[j%AR][0],acc[nt][0]);
      if constexpr(j==0) acc[nt][1]=MF16(wf[i%PF],a2[j%AR][1],(f32x4){}); else acc[nt][1]=MF16(wf[i%PF],a2[j%AR][1],acc[nt][1]);
      SB();
      if constexpr(actAt(i)>=0) loadA2(IC<actAt(i)/2>{},IC<actAt(i)%2>{});
      if constexpr(wAt(i)>=0) loadW(IC<wAt(i)/3>{},IC<wAt(i)%3>{});
      SB();
    });
    STAMP(18);
    BAR();
    { char* xs=sm+(w*9)*1024+voff;
      #pragma unroll
      for(int n=0;n<9;n++){ f32x4 v= kh? acc[n][0]:acc[n][1]; *(f32x4*)(xs+n*1024)=v; } }
    LBAR();
    f32x4 part[10];
    { const char* xr=sm+((w^4)*9)*1024+voff;
      #pragma unroll
      for(int n=0;n<9;n++){ f32x4 mine= kh? acc[n][1]:acc[n][0]; part[n]=mine+*(const f32x4*)(xr+n*1024); }
      part[9]=part[8]; }
    const int grp=r2t*4+ct;
    const rsrc_t xr=mkrs(ws+O_X+(size_t)grp*160*1024,160*1024);
    if(tq!=sp){
      #pragma unroll
      for(int n=0;n<5;n++) ST16(((u32x4){pk2(part[2*n][0],part[2*n][1]),pk2(part[2*n][2],part[2*n][3]),pk2(part[2*n+1][0],part[2*n+1][1]),pk2(part[2*n+1][2],part[2*n+1][3])}),xr,voff,((tq*4+sp)*10+kh*5+n)*1024);
    } else {
      char* xs=sm+(72+kh*10)*1024+voff;
      #pragma unroll
      for(int n=0;n<10;n++) *(f32x4*)(xs+n*1024)=part[n];
    }
    publish(ws,O_FX+grp*128+sp*8,tag,w);
    STAMP(19);
    const int tk0=r2t*128+sp*32;
    const rsrc_t rh=mkrs(ws+O_C+(size_t)(tk0>>4)*18432,2*18432), rq=mkrs(ws+O_SSQ+(size_t)tk0*64,2048), rmh=mkrs(ws+O_MH+(size_t)tk0*4,128);
    u32x2 hv[2][2]; float r2[2], mh[2]; int go[2][2];
    #pragma unroll
    for(int x=0;x<2;x++){ int u=w+8*x; if(u>9) u=9; const int tl=u>=5, pr=u-5*tl;
      #pragma unroll
      for(int y=0;y<2;y++){ int nt=2*pr+y; if(nt>8) nt=8; const int nl=nt*16+g*4;
        go[x][y]=(tl*16+c16)*1152+(ct*144+nl)*2;
        const int c3=ct*2+(nl>=72), o72=nl>=72? nl-72:nl;
        int kc,gp,hfh; if(o72<64){ kc=2*c3+(o72>>5); hfh=(o72>>4)&1; gp=(o72>>2)&3; } else { kc=16+(c3>>2); gp=2*(c3&1)+((o72-64)>>2); hfh=(c3>>1)&1; }
        hv[x][y]=LD8(rh,(tl*18+kc)*1024+(gp*16+c16)*16+hfh*8,0); }
      const f32x4 s4=__builtin_bit_cast(f32x4,LD16(rq,tl*1024+voff,0));
      mh[x]=__uint_as_float(__builtin_amdgcn_raw_buffer_load_b32(rmh,(tl*16+c16)*4,0,16));
      r2[x]=__builtin_amdgcn_rcpf(ssq_sum(s4,lane)*(1.f/576.f)+1e-5f); }
    if(w==0) poll<4>(ws,O_FX+grp*128,tag,lane);
    BAR();
    STAMP(20);
    u32x4 rv[2][3];
    #pragma unroll
    for(int x=0;x<2;x++){ int u=w+8*x; if(u>9) u=9;
      #pragma unroll
      for(int y=0;y<3;y++){ int src=(sp+1+y)&3; rv[x][y]=LD16(xr,voff,((sp*4+src)*10+u)*1024); } }
    char* const ob=(char*)p.out+(size_t)(r2t*128+sp*32)*1152;
    #pragma unroll
    for(int x=0;x<2;x++){ if(x==0 || w<2){ const int u=w+8*x; const int tl=u>=5, pr=u-5*tl;
      #pragma unroll
      for(int y=0;y<2;y++){ if(y==0 || pr<4){
        f32x4 v=*(const f32x4*)(sm+(72+tl*10+2*pr+y)*1024+voff);
        #pragma unroll
        for(int z=0;z<3;z++){ v[0]+=lo16(rv[x][z][2*y]); v[1]+=hi16(rv[x][z][2*y]); v[2]+=lo16(rv[x][z][2*y+1]); v[3]+=hi16(rv[x][z][2*y+1]); }
        u32x2 ov; ov[0]=pk2(v[0]*r2[x]+(lo16(hv[x][y][0])+mh[x]),v[1]*r2[x]+(hi16(hv[x][y][0])+mh[x])); ov[1]=pk2(v[2]*r2[x]+(lo16(hv[x][y][1])+mh[x]),v[3]*r2[x]+(hi16(hv[x][y][1])+mh[x]));
        *(u32x2*)(ob+go[x][y])=ov; } } } }
  }
  STAMP(21);
  STAMP_OUT();
}
}

extern "C" void kernel_launch(void* const* d_in, const int* in_sizes, int n_in,
                              void* d_out, int out_size, void* d_ws, size_t ws_size,
                              hipStream_t stream) {
  using namespace k;
  Params p;
  p.x=(const u16*)d_in[0]; p.wqkv=(const u16*)d_in[1]; p.wo=(const u16*)d_in[2];
  p.wg=(const u16*)d_in[4]; p.wu=(const u16*)d_in[5]; p.wd=(const u16*)d_in[6];
  p.out=(u16*)d_out; p.ws=(char*)d_ws;
  fused_k<<<256,512,0,stream>>>(p);
}
```

```cpp
#include <hip/hip_runtime.h>
#include <hip/hip_bf16.h>
typedef unsigned short u16;
typedef unsigned long long u64;
typedef short s16x4 __attribute__((ext_vector_type(4)));
typedef __bf16 bf16x8 __attribute__((ext_vector_type(8)));
typedef __bf16 bf16x2 __attribute__((ext_vector_type(2)));
typedef float f32x4 __attribute__((ext_vector_type(4)));
typedef float f32x2 __attribute__((ext_vector_type(2)));
typedef unsigned u32x4 __attribute__((ext_vector_type(4)));
typedef unsigned u32x2 __attribute__((ext_vector_type(2)));
#define LAS __attribute__((address_space(3)))
#define GAS __attribute__((address_space(1)))
#define DI __device__ __forceinline__

#define WARM_SLEEP 20
#define QSPLIT 24
#define PA1 7
#define PA2 13

namespace k {
constexpr int S=2048, D=576, F=4608;
constexpr int TAG_LDS=155*1024;
DI int fresh_lane(){ int l; asm volatile("v_mbcnt_lo_u32_b32 %0, -1, 0\n\tv_mbcnt_hi_u32_b32 %0, -1, %0" : "=v"(l)); return l; }
DI u16 f2bf(float f){ __bf16 b=(__bf16)f; return __builtin_bit_cast(u16,b); }
DI unsigned pk2(float lo,float hi){ bf16x2 v; v[0]=(__bf16)lo; v[1]=(__bf16)hi; return __builtin_bit_cast(unsigned,v); }
DI float lo16(unsigned v){ return __uint_as_float(v<<16); }
DI float hi16(unsigned v){ return __uint_as_float(v&0xffff0000u); }
DI float phi(float t){ return (t>0.f? t+1.f: __builtin_amdgcn_exp2f(t*1.44269504089f))+1e-6f; }
DI float bcast_hi(float x){ unsigned a=__float_as_uint(x); auto r=__builtin_amdgcn_permlane32_swap(a,a,false,false); return __uint_as_float(r[1]); }
DI float sum_g(float x){
  unsigned a=__float_as_uint(x); auto r=__builtin_amdgcn_permlane32_swap(a,a,false,false); x=__uint_as_float(r[0])+__uint_as_float(r[1]);
  a=__float_as_uint(x); auto r2=__builtin_amdgcn_permlane16_swap(a,a,false,false); return __uint_as_float(r2[0])+__uint_as_float(r2[1]); }
DI float ssq_sum(f32x4 v,int lane){
  #pragma unroll
  for(int r=0;r<4;r++){ float x=sum_g(v[r]); x+=__shfl_xor(x,4); x+=__shfl_xor(x,8); v[r]=x; }
  float o[4];
  #pragma unroll
  for(int r=0;r<4;r++) o[r]=__shfl(v[r],(lane>>2)&3);
  const int k=lane&3; return k==0? o[0] : (k==1? o[1] : (k==2? o[2]:o[3])); }
DI float dot2(unsigned a,unsigned b,float c){ return __builtin_amdgcn_fdot2_f32_bf16(__builtin_bit_cast(bf16x2,a),__builtin_bit_cast(bf16x2,b),c,false); }
DI s16x4 trread(unsigned a){ return __builtin_amdgcn_ds_read_tr16_b64_v4i16((LAS s16x4*)(size_t)a); }
DI bf16x8 cat(s16x4 a,s16x4 b){ return __builtin_bit_cast(bf16x8,__builtin_shufflevector(a,b,0,1,2,3,4,5,6,7)); }
DI bf16x8 cat2(u32x2 a,u32x2 b){ return __builtin_bit_cast(bf16x8,(u32x4){a[0],a[1],b[0],b[1]}); }
#define BAR() do{ asm volatile("":::"memory"); __builtin_amdgcn_s_barrier(); asm volatile("":::"memory"); }while(0)
#define LBAR() do{ asm volatile("s_waitcnt lgkmcnt(0)":::"memory"); __builtin_amdgcn_s_barrier(); asm volatile("":::"memory"); }while(0)
#define MF16(a,bb,c) __builtin_amdgcn_mfma_f32_16x16x32_bf16(a,bb,c,0,0,0)
#define SB() __builtin_amdgcn_sched_barrier(0)
template<int I> struct IC{ static constexpr int v=I; };
template<int B,int E,class Fn> DI void sfor(Fn&& f){ if constexpr(B<E){ f(IC<B>{}); sfor<B+1,E>(f); } }
DI void glds16c(const void* sbase, unsigned voff, unsigned lds){
  unsigned keep;
  asm volatile("s_mov_b32 %0, m0\n\ts_mov_b32 m0, %3\n\ts_nop 0\n\tglobal_load_lds_dwordx4 %1, %2 sc1\n\ts_mov_b32 m0, %0" : "=&s"(keep) : "v"(voff), "s"(sbase), "s"(lds) : "memory");
}
DI u32x4 mkrs4(const void* p){ const u64 a=(u64)p; u32x4 r; r[0]=__builtin_amdgcn_readfirstlane((unsigned)a); r[1]=__builtin_amdgcn_readfirstlane((unsigned)(a>>32)); r[2]=0x7fffffffu; r[3]=0x00020000u; return r; }
template<int SOFF,int LOFF> DI void bl(u32x4 rs,unsigned voff,unsigned lds){ unsigned t;
  asm volatile("s_add_i32 m0, %3, %5\n\ts_mov_b32 %0, %4\n\tbuffer_load_dwordx4 %1, %2, %0 offen lds" : "=&s"(t) : "v"(voff), "s"(rs), "s"(lds), "i"(SOFF), "i"(LOFF) : "memory","scc"); }
template<int SOFF,int LOFF> DI void blm(u32x4 rs,unsigned voff,unsigned lds,u64 mask){ unsigned t;
  asm volatile("s_add_i32 m0, %3, %5\n\ts_mov_b32 %0, %4\n\ts_mov_b64 exec, %6\n\tbuffer_load_dwordx4 %1, %2, %0 offen lds\n\ts_mov_b64 exec, -1" : "=&s"(t) : "v"(voff), "s"(rs), "s"(lds), "i"(SOFF), "i"(LOFF), "s"(mask) : "memory","scc"); }
template<int SOFF,int LOFF,int EQ> DI void blq(u32x4 rs,unsigned voff,unsigned lds,int sel){ unsigned t;
  asm volatile("s_cmp_lg_u32 %6, %7\n\ts_cbranch_scc1 1f\n\ts_add_i32 m0, %3, %5\n\ts_mov_b32 %0, %4\n\tbuffer_load_dwordx4 %1, %2, %0 offen lds\n1:" : "=&s"(t) : "v"(voff), "s"(rs), "s"(lds), "i"(SOFF), "i"(LOFF), "s"(sel), "i"(EQ) : "memory","scc"); }
template<int SOFF,class T> DI void bload16c(T& r,u32x4 rs,unsigned voff){ unsigned t;
  asm volatile("s_mov_b32 %1, %4\n\tbuffer_load_dwordx4 %0, %2, %3, %1 offen sc1" : "=v"(r), "=&s"(t) : "v"(voff), "s"(rs), "i"(SOFF) : "memory"); }
template<int OFF,class T> DI void gload16(T& r,const void* sbase,unsigned voff){ asm volatile("global_load_dwordx4 %0, %1, %2 offset:%3" : "=v"(r) : "v"(voff), "s"(sbase), "i"(OFF) : "memory"); }
template<int OFF,class T> DI void gload16c(T& r,const void* sbase,unsigned voff){ asm volatile("global_load_dwordx4 %0, %1, %2 offset:%3 sc1" : "=v"(r) : "v"(voff), "s"(sbase), "i"(OFF) : "memory"); }
DI void gtouch(unsigned& r,const void* sbase,unsigned voff){ asm volatile("global_load_dword %0, %1, %2" : "=v"(r) : "v"(voff), "s"(sbase) : "memory"); }
DI void asm_use(unsigned r){ asm volatile("" :: "v"(r)); }
template<int N,class T> DI void gwait(T& a,T& b){ asm volatile("s_waitcnt vmcnt(%2)" : "+v"(a),"+v"(b) : "i"(N) : "memory"); }
template<int N> DI void waitv(){ asm volatile("s_waitcnt vmcnt(%0)" :: "i"(N) : "memory"); }
typedef __amdgpu_buffer_rsrc_t rsrc_t;
DI rsrc_t mkrs(const void* p,unsigned bytes){ return __builtin_amdgcn_make_buffer_rsrc((void*)p,0,bytes,0x00020000); }
#define ST16(v,rs,vo,so) __builtin_amdgcn_raw_buffer_store_b128(__builtin_bit_cast(u32x4,v),rs,vo,so,16)
#define ST8(v,rs,vo,so)  __builtin_amdgcn_raw_buffer_store_b64(__builtin_bit_cast(u32x2,v),rs,vo,so,16)
#define ST4(v,rs,vo,so)  __builtin_amdgcn_raw_buffer_store_b32(__float_as_uint(v),rs,vo,so,16)
#define LD16(rs,vo,so) __builtin_amdgcn_raw_buffer_load_b128(rs,vo,so,16)
#define LD8(rs,vo,so)  __builtin_amdgcn_raw_buffer_load_b64(rs,vo,so,16)

constexpr size_t O_EP=0;
constexpr size_t O_F0 =256;
constexpr size_t O_FW =O_F0+2048;
constexpr size_t O_FWO=O_FW+2048;
constexpr size_t O_FKV=O_FWO+1024;
constexpr size_t O_FO =O_FKV+16*256;
constexpr size_t O_FH =O_FO+16*256;
constexpr size_t O_FA =O_FH+8*256;
constexpr size_t O_FA2=O_FA+8*256;
constexpr size_t O_FX =O_FA2+8*256;
constexpr size_t O_KVP=32768;
constexpr size_t O_XN =O_KVP+(size_t)256*8192;
constexpr size_t O_MX =O_XN+(size_t)S*D*2;
constexpr size_t O_WB =O_MX+(size_t)S*4;
constexpr size_t O_WQ =O_WB+4096;
constexpr size_t O_WO =O_WQ+(size_t)16*144*1024;
constexpr size_t O_O  =O_WO+(size_t)8*120*1024;
constexpr size_t O_MU =O_O+(size_t)128*16*1536;
constexpr size_t O_MH =O_MU+(size_t)S*64;
constexpr size_t O_C  =O_MH+(size_t)S*4;
constexpr size_t O_SSQ=O_C+(size_t)S*D*2;
constexpr size_t O_ACT=O_SSQ+(size_t)S*64;
constexpr size_t O_X  =O_ACT+(size_t)S*F*2;
constexpr size_t O_END=O_X+(size_t)64*160*1024;
struct Params { const u16 *x,*wqkv,*wo,*wg,*wu,*wd; u16* out; char* ws; };

DI void publish(char* ws,size_t off,u64 tag,int w){
  asm volatile("s_waitcnt vmcnt(0) lgkmcnt(0)":::"memory");
  BAR();
  if(w==0 && fresh_lane()==0) __hip_atomic_store((GAS u64*)(ws+off),tag,__ATOMIC_RELAXED,__HIP_MEMORY_SCOPE_AGENT);
}
DI void flag_store(char* ws,size_t off,u64 tag){ if(fresh_lane()==0) __hip_atomic_store((GAS u64*)(ws+off),tag,__ATOMIC_RELAXED,__HIP_MEMORY_SCOPE_AGENT); }
DI void pollp(const GAS u64* p,u64 tag){
  for(unsigned spins=0; spins<(1u<<20); ++spins){
    u64 v=__hip_atomic_load(p,__ATOMIC_RELAXED,__HIP_MEMORY_SCOPE_AGENT);
    if(__all(v==tag)) break;
    __builtin_amdgcn_s_sleep(1);
  }
  __builtin_amdgcn_fence(__ATOMIC_ACQUIRE,"wavefront");
}
template<int N> DI void poll(char* ws,size_t off,u64 tag,int lane){ pollp((const GAS u64*)(ws+off)+(lane<N?lane:0),tag); }

namespace f1 {
constexpr int PF=6, XA=8, WA=6;
constexpr int wr(int r){ return r<WA? -1 : (r<12? 5*(r-WA)+3 : 53+5*(r-12)); }
constexpr int roundAt(int i){ for(int r=WA;r<18;r++) if(wr(r)==i) return r; return -1; }
constexpr int vmc(int iw,int xs,int pw){
  int cnt=0;
  for(int k=0;k<XA;k++){ if(k<WA){ if(8*k<pw) cnt=0; else cnt++; } if(k<=xs) cnt=0; else cnt++; }
  for(int i=0;i<iw;i++){
    if(i%5==1 && i/5+XA<18){ if(i/5+XA<=xs) cnt=0; else cnt++; }
    int r=roundAt(i); if(r>=0){ if(8*r<pw) cnt=0; else cnt++; }
  }
  return cnt;
}
constexpr int lpos(int p){ return p<90? p : p+12; }
}
namespace g1 {
constexpr int PF=4, LAK=5;
constexpr int pT(int i){ return i<108? (i%6)/2 : 3+(i-108)/36; }
constexpr int pS(int i){ return i<108? i/6 : ((i-108)%36)/2; }
constexpr int slotOf(int T){ return T<4? T : (T<8? T-4 : 0); }
constexpr int krow(int kc){ return kc<16? 72*(kc>>1)+32*(kc&1) : 288*(kc-16)+64; }
constexpr int kstep8(int kc){ return kc<16? 8:72; }
}
namespace g2 {
constexpr int LW=5, PF=5, BO=8, NP=162, AR=6;
constexpr int kord(int j){ constexpr int t[18]={0,1,5,6,9,10,14,15, 2,3,4,7,8,11,12,13,16,17}; return t[j]; }
constexpr int PRO=4;
constexpr int actStep(int j,int t){ return j<PRO? -1 : (j<6? 5*(j-PRO)+2+2*t : ((j>=8&&j<=10)? 50+2*(j-8)+t : (j<8? 9*(j-4)+2+3*t : 9*(j-5)+2+3*t))); }
constexpr int wStep(int j,int x){ return j<PRO? -1 : (j<8? 5*(j-PRO)+(x==2?3:x) : 9*(j-LW)+(x==0?1:(x==1?3:6))); }
constexpr int vmc(int iw,int ja,int jw){
  int best=63;
  for(int tq=0;tq<4;tq++){
    int cnt=0;
    for(int j=0;j<PRO;j++){ int n=2+((j%4)==tq); if(j<=jw) cnt=0; else cnt+=n; if(j<=ja) cnt=0; else cnt+=2; }
    for(int i=0;i<iw;i++){
      for(int j=PRO;j<18;j++) for(int t=0;t<2;t++) if(actStep(j,t)==i){ if(j<=ja) cnt=0; else cnt++; }
      for(int j=PRO;j<18;j++) for(int x=0;x<3;x++) if(wStep(j,x)==i && (x<2||(j%4)==tq)){ if(j<=jw) cnt=0; else cnt++; }
    }
    if(cnt<best) best=cnt;
  }
  return best;
}
constexpr int wAt(int i){ for(int j=PRO;j<18;j++) for(int x=0;x<3;x++) if(wStep(j,x)==i) return j*3+x; return -1; }
constexpr int actAt(int i){ for(int j=PRO;j<18;j++) for(int t=0;t<2;t++) if(actStep(j,t)==i) return j*2+t; return -1; }
}

__global__ __launch_bounds__(512,2) void fused_k(Params p){
  __shared__ __attribute__((aligned(16))) char sm[160*1024];
  const int tid=threadIdx.x, lane=tid&63, w=__builtin_amdgcn_readfirstlane(tid>>6);
  const int b=blockIdx.x, xcd=b&7, idx=b>>3;
  const unsigned smb=(unsigned)(size_t)(LAS char*)sm;
  const unsigned voff=lane*16;
  const int g=lane>>4, q=(lane>>2)&3, pp=lane&3, c16=lane&15;
  char* const ws=p.ws;
  u64 tag;
  const int hp=(xcd&1)*8+(idx>>2), rt=(xcd>>1)*4+(idx&3);
  unsigned wm[2]={0,0};
  auto touch_gu=[&](int slot,int unit,int lo){
    int ln=unit*64+lane; if(ln>323) ln=323; const int mt=ln>=162; ln-=mt*162;
    const char* src=(const char*)(mt? p.wu : p.wg)+(size_t)b*20736+ln*128;
    asm volatile("s_cmp_lt_u32 %2, %3\n\ts_cbranch_scc1 1f\n\tglobal_load_dword %0, %1, off\n1:" : "+v"(wm[slot]) : "v"(src), "s"(w), "s"(lo) : "memory","scc"); };
  {
    char* const smw=sm+w*16384;
    const unsigned smwb=smb+w*16384;
    if(w<2){
      u32x2 ep; { const unsigned z=0; asm volatile("global_load_dwordx2 %0, %1, %2 sc1" : "=v"(ep) : "v"(z), "s"(ws+(w?(size_t)O_XN:0)) : "memory"); }
      u32x4 v[5];
      { const char* xb=(const char*)p.x+(size_t)(b*8+w*4)*1152; const unsigned v0=g*1152+c16*16, v4=g*1152+(64+(c16&7))*16;
        gload16<0>(v[0],xb,v0); gload16<256>(v[1],xb,v0); gload16<512>(v[2],xb,v0); gload16<768>(v[3],xb,v0); gload16<0>(v[4],xb,v4); }
      asm volatile("s_waitcnt vmcnt(0)" : "+v"(ep),"+v"(v[0]),"+v"(v[1]),"+v"(v[2]),"+v"(v[3]),"+v"(v[4]) :: "memory");
      if(tid==0) *(u32x2*)(sm+TAG_LDS)=ep;
      float s1=0;
      #pragma unroll
      for(int i=0;i<5;i++){ const unsigned one=(i<4||c16<8)?0x3f803f80u:0u;
        #pragma unroll
        for(int j=0;j<4;j++) s1=dot2(v[i][j],one,s1); }
      s1+=__shfl_xor(s1,1); s1+=__shfl_xor(s1,2); s1+=__shfl_xor(s1,4); s1+=__shfl_xor(s1,8);
      const float m=s1*(1.f/576.f);
      float f[5][8]; float s2=0,s2t=0;
      #pragma unroll
      for(int i=0;i<5;i++)
        #pragma unroll
        for(int j=0;j<4;j++){ f[i][2*j]=lo16(v[i][j])-m; f[i][2*j+1]=hi16(v[i][j])-m;
          if(i<4){ s2=fmaf(f[i][2*j],f[i][2*j],s2); s2=fmaf(f[i][2*j+1],f[i][2*j+1],s2); } else { s2t=fmaf(f[i][2*j],f[i][2*j],s2t); s2t=fmaf(f[i][2*j+1],f[i][2*j+1],s2t); } }
      s2+= c16<8? s2t:0.f;
      s2+=__shfl_xor(s2,1); s2+=__shfl_xor(s2,2); s2+=__shfl_xor(s2,4); s2+=__shfl_xor(s2,8);
      const float r=__builtin_amdgcn_rsqf(s2*(1.f/576.f)+1e-5f);
      #pragma unroll
      for(int i=0;i<5;i++){ u32x4 o;
        #pragma unroll
        for(int j=0;j<4;j++) o[j]=pk2(f[i][2*j]*r,f[i][2*j+1]*r);
        *(u32x4*)(smw+(i<4? c16+16*i: 64+(c16&7))*80+g*16)=o; }
      asm volatile("s_waitcnt lgkmcnt(0)":::"memory");
      const rsrc_t rx=mkrs(ws+O_XN+(size_t)(b>>1)*18432+(b&1)*128+w*64,18432);
      #pragma unroll
      for(int i=0;i<5;i++){ const int ch=(lane>>2)+16*i; if(ch<72) ST16(*(const u32x4*)(smw+ch*80+pp*16),rx,ch*256+pp*16,0); }
      const rsrc_t rm=mkrs(ws+O_MX+(size_t)b*32+w*16,16);
      if(c16==0) ST4(m,rm,g*4,0);
    } else if(w<4){
      if(b<144){
        const int s=b>>3, hq=b&7, h2=w-2;
        u32x4 v[8];
        const char* src=(const char*)p.wqkv+(size_t)s*32*3456+hq*144+h2*64;
        int lo_[8];
        #pragma unroll
        for(int i=0;i<8;i++){ const int r=g+4*i; int c=c16<15?c16:14; const int part=c/5, cc=c-part*5; lo_[i]=r*272+c*16; gload16<0>(v[i],src,r*3456+part*1152+cc*16); }
        asm volatile("s_waitcnt vmcnt(0)" : "+v"(v[0]),"+v"(v[1]),"+v"(v[2]),"+v"(v[3]),"+v"(v[4]),"+v"(v[5]),"+v"(v[6]),"+v"(v[7]) :: "memory");
        #pragma unroll
        for(int i=0;i<8;i++) *(u32x4*)(smw+lo_[i])=v[i];
        asm volatile("s_waitcnt lgkmcnt(0)":::"memory");
        const rsrc_t rw=mkrs(ws+O_WQ+(size_t)(hq*2+h2)*144*1024,144*1024);
        #pragma unroll
        for(int t=0;t<8;t++){
          const int vc=(t<3? 16*t:16*(t-3))+4*pp;
          const int col= t<3? (vc<36? vc:0) : (vc<36? 40+vc : (vc<72? 80+vc-36 : 0));
          const unsigned ba=smwb+(8*g+q)*272+(h2*4+col)*2;
          u32x4 o=__builtin_bit_cast(u32x4,cat(trread(ba),trread(ba+4*272)));
          if((t<3? 16*t:16*(t-3))+c16>=(t<3?36:72)) o=(u32x4){0,0,0,0};
          ST16(o,rw,voff,(t<3? 90+s*3+t : s*5+t-3)*1024); }
      } else if(b<240){
        const int jb=b-144, kap=jb>>2, ct=(jb&3)*2+(w-2);
        const int kq=kap/6, kl=kap%6;
        u32x4 v[5];
        const char* src=(const char*)p.wo+ct*144;
        bool okr[5]; int lo_[5];
        #pragma unroll
        for(int i=0;i<5;i++){ int e=lane+64*i; if(e>287) e=287; const int r=e/9, c=e-r*9, row=r&15, hi=r>>4;
          int kr; if(kl<4){ kr=(4*kq+kl)*36+(hi? 12+row: row-4); okr[i]= hi|| row>=4; } else { kr=(4*kq+2*(kl-4)+hi)*36+28+row; okr[i]=row<8; }
          lo_[i]=r*160+c*16; gload16<0>(v[i],src,(okr[i]?kr:0)*1152+c*16); }
        asm volatile("s_waitcnt vmcnt(0)" : "+v"(v[0]),"+v"(v[1]),"+v"(v[2]),"+v"(v[3]),"+v"(v[4]) :: "memory");
        #pragma unroll
        for(int i=0;i<5;i++) *(u32x4*)(smw+lo_[i])= okr[i]? v[i]:(u32x4){0,0,0,0};
        asm volatile("s_waitcnt lgkmcnt(0)":::"memory");
        const rsrc_t rw=mkrs(ws+O_WO+(size_t)ct*120*1024,120*1024);
        #pragma unroll
        for(int nt=0;nt<5;nt++){
          const int col=nt*16+4*((nt==4)?(pp&1):pp);
          const unsigned ba=smwb+(4*g+q)*160+col*2;
          u32x4 o=__builtin_bit_cast(u32x4,cat(trread(ba),trread(ba+16*160)));
          if(nt==4 && c16>=8) o=(u32x4){ (kl<4 && g==0 && (c16&3)==0)? 0x3f803f80u:0u ,0u,0u,0u};
          ST16(o,rw,voff,(kap*5+nt)*1024); }
      }
    } else if(w==4 && rt<9){
      u32x4 v[5];
      const char* rp=(const char*)p.wo+(size_t)(hp*36+rt*4)*1152;
      const unsigned v0=g*1152+c16*16, v4=g*1152+(64+(c16&7))*16;
      gload16<0>(v[0],rp,v0); gload16<256>(v[1],rp,v0); gload16<512>(v[2],rp,v0); gload16<768>(v[3],rp,v0); gload16<0>(v[4],rp,v4);
      asm volatile("s_waitcnt vmcnt(0)" : "+v"(v[0]),"+v"(v[1]),"+v"(v[2]),"+v"(v[3]),"+v"(v[4]) :: "memory");
      float sacc=0;
      #pragma unroll
      for(int i=0;i<5;i++){ const unsigned one=(i<4||c16<8)?0x3f803f80u:0u;
        #pragma unroll
        for(int j=0;j<4;j++) sacc=dot2(v[i][j],one,sacc); }
      sacc+=__shfl_xor(sacc,1); sacc+=__shfl_xor(sacc,2); sacc+=__shfl_xor(sacc,4); sacc+=__shfl_xor(sacc,8);
      if(c16==0) ST4(sacc*(1.f/576.f),mkrs(ws+O_WB+(size_t)(hp*36+rt*4)*4,16),g*4,0);
    }
    if(w>=5){ __builtin_amdgcn_s_sleep(WARM_SLEEP); touch_gu(1,w==5?4:5,5); }
    if(w<5) asm volatile("s_waitcnt vmcnt(0) lgkmcnt(0)":::"memory");
    BAR();
    { const u32x2 e=*(const u32x2*)(sm+TAG_LDS); unsigned lo=__builtin_amdgcn_readfirstlane(e[0]), hi=__builtin_amdgcn_readfirstlane(e[1]); tag=(((u64)hi<<32)|lo)+1ull; }
    if(tid<2 && (tid==0 || b<240)){
      const size_t fo= tid==0? O_F0+b*8 : (b<144? O_FW+(b&7)*256+(b>>3)*8 : O_FWO+((b-144)&3)*256+((b-144)>>2)*8);
      __hip_atomic_store((GAS u64*)(ws+fo),tag,__ATOMIC_RELAXED,__HIP_MEMORY_SCOPE_AGENT); }
    if(w==0) pollp((const GAS u64*)(ws+(lane<16? O_F0+rt*128+lane*8 : (lane<34? O_FW+(hp>>1)*256+(lane-16)*8 : O_FWO+((idx&7)>>1)*256+(lane<58?lane-34:0)*8))),tag);
    BAR();
  }
  const int tt=rt*8+w;
  const int ct3=idx&7, r3=xcd*4+(idx>>3);
  constexpr int WOL=8*1024;
  bf16x8 q01; u32x2 q2;
  bool okv[2][4]; bool anyok[2]={false,false};
  {
    using namespace f1;
    const char* wbase=ws+O_WQ+(size_t)hp*144*1024+w*1024;
    const char* abase=ws+O_XN+(size_t)tt*18*1024;
    bf16x8 af[18];
    auto ldW=[&](auto R){ constexpr int r=R.v; const int pc=r*8+w; glds16c(wbase+r*8192,voff,smb+((r<11||(r==11&&0))? pc : (pc<90? pc:pc+12))*1024); };
    auto ldX=[&](auto Sx){ constexpr int s=Sx.v; gload16c<(s&3)*1024>(af[s],abase+(s>>2)*4096,voff); };
    sfor<0,XA>([&](auto K){ if constexpr(K.v<WA) ldW(K); ldX(K); });
    f32x4 acc[8];
    unsigned lwb[4];
    #pragma unroll
    for(int i=0;i<4;i++){ lwb[i]=smb+voff+i*49152; asm volatile("" : "+v"(lwb[i])); }
    bf16x8 wf[PF];
    #define LDW1(i) (*(const LAS bf16x8*)((const LAS char*)(size_t)lwb[lpos(i)/48]+(lpos(i)%48)*1024))
    #define WAITP(m,n) asm volatile("s_waitcnt vmcnt(%2)" : "+v"(af[2*(m)]),"+v"(af[2*(m)+1]) : "i"(n) : "memory")
    WAITP(0,vmc(0,1,10)); BAR();
    sfor<0,PF-1>([&](auto I){ wf[I.v]=LDW1(I.v); });
    sfor<0,90>([&](auto I){ constexpr int i=I.v; constexpr int s=i/5,t=i%5;
      if constexpr(i%10==4 && i/10<8){ constexpr int m=i/10+1; WAITP(m,vmc(i,2*m+1,10*(m+1))); BAR(); }
      if constexpr(i+PF-1<90) wf[(i+PF-1)%PF]=LDW1(i+PF-1);
      SB();
      if constexpr(s==0) acc[3+t]=MF16(af[s],wf[i%PF],(f32x4){}); else acc[3+t]=MF16(af[s],wf[i%PF],acc[3+t]);
      SB();
      if constexpr(t==1 && s+XA<18) ldX(IC<s+XA>{});
      if constexpr(roundAt(i)>=0) ldW(IC<roundAt(i)>{});
      SB();
    });
    u32x2 kvb[5];
    #pragma unroll
    for(int t=0;t<5;t++){ float x[4];
      #pragma unroll
      for(int r4=0;r4<4;r4++){ float a=acc[3+t][r4];
        if(t<2) a=phi(a); else if(t==2) a= c16<4? phi(a):a; else if(t==4) a= c16<8? a : 1.f;
        x[r4]=a; }
      kvb[t][0]=pk2(x[0],x[1]); kvb[t][1]=pk2(x[2],x[3]); }
    BAR();
    { char* xs=sm+w*8192+voff;
      #define KVM(idx,kt,vt) *(f32x4*)(xs+(idx)*1024)=__builtin_amdgcn_mfma_f32_16x16x16bf16_1k(__builtin_bit_cast(s16x4,kvb[kt]),__builtin_bit_cast(s16x4,kvb[vt]),(f32x4){},0,0,0)
      KVM(0,0,2); KVM(1,1,2); KVM(2,0,3); KVM(3,1,3); KVM(4,2,3); KVM(5,0,4); KVM(6,1,4); KVM(7,2,4); }
    waitv<0>();
    LBAR();
    touch_gu(0,w-4,4);
    #pragma unroll
    for(int x=0;x<2;x++){ const int tl=(w&3)+4*x;
      const int kt=(tl==0||tl==2||tl==5)?0:((tl==1||tl==3||tl==6)?1:2), vt= tl<2?2:(tl<5?3:4);
      #pragma unroll
      for(int r4=0;r4<4;r4++){ int d=16*kt+4*g+r4, v=16*vt+c16;
        bool ok; if(v<72) ok= v>=36 && d<36 && ((d<18)==(v-36<18)); else ok=(c16==8||c16==12)? d<18 : ((c16==9||c16==13)? (d>=18&&d<36):false);
        okv[x][r4]=ok; anyok[x]|=ok; } }
    if(w<4){
      const rsrc_t rkv=mkrs(ws+O_KVP+(size_t)hp*16*8192,16*8192);
      #pragma unroll
      for(int x=0;x<2;x++){ const int tl=w+4*x;
        f32x4 sacc=*(const f32x4*)(sm+tl*1024+voff);
        #pragma unroll
        for(int ww=1;ww<8;ww++) sacc+=*(const f32x4*)(sm+ww*8192+tl*1024+voff);
        if(anyok[x]) ST16(sacc,rkv,voff,rt*8192+tl*1024); }
    }
    auto qsteps=[&](auto B,auto E){ constexpr int b0=B.v,e0=E.v;
      sfor<b0,(b0+PF-1<e0?b0+PF-1:e0)>([&](auto I){ wf[I.v%PF]=LDW1(I.v); });
      sfor<b0,e0>([&](auto I){ constexpr int i=I.v; constexpr int s=(i-90)/3,t=(i-90)%3;
        if constexpr(i+PF-1<e0) wf[(i+PF-1)%PF]=LDW1(i+PF-1);
        SB();
        if constexpr(s==0) acc[t]=MF16(wf[i%PF],af[s],(f32x4){}); else acc[t]=MF16(wf[i%PF],af[s],acc[t]);
        SB(); }); };
    qsteps(IC<90>{},IC<90+QSPLIT>{});
    if(w<4) waitv<0>();
    BAR();
    if(tid==0) __hip_atomic_store((GAS u64*)(ws+O_FKV+hp*256+rt*8),tag,__ATOMIC_RELAXED,__HIP_MEMORY_SCOPE_AGENT);
    if(w>=4){
      const char* wob=ws+O_WO+(size_t)ct3*120*1024;
      #pragma unroll
      for(int i=0;i<24;i++){ int pc=(w-4)+4*i; if(pc>93) pc=93; glds16c(wob+pc*1024,voff,smb+WOL+pc*1024); } }
    qsteps(IC<90+QSPLIT>{},IC<144>{});
    { u32x4 v; v[0]=pk2(phi(acc[0][0]),phi(acc[0][1])); v[1]=pk2(phi(acc[0][2]),phi(acc[0][3])); v[2]=pk2(phi(acc[1][0]),phi(acc[1][1])); v[3]=pk2(phi(acc[1][2]),phi(acc[1][3]));
      q01=__builtin_bit_cast(bf16x8,v);
      q2[0]=pk2(phi(acc[2][0]),phi(acc[2][1])); q2[1]=pk2(phi(acc[2][2]),phi(acc[2][3])); }
  }
  if(w==0) poll<16>(ws,O_FKV+hp*256,tag,lane);
  BAR();
  if(w<4){
    u32x4 pv[2][16]; u32x4 wb4;
    { const char* pb=ws+O_KVP+(size_t)hp*16*8192+w*1024;
      #pragma unroll
      for(int x=0;x<2;x++){ const unsigned vo=(anyok[x]?voff:0u)+x*4096;
        #pragma unroll
        for(int i=0;i<16;i++) gload16c<0>(pv[x][i],pb+i*8192,vo); }
      gload16c<0>(wb4,ws+O_WB+hp*144,(lane<9?lane:0)*16); }
    #pragma unroll
    for(int x=0;x<2;x++){
      if(x==0) waitv<17>(); else waitv<0>();
      asm volatile("" : "+v"(pv[x][0]),"+v"(pv[x][1]),"+v"(pv[x][2]),"+v"(pv[x][3]),"+v"(pv[x][4]),"+v"(pv[x][5]),"+v"(pv[x][6]),"+v"(pv[x][7]),
         "+v"(pv[x][8]),"+v"(pv[x][9]),"+v"(pv[x][10]),"+v"(pv[x][11]),"+v"(pv[x][12]),"+v"(pv[x][13]),"+v"(pv[x][14]),"+v"(pv[x][15]) :: "memory");
      f32x4 sacc=__builtin_bit_cast(f32x4,pv[x][0]);
      #pragma unroll
      for(int i=1;i<16;i++) sacc+=__builtin_bit_cast(f32x4,pv[x][i]);
      u32x2 pk; pk[0]=pk2(okv[x][0]?sacc[0]:0.f,okv[x][1]?sacc[1]:0.f); pk[1]=pk2(okv[x][2]?sacc[2]:0.f,okv[x][3]?sacc[3]:0.f);
      *(u32x2*)(sm+(w+4*x)*512+lane*8)=pk; }
    asm volatile("" : "+v"(wb4));
    if(w==0 && lane<9) *(u32x4*)(sm+4096+lane*16)=wb4;
  } else {
    __builtin_amdgcn_s_sleep(8);
    const char* wob=ws+O_WO+(size_t)ct3*120*1024;
    #pragma unroll
    for(int i=0;i<7;i++){ int pc=94+(w-4)+4*i; if(pc>119) pc=119; glds16c(wob+pc*1024,voff,smb+WOL+pc*1024); }
  }
  LBAR();
  { const char* kk=sm+lane*8; const u32x2 z2={0,0};
    #define KF(i) (*(const u32x2*)(kk+(i)*512))
    f32x4 A=MF16(cat2(KF(0),KF(1)),q01,(f32x4){});
    f32x4 B=MF16(cat2(KF(2),KF(3)),q01,(f32x4){}); B=MF16(cat2(KF(4),z2),cat2(q2,z2),B);
    f32x4 C=MF16(cat2(KF(5),KF(6)),q01,(f32x4){}); C=MF16(cat2(KF(7),z2),cat2(q2,z2),C);
    const float ra=__builtin_amdgcn_rcpf(bcast_hi(C[0])+1e-6f), rb=__builtin_amdgcn_rcpf(bcast_hi(C[1])+1e-6f);
    const float b01= g<2? ra:rb, b23= g<1? ra:rb, cc= g<2? rb:0.f;
    A*=ra; B[0]*=b01; B[1]*=b01; B[2]*=b23; B[3]*=b23; C*=cc;
    const char* wbl=sm+4096;
    const f32x4 t4=A*(*(const f32x4*)(wbl+(g>0?g-1:0)*16))+B*(*(const f32x4*)(wbl+48+g*16))+C*(*(const f32x4*)(wbl+112+(g<2?g:0)*16));
    const float mu=sum_g((t4[0]+t4[1])+(t4[2]+t4[3]));
    const float muh=lo16((unsigned)f2bf(mu));
    if(g==0){ A[0]=muh; A[1]=mu-muh; }
    const rsrc_t ro=mkrs(ws+O_O+((size_t)tt*16+hp)*1536,1536);
    ST16(((u32x4){pk2(A[0],A[1]),pk2(A[2],A[3]),pk2(B[0],B[1]),pk2(B[2],B[3])}),ro,voff,0);
    ST8(((u32x2){pk2(C[0],C[1]),pk2(C[2],C[3])}),ro,lane*8,1024);
  }
  publish(ws,O_FO+rt*256+hp*8,tag,w);
  asm volatile("" :: "v"(wm[0]),"v"(wm[1]));
  const int r1=(xcd&1)*4+(idx&3), fs=(xcd>>1)*8+(idx>>2);
  const int odd=fs&1;
  const char* wsrc=(const char*)((w>>2)? p.wu:p.wg)+(size_t)(fs*9+odd)*32;
  const unsigned wdst=smb+(w>>2)*73728+(w&3)*1024;
  unsigned vA,vC; u64 mB,mC;
  { const int r8=lane>>3, pos=lane&7, sl=(pos>>1)^((r8>>1)&3);
    vA=r8*(F*2)+sl*32+(pos&1)*16;
    vC=r8*(F*2)+(sl==3? 256:(odd?0:288))+(pos&1)*16;
    mB=__ballot(sl<3); mC=__ballot(sl==3||sl==0); }
  const int w3=w&3;
  const u32x4 rwA=mkrs4(wsrc-32+(size_t)w3*8*(F*2)), rwB=mkrs4(wsrc-32+(size_t)w3*72*(F*2));
  auto ldG1=[&](auto R){ constexpr int r=R.v; bl<g1::krow(r)*(F*2)+32,r*4096>(r<16?rwA:rwB,vA,wdst); };
  {
    const int th=w&1, kq=w>>1;
    const int tq=th*2+(kq>>1), hf=kq&1;
    const int t3=r3*4+tq;
    constexpr int XS=116*1024;
    bf16x8 wpre[30];
    { const char* rb=sm+WOL+kq*30720+voff;
      #pragma unroll
      for(int i=0;i<30;i++) wpre[i]=*(const bf16x8*)(rb+i*1024); }
    u32x2 xv[3]; unsigned mxu;
    { const char* xb=(const char*)p.x+(size_t)t3*16*1152+ct3*144;
      #pragma unroll
      for(int i=0;i<3;i++){ int nt= hf? 2+i : (i<2? i:1); int n=nt*16+4*g; if(n>=72) n=68;
        asm volatile("global_load_dwordx2 %0, %1, %2" : "=v"(xv[i]) : "v"(c16*1152+n*2), "s"(xb) : "memory"); } }
    LBAR();
    #pragma unroll
    for(int i=0;i<30;i++) asm volatile("" : "+v"(wpre[i]));
    if(w==7){
      poll<16>(ws,O_FO+(r3>>1)*256,tag,lane);
    } else {
      sfor<0,PA1>(ldG1);
    }
    BAR();
    u32x4 oab[2][4]; u32x2 oc[2][4];
    { const char* ob=ws+O_O+((size_t)(r3*4+th*2)*16+kq*4)*1536;
      #pragma unroll
      for(int i=0;i<4;i++){ gload16c<0>(oab[0][i],ob+i*1536,voff); gload16c<0>(oab[1][i],ob+24576+i*1536,voff); }
      #pragma unroll
      for(int i=0;i<4;i++){ asm volatile("global_load_dwordx2 %0, %1, %2 offset:1024 sc1" : "=v"(oc[0][i]) : "v"(lane*8), "s"(ob+i*1536) : "memory");
                            asm volatile("global_load_dwordx2 %0, %1, %2 offset:1024 sc1" : "=v"(oc[1][i]) : "v"(lane*8), "s"(ob+24576+i*1536) : "memory"); }
      asm volatile("global_load_dword %0, %1, %2 sc1" : "=v"(mxu) : "v"(c16*4), "s"(ws+O_MX+(size_t)t3*64) : "memory"); }
    f32x4 acc[2][5]={};
    sfor<0,4>([&](auto KL){ constexpr int kl=KL.v;
      gwait<15-2*kl>(oab[0][kl],oab[1][kl]);
      #pragma unroll
      for(int t=0;t<2;t++)
        #pragma unroll
        for(int n=0;n<5;n++) acc[t][n]=MF16(wpre[kl*5+n],__builtin_bit_cast(bf16x8,oab[t][kl]),acc[t][n]); });
    asm volatile("s_waitcnt vmcnt(0)" : "+v"(oc[0][0]),"+v"(oc[0][1]),"+v"(oc[0][2]),"+v"(oc[0][3]),"+v"(oc[1][0]),"+v"(oc[1][1]),"+v"(oc[1][2]),"+v"(oc[1][3]),"+v"(mxu),"+v"(xv[0]),"+v"(xv[1]),"+v"(xv[2]) :: "memory");
    #pragma unroll
    for(int kl=0;kl<2;kl++)
      #pragma unroll
      for(int t=0;t<2;t++)
        #pragma unroll
        for(int n=0;n<5;n++) acc[t][n]=MF16(wpre[(4+kl)*5+n],cat2(oc[t][2*kl],oc[t][2*kl+1]),acc[t][n]);
    f32x4 a[5];
    { char* xs=sm+XS+w*5120+voff; const bool hi=kq>>1;
      #pragma unroll
      for(int n=0;n<5;n++){ f32x4 u0=acc[0][n],u1=acc[1][n]; asm volatile("" : "+v"(u0),"+v"(u1)); a[n]=hi?u1:u0; *(f32x4*)(xs+n*1024)=hi?u0:u1; } }
    LBAR();
    { const char* xr=sm+XS+(w^4)*5120+voff;
      #pragma unroll
      for(int n=0;n<5;n++) a[n]+=*(const f32x4*)(xr+n*1024); }
    #pragma unroll
    for(int n=0;n<5;n++) asm volatile("" : "+v"(a[n]));
    f32x4 keep[3];
    { char* xs=sm+XS+(w^4)*5120+voff;
      keep[0]=hf?a[2]:a[0]; keep[1]=hf?a[3]:a[1]; keep[2]=a[4];
      *(f32x4*)(xs)=hf?a[0]:a[2]; *(f32x4*)(xs+1024)=hf?a[1]:a[3]; *(f32x4*)(xs+2048)=a[4]; }
    LBAR();
    { const char* xr=sm+XS+(w^6)*5120+voff;
      const rsrc_t rc=mkrs(ws+O_C+(size_t)t3*18*1024,18*1024), rq=mkrs(ws+O_SSQ+(size_t)t3*1024,1024);
      #pragma unroll
      for(int i=0;i<3;i++) keep[i]+=*(const f32x4*)(xr+i*1024);
      const float m=__uint_as_float(mxu)+bcast_hi(keep[2][0]);
      float qs=0; unsigned cp[6];
      #pragma unroll
      for(int i=0;i<3;i++){
        const f32x4 v=keep[i];
        const float c0=(lo16(xv[i][0])+v[0])-m,c1=(hi16(xv[i][0])+v[1])-m,c2=(lo16(xv[i][1])+v[2])-m,c3=(hi16(xv[i][1])+v[3])-m;
        const float qq=(c0*c0+c1*c1)+(c2*c2+c3*c3);
        qs+= (i<2 || (hf && g<2))? qq:0.f;
        cp[2*i]=pk2(c0,c1); cp[2*i+1]=pk2(c2,c3); }
      ST16(((u32x4){cp[0],cp[1],cp[2],cp[3]}),rc,voff,(2*ct3+hf)*1024);
      if(hf && g<2) ST8(((u32x2){cp[4],cp[5]}),rc,((2*(ct3&1)+g)*16+c16)*16+((ct3>>1)&1)*8,(16+(ct3>>2))*1024);
      qs=sum_g(qs);
      if(lane<16){ ST4(qs,rq,lane*4,(ct3*2+hf)*64); if(ct3==0 && hf==0) ST4(m,mkrs(ws+O_MH+(size_t)t3*64,64),lane*4,0); }
    }
    publish(ws,O_FH+(r3>>2)*256+((r3&3)*8+ct3)*8,tag,w);
    if(b==0 && tid==64) __hip_atomic_store((GAS u64*)(ws+O_EP),tag,__ATOMIC_RELAXED,__HIP_MEMORY_SCOPE_AGENT);
  }
  const int sp=xcd>>1, r2t=(xcd&1)*8+(idx>>2), ct=idx&3;
  const int tq=w&3, kh=w>>2;
  const unsigned rb=smb+kh*73728;
  const u32x4 rw2=mkrs4((const char*)p.wd+(size_t)(sp*36+kh*18)*32*1152+ct*288);
  const u32x4 ra2=mkrs4(ws+O_ACT+((size_t)(r2t*8+tq*2)*144+sp*36+kh*18)*1024);
  const unsigned rbt=rb+tq*1024;
  unsigned wv[3], voff2;
  bf16x8 a2[g2::AR][2];
  auto loadW=[&](auto J,auto X_){ constexpr int j=J.v,x=X_.v;
    if constexpr(x<2) bl<g2::kord(j)*32*1152,(j%8)*9216+4096*x>(rw2,wv[x],rbt);
    else blq<g2::kord(j)*32*1152,(j%8)*9216+8192,j%4>(rw2,wv[2],rb,tq); };
  auto loadA2=[&](auto J,auto T){ constexpr int j=J.v,t=T.v; bload16c<t*144*1024+g2::kord(j)*1024>(a2[j%g2::AR][t],ra2,voff2); };
  auto g2_prologue=[&](){
    const int l2=fresh_lane(); voff2=l2*16;
    #pragma unroll
    for(int i=0;i<3;i++){ const int sub= i<2? tq+4*i:8; const int id=sub*64+l2; const int row=id/18; wv[i]=row*1152+(id-row*18)*16; }
    sfor<0,g2::PRO>([&](auto J){ loadW(J,IC<0>{}); loadW(J,IC<1>{}); loadW(J,IC<2>{}); loadA2(J,IC<0>{}); loadA2(J,IC<1>{}); }); };
  {
    using namespace g1;
    const int t1=r1*16+w*2;
    const char* abase=ws+O_C+(size_t)t1*18*1024;
    if(w==7){ sfor<0,PA1>(ldG1); }
    if(w==0) poll<32>(ws,O_FH+r1*256,tag,lane);
    else sfor<PA1,PA2>(ldG1);
    if(w==7) waitv<PA2-PA1>();
    BAR();
    bf16x8 af[18][2];
    f32x4 sq[2];
    { const char* sp=ws+O_SSQ+(size_t)t1*1024; gload16c<0>(sq[0],sp,voff); gload16c<1024>(sq[1],sp,voff); }
    auto loadA=[&](auto Sx,auto T){ constexpr int s=Sx.v,t=T.v; gload16c<(s&3)*1024>(af[s][t], abase+t*18*1024+(s>>2)*4096, voff); };
    sfor<0,LAK>([&](auto Sx){ loadA(Sx,IC<0>{}); loadA(Sx,IC<1>{}); });
    if(w==0) sfor<PA1,PA2>(ldG1);
    f32x4 acc[4][2][2];
    const unsigned lb0=smb+(4*g+q)*128+((((g&1)<<1)|(q>>1))*32)+pp*8;
    unsigned ab[4][2];
    bf16x8 wf[PF];
    float c1[2]; unsigned wmd;
    u32x4 hold[2];
    const rsrc_t ors=mkrs(ws+O_ACT+(size_t)t1*144*1024,2*144*1024);
    const int obase=((fs*9+odd)>>1)*1024;
    const int olast=((fs*9+(odd?0:8))>>1)*1024+(odd?8:0);
    auto rdW=[&](auto I){ constexpr int i=I.v, sl=slotOf(pT(i)), s=pS(i), mat=i&1;
      if constexpr(s==0){ ab[sl][mat]=(lb0^(sl*32))+mat*73728; asm volatile("" : "+v"(ab[sl][mat])); }
      if constexpr(s==9){ ab[sl][mat]+=36864; asm volatile("" : "+v"(ab[sl][mat])); }
      wf[i%PF]=cat(trread(ab[sl][mat]+(s%9)*4096),trread(ab[sl][mat]+(s%9)*4096+2048)); };
    sfor<0,PF-1>(rdW);
    f32x2 e2,p2;
    auto epi=[&](auto E,auto K){ constexpr int e=E.v,kk=K.v; constexpr int ET=e>>2, t=(e>>1)&1, h=e&1;
      if constexpr(kk==0){ const f32x2 gg={acc[ET&3][0][t][2*h],acc[ET&3][0][t][2*h+1]}, u={acc[ET&3][1][t][2*h],acc[ET&3][1][t][2*h+1]};
        e2=gg*c1[t]; p2=gg*u; asm volatile("" : "+v"(e2),"+v"(p2)); }
      if constexpr(kk==1){ e2[0]=__builtin_amdgcn_exp2f(e2[0]); asm volatile("" : "+v"(e2)); }
      if constexpr(kk==2){ e2[1]=__builtin_amdgcn_exp2f(e2[1]); asm volatile("" : "+v"(e2)); }
      if constexpr(kk==3){ e2=e2+1.f; asm volatile("" : "+v"(e2)); }
      if constexpr(kk==4){ e2[0]=__builtin_amdgcn_rcpf(e2[0]); asm volatile("" : "+v"(e2)); }
      if constexpr(kk==5){ e2[1]=__builtin_amdgcn_rcpf(e2[1]); asm volatile("" : "+v"(e2)); }
      if constexpr(kk==6){
        const f32x2 o=p2*e2;
        constexpr int hi=(ET==8||(ET&1)==0)?0:2;
        hold[t][hi+h]=pk2(o[0],o[1]);
        asm volatile("" : "+v"(hold[t][hi+h]));
        if constexpr(h==1){
          if constexpr(ET==8){ ST8(((u32x2){hold[t][0],hold[t][1]}),ors,voff,olast+t*144*1024); }
          else if constexpr((ET&1)==1){ ST16(hold[t],ors,voff,obase+t*144*1024+(ET>>1)*1024); }
        }
      } };
    auto slot=[&](auto M){ constexpr int m=M.v-216-2;
      if constexpr(m>=0){ constexpr int e=(m*2+1)/27; constexpr int d=m-(e*27)/2;
        if constexpr(e<32 && d>=0 && d<14 && d<7) epi(IC<e>{},IC<d>{}); } };
    sfor<0,324>([&](auto I){ constexpr int i=I.v; constexpr int T=pT(i), s=pS(i), mat=i&1, j=i-108;
      if constexpr(i<108 && i%6==0){
        constexpr int iss=(s+LAK<18)?s+LAK:18;
        asm volatile("s_waitcnt vmcnt(%2)" : "+v"(af[s][0]),"+v"(af[s][1]) : "i"(2*(iss-s-1)) : "memory");
        if constexpr(i==0){ asm volatile("" : "+v"(sq[0]),"+v"(sq[1]));
          #pragma unroll
          for(int t=0;t<2;t++){ const float ss=ssq_sum(sq[t],lane); c1[t]=-1.44269504089f*__builtin_amdgcn_rsqf(ss*(1.f/576.f)+1e-5f); }
          asm volatile("" : "+v"(c1[0]),"+v"(c1[1])); } }
      if constexpr(j==2||j==74) BAR();
      if constexpr(j==31){ waitv<5>(); BAR(); }
      if constexpr(j==49||j==139){ waitv<0>(); BAR(); }
      if constexpr(j==139){ if(w==0) flag_store(ws,O_FA+r1*256+fs*8,tag); }
      if constexpr(j==204){ if(w==0) poll<8>(ws,O_FA+((xcd&1)*4+(idx>>3))*256+(xcd>>1)*64,tag,lane); }
      if constexpr(i==36||i==72) BAR();
      if constexpr(i+PF-1<324) rdW(IC<i+PF-1>{});
      SB();
      if constexpr(s==0) acc[T&3][mat][0]=MF16(wf[i%PF],af[s][0],(f32x4){}); else acc[T&3][mat][0]=MF16(wf[i%PF],af[s][0],acc[T&3][mat][0]);
      SB();
      slot(IC<2*i>{});
      SB();
      if constexpr(s==0) acc[T&3][mat][1]=MF16(wf[i%PF],af[s][1],(f32x4){}); else acc[T&3][mat][1]=MF16(wf[i%PF],af[s][1],acc[T&3][mat][1]);
      SB();
      slot(IC<2*i+1>{});
      if constexpr(j==50){
        unsigned o=(w*21+(lane<21?lane:20))*128; if(o>20608) o=20608; gtouch(wmd,(const char*)p.wd+(size_t)b*20736,o); }
      if constexpr(j==140) asm_use(wmd);
      if constexpr(i<108 && i%6==3 && PA2+s<18) ldG1(IC<PA2+s>{});
      if constexpr(i<108 && (i%6==1||i%6==4) && s+LAK<18) loadA(IC<s+LAK>{},IC<(i%6)/3>{});
      if constexpr(j>=2 && j<=36 && (j&1)==0){ constexpr int r=(j-2)/2; blm<krow(r)*(F*2)+160,r*4096>(r<16?rwA:rwB,vA,wdst,mB); }
      if constexpr(j>=75 && j<=126 && (j-75)%3==0){ constexpr int r=(j-75)/3; blm<krow(r)*(F*2),r*4096>(r<16?rwA:rwB,vC,wdst,mC); }
      SB();
    });
    BAR();
    g2_prologue();
    sfor<32,36>([&](auto E){ sfor<0,7>([&](auto K){ epi(E,K); }); });
  }
  {
    using namespace g2;
    const int lane=fresh_lane(); const unsigned voff=lane*16; const int g=lane>>4, q=(lane>>2)&3, pp=lane&3, c16=lane&15;
    f32x4 acc[9][2];
    unsigned lwb[2];
    #pragma unroll
    for(int i=0;i<2;i++){ lwb[i]=rb+(4*g+q)*288+pp*8+i*36864; asm volatile("" : "+v"(lwb[i])); }
    bf16x8 wf[PF];
    #define LDW2(i) cat(trread(lwb[(((i)/9)%8)/4]+((((i)/9)%8)%4)*9216+((i)%9)*32),trread(lwb[(((i)/9)%8)/4]+((((i)/9)%8)%4)*9216+((i)%9)*32+16*288))
    waitv<0>(); BAR();
    if(w==1) flag_store(ws,O_FA2+r1*256+fs*8,tag);
    sfor<0,PF-1>([&](auto I){ wf[I.v]=LDW2(I.v); });
    sfor<0,NP>([&](auto I){ constexpr int i=I.v; constexpr int j=i/9, nt=i%9;
      if constexpr((i+BO)%18==0 && i+BO<NP){
        if constexpr(i+BO==54){ if(w==0) poll<8>(ws,O_FA2+(r2t>>1)*256+sp*64,tag,lane); }
        waitv<vmc(i,-1,(i+BO)/9+1)>();
        BAR(); }
      if constexpr(nt==0) gwait<vmc(i,j,-1)>(a2[j%AR][0],a2[j%AR][1]);
      if constexpr(i+PF-1<NP) wf[(i+PF-1)%PF]=LDW2(i+PF-1);
      SB();
      if constexpr(j==0) acc[nt][0]=MF16(wf[i%PF],a2[j%AR][0],(f32x4){}); else acc[nt][0]=MF16(wf[i%PF],a2[j%AR][0],acc[nt][0]);
      if constexpr(j==0) acc[nt][1]=MF16(wf[i%PF],a2[j%AR][1],(f32x4){}); else acc[nt][1]=MF16(wf[i%PF],a2[j%AR][1],acc[nt][1]);
      SB();
      if constexpr(actAt(i)>=0) loadA2(IC<actAt(i)/2>{},IC<actAt(i)%2>{});
      if constexpr(wAt(i)>=0) loadW(IC<wAt(i)/3>{},IC<wAt(i)%3>{});
      SB();
    });
    BAR();
    { char* xs=sm+(w*9)*1024+voff;
      #pragma unroll
      for(int n=0;n<9;n++){ f32x4 v= kh? acc[n][0]:acc[n][1]; *(f32x4*)(xs+n*1024)=v; } }
    LBAR();
    f32x4 part[10];
    { const char* xr=sm+((w^4)*9)*1024+voff;
      #pragma unroll
      for(int n=0;n<9;n++){ f32x4 mine= kh? acc[n][1]:acc[n][0]; part[n]=mine+*(const f32x4*)(xr+n*1024); }
      part[9]=part[8]; }
    const int grp=r2t*4+ct;
    const rsrc_t xr=mkrs(ws+O_X+(size_t)grp*160*1024,160*1024);
    if(tq!=sp){
      #pragma unroll
      for(int n=0;n<5;n++) ST16(((u32x4){pk2(part[2*n][0],part[2*n][1]),pk2(part[2*n][2],part[2*n][3]),pk2(part[2*n+1][0],part[2*n+1][1]),pk2(part[2*n+1][2],part[2*n+1][3])}),xr,voff,((tq*4+sp)*10+kh*5+n)*1024);
    } else {
      char* xs=sm+(72+kh*10)*1024+voff;
      #pragma unroll
      for(int n=0;n<10;n++) *(f32x4*)(xs+n*1024)=part[n];
    }
    publish(ws,O_FX+grp*128+sp*8,tag,w);
    const int tk0=r2t*128+sp*32;
    const rsrc_t rh=mkrs(ws+O_C+(size_t)(tk0>>4)*18432,2*18432), rq=mkrs(ws+O_SSQ+(size_t)tk0*64,2048), rmh=mkrs(ws+O_MH+(size_t)tk0*4,128);
    u32x2 hv[2][2]; float r2[2], mh[2]; int go[2][2];
    #pragma unroll
    for(int x=0;x<2;x++){ int u=w+8*x; if(u>9) u=9; const int tl=u>=5, pr=u-5*tl;
      #pragma unroll
      for(int y=0;y<2;y++){ int nt=2*pr+y; if(nt>8) nt=8; const int nl=nt*16+g*4;
        go[x][y]=(tl*16+c16)*1152+(ct*144+nl)*2;
        const int c3=ct*2+(nl>=72), o72=nl>=72? nl-72:nl;
        int kc,gp,hfh; if(o72<64){ kc=2*c3+(o72>>5); hfh=(o72>>4)&1; gp=(o72>>2)&3; } else { kc=16+(c3>>2); gp=2*(c3&1)+((o72-64)>>2); hfh=(c3>>1)&1; }
        hv[x][y]=LD8(rh,(tl*18+kc)*1024+(gp*16+c16)*16+hfh*8,0); }
      const f32x4 s4=__builtin_bit_cast(f32x4,LD16(rq,tl*1024+voff,0));
      mh[x]=__uint_as_float(__builtin_amdgcn_raw_buffer_load_b32(rmh,(tl*16+c16)*4,0,16));
      r2[x]=__builtin_amdgcn_rcpf(ssq_sum(s4,lane)*(1.f/576.f)+1e-5f); }
    if(w==0) poll<4>(ws,O_FX+grp*128,tag,lane);
    BAR();
    u32x4 rv[2][3];
    #pragma unroll
    for(int x=0;x<2;x++){ int u=w+8*x; if(u>9) u=9;
      #pragma unroll
      for(int y=0;y<3;y++){ int src=(sp+1+y)&3; rv[x][y]=LD16(xr,voff,((sp*4+src)*10+u)*1024); } }
    char* const ob=(char*)p.out+(size_t)(r2t*128+sp*32)*1152;
    #pragma unroll
    for(int x=0;x<2;x++){ if(x==0 || w<2){ const int u=w+8*x; const int tl=u>=5, pr=u-5*tl;
      #pragma unroll
      for(int y=0;y<2;y++){ if(y==0 || pr<4){
        f32x4 v=*(const f32x4*)(sm+(72+tl*10+2*pr+y)*1024+voff);
        #pragma unroll
        for(int z=0;z<3;z++){ v[0]+=lo16(rv[x][z][2*y]); v[1]+=hi16(rv[x][z][2*y]); v[2]+=lo16(rv[x][z][2*y+1]); v[3]+=hi16(rv[x][z][2*y+1]); }
        u32x2 ov; ov[0]=pk2(v[0]*r2[x]+(lo16(hv[x][y][0])+mh[x]),v[1]*r2[x]+(hi16(hv[x][y][0])+mh[x])); ov[1]=pk2(v[2]*r2[x]+(lo16(hv[x][y][1])+mh[x]),v[3]*r2[x]+(hi16(hv[x][y][1])+mh[x]));
        *(u32x2*)(ob+go[x][y])=ov; } } } }
  }
}
}

extern "C" void kernel_launch(void* const* d_in, const int* in_sizes, int n_in,
                              void* d_out, int out_size, void* d_ws, size_t ws_size,
                              hipStream_t stream) {
  using namespace k;
  Params p;
  p.x=(const u16*)d_in[0]; p.wqkv=(const u16*)d_in[1]; p.wo=(const u16*)d_in[2];
  p.wg=(const u16*)d_in[4]; p.wu=(const u16*)d_in[5]; p.wd=(const u16*)d_in[6];
  p.out=(u16*)d_out; p.ws=(char*)d_ws;
  fused_k<<<256,512,0,stream>>>(p);
}
```

```cpp
#include <hip/hip_runtime.h>
#include <hip/hip_bf16.h>
typedef unsigned short u16;
typedef unsigned long long u64;
typedef short s16x4 __attribute__((ext_vector_type(4)));
typedef short s16x8 __attribute__((ext_vector_type(8)));
typedef __bf16 bf16x8 __attribute__((ext_vector_type(8)));
typedef __bf16 bf16x2 __attribute__((ext_vector_type(2)));
typedef float f32x4 __attribute__((ext_vector_type(4)));
typedef float f32x2 __attribute__((ext_vector_type(2)));
typedef unsigned u32x4 __attribute__((ext_vector_type(4)));
typedef unsigned u32x2 __attribute__((ext_vector_type(2)));
#define LAS __attribute__((address_space(3)))
#define GAS __attribute__((address_space(1)))
#define DI __device__ __forceinline__

#define STAMP(i)
#define STAMP_OUT()

#define WARM 21
#define WARM_SLEEP 20
#define STAG 1
#define PRETOUCH 1
#define CODE_KB 48
#define WARM_MODE 1
#define TOUCH_AT 121
#define F0_UNITS 0
#define TOUCH_D 50
#define QSPLIT 24
#define F1_XA 8
#define F1_WA 6
#define P1BAR 36
#define G2_PF 5
#define G2_PRIO 0
#define G2_PRO 4
#define PA1 7
#define PA2 13
#define EPS 1
#define G1_PF 4
#define G1_LAK 5
#define ASP 5
#define F1_LA 6
namespace k {
constexpr int S=2048, D=576, F=4608;
constexpr int STAMP_LDS=156*1024, TAG_LDS=155*1024;
DI int fresh_lane(){ int l; asm volatile("v_mbcnt_lo_u32_b32 %0, -1, 0\n\tv_mbcnt_hi_u32_b32 %0, -1, %0" : "=v"(l)); return l; }
DI u16 f2bf(float f){ __bf16 b=(__bf16)f; return __builtin_bit_cast(u16,b); }
DI unsigned pk2(float lo,float hi){ bf16x2 v; v[0]=(__bf16)lo; v[1]=(__bf16)hi; return __builtin_bit_cast(unsigned,v); }
DI float lo16(unsigned v){ return __uint_as_float(v<<16); }
DI float hi16(unsigned v){ return __uint_as_float(v&0xffff0000u); }
DI float phi(float t){ return (t>0.f? t+1.f: __builtin_amdgcn_exp2f(t*1.44269504089f))+1e-6f; }
DI float bcast_hi(float x){ unsigned a=__float_as_uint(x); auto r=__builtin_amdgcn_permlane32_swap(a,a,false,false); return __uint_as_float(r[1]); }
DI float sum_g(float x){
  unsigned a=__float_as_uint(x); auto r=__builtin_amdgcn_permlane32_swap(a,a,false,false); x=__uint_as_float(r[0])+__uint_as_float(r[1]);
  a=__float_as_uint(x); auto r2=__builtin_amdgcn_permlane16_swap(a,a,false,false); return __uint_as_float(r2[0])+__uint_as_float(r2[1]); }
template<int CTRL> DI float dppf(float x){ return __uint_as_float(__builtin_amdgcn_update_dpp(0u,__float_as_uint(x),CTRL,0xf,0xf,false)); }
DI float ssq_sum(f32x4 v,int){
  const int l=fresh_lane(); const int k=(l>>2)&3;
  #pragma unroll
  for(int r=0;r<4;r++){ float x=v[r]; x+=dppf<0x124>(x); x+=dppf<0x128>(x); v[r]=x; }
  const float t=sum_g(k==0? v[0] : (k==1? v[1] : (k==2? v[2]:v[3])));
  return __uint_as_float(__builtin_amdgcn_ds_bpermute((((l&3)<<2)|k)*4,__float_as_uint(t))); }
DI float dot2(unsigned a,unsigned b,float c){ return __builtin_amdgcn_fdot2_f32_bf16(__builtin_bit_cast(bf16x2,a),__builtin_bit_cast(bf16x2,b),c,false); }
DI s16x4 trread(unsigned a){ return __builtin_amdgcn_ds_read_tr16_b64_v4i16((LAS s16x4*)(size_t)a); }
DI bf16x8 cat(s16x4 a,s16x4 b){ return __builtin_bit_cast(bf16x8,__builtin_shufflevector(a,b,0,1,2,3,4,5,6,7)); }
DI bf16x8 cat2(u32x2 a,u32x2 b){ return __builtin_bit_cast(bf16x8,(u32x4){a[0],a[1],b[0],b[1]}); }
#define BAR() do{ asm volatile("":::"memory"); __builtin_amdgcn_s_barrier(); asm volatile("":::"memory"); }while(0)
#define LBAR() do{ asm volatile("s_waitcnt lgkmcnt(0)":::"memory"); __builtin_amdgcn_s_barrier(); asm volatile("":::"memory"); }while(0)
#define MF16(a,bb,c) __builtin_amdgcn_mfma_f32_16x16x32_bf16(a,bb,c,0,0,0)
#define SB() __builtin_amdgcn_sched_barrier(0)
#define WAITV0() __builtin_amdgcn_s_waitcnt(0x0F70)
template<int I> struct IC{ static constexpr int v=I; };
template<int B,int E,class Fn> DI void sfor(Fn&& f){ if constexpr(B<E){ f(IC<B>{}); sfor<B+1,E>(f); } }
DI void glds16(const void* sbase, unsigned voff, unsigned lds){
  unsigned keep;
  asm volatile("s_mov_b32 %0, m0\n\ts_mov_b32 m0, %3\n\ts_nop 0\n\tglobal_load_lds_dwordx4 %1, %2\n\ts_mov_b32 m0, %0" : "=&s"(keep) : "v"(voff), "s"(sbase), "s"(lds) : "memory");
}
DI void glds16c(const void* sbase, unsigned voff, unsigned lds){
  unsigned keep;
  asm volatile("s_mov_b32 %0, m0\n\ts_mov_b32 m0, %3\n\ts_nop 0\n\tglobal_load_lds_dwordx4 %1, %2 sc1\n\ts_mov_b32 m0, %0" : "=&s"(keep) : "v"(voff), "s"(sbase), "s"(lds) : "memory");
}
DI void glds16m(const void* sbase, unsigned voff, unsigned lds, u64 mask){
  unsigned keep;
  asm volatile("s_mov_b32 %0, m0\n\ts_mov_b32 m0, %3\n\ts_mov_b64 exec, %4\n\tglobal_load_lds_dwordx4 %1, %2\n\ts_mov_b64 exec, -1\n\ts_mov_b32 m0, %0" : "=&s"(keep) : "v"(voff), "s"(sbase), "s"(lds), "s"(mask) : "memory");
}
DI void glds16ms(const void* sbase, unsigned voff, unsigned lds, u64 mask, int skip){
  unsigned keep;
  asm volatile("s_cmp_lg_u32 %5, 0\n\ts_cbranch_scc1 1f\n\ts_mov_b32 %0, m0\n\ts_mov_b32 m0, %3\n\ts_mov_b64 exec, %4\n\tglobal_load_lds_dwordx4 %1, %2\n\ts_mov_b64 exec, -1\n\ts_mov_b32 m0, %0\n1:" : "=&s"(keep) : "v"(voff), "s"(sbase), "s"(lds), "s"(mask), "s"(skip) : "memory","scc");
}
DI u32x4 mkrs4(const void* p){ const u64 a=(u64)p; u32x4 r; r[0]=__builtin_amdgcn_readfirstlane((unsigned)a); r[1]=__builtin_amdgcn_readfirstlane((unsigned)(a>>32)); r[2]=0x7fffffffu; r[3]=0x00020000u; return r; }
template<int SOFF,int LOFF> DI void bl(u32x4 rs,unsigned voff,unsigned lds){ unsigned t;
  asm volatile("s_add_i32 m0, %3, %5\n\ts_mov_b32 %0, %4\n\tbuffer_load_dwordx4 %1, %2, %0 offen lds" : "=&s"(t) : "v"(voff), "s"(rs), "s"(lds), "i"(SOFF), "i"(LOFF) : "memory","scc"); }
template<int SOFF,int LOFF> DI void blm(u32x4 rs,unsigned voff,unsigned lds,u64 mask){ unsigned t;
  asm volatile("s_add_i32 m0, %3, %5\n\ts_mov_b32 %0, %4\n\ts_mov_b64 exec, %6\n\tbuffer_load_dwordx4 %1, %2, %0 offen lds\n\ts_mov_b64 exec, -1" : "=&s"(t) : "v"(voff), "s"(rs), "s"(lds), "i"(SOFF), "i"(LOFF), "s"(mask) : "memory","scc"); }
template<int SOFF,int LOFF,int EQ> DI void blq(u32x4 rs,unsigned voff,unsigned lds,int sel){ unsigned t;
  asm volatile("s_cmp_lg_u32 %6, %7\n\ts_cbranch_scc1 1f\n\ts_add_i32 m0, %3, %5\n\ts_mov_b32 %0, %4\n\tbuffer_load_dwordx4 %1, %2, %0 offen lds\n1:" : "=&s"(t) : "v"(voff), "s"(rs), "s"(lds), "i"(SOFF), "i"(LOFF), "s"(sel), "i"(EQ) : "memory","scc"); }
template<int SOFF,class T> DI void bload16c(T& r,u32x4 rs,unsigned voff){ unsigned t;
  asm volatile("s_mov_b32 %1, %4\n\tbuffer_load_dwordx4 %0, %2, %3, %1 offen sc1" : "=v"(r), "=&s"(t) : "v"(voff), "s"(rs), "i"(SOFF) : "memory"); }
template<int OFF,class T> DI void gload16(T& r,const void* sbase,unsigned voff){ asm volatile("global_load_dwordx4 %0, %1, %2 offset:%3" : "=v"(r) : "v"(voff), "s"(sbase), "i"(OFF) : "memory"); }
template<int OFF,class T> DI void gload16c(T& r,const void* sbase,unsigned voff){ asm volatile("global_load_dwordx4 %0, %1, %2 offset:%3 sc1" : "=v"(r) : "v"(voff), "s"(sbase), "i"(OFF) : "memory"); }
DI void gtouch(unsigned& r,const void* sbase,unsigned voff){ asm volatile("global_load_dword %0, %1, %2" : "=v"(r) : "v"(voff), "s"(sbase) : "memory"); }
DI void asm_use(unsigned r){ asm volatile("" :: "v"(r)); }
template<int N,class T> DI void gwait(T& a,T& b){ asm volatile("s_waitcnt vmcnt(%2)" : "+v"(a),"+v"(b) : "i"(N) : "memory"); }
template<int N> DI void waitv(){ asm volatile("s_waitcnt vmcnt(%0)" :: "i"(N) : "memory"); }
typedef __amdgpu_buffer_rsrc_t rsrc_t;
DI rsrc_t mkrs(const void* p,unsigned bytes){ return __builtin_amdgcn_make_buffer_rsrc((void*)p,0,bytes,0x00020000); }
#define ST16(v,rs,vo,so) __builtin_amdgcn_raw_buffer_store_b128(__builtin_bit_cast(u32x4,v),rs,vo,so,16)
#define ST8(v,rs,vo,so)  __builtin_amdgcn_raw_buffer_store_b64(__builtin_bit_cast(u32x2,v),rs,vo,so,16)
#define ST4(v,rs,vo,so)  __builtin_amdgcn_raw_buffer_store_b32(__float_as_uint(v),rs,vo,so,16)
#define LD16(rs,vo,so) __builtin_amdgcn_raw_buffer_load_b128(rs,vo,so,16)
#define LD8(rs,vo,so)  __builtin_amdgcn_raw_buffer_load_b64(rs,vo,so,16)

constexpr size_t O_EP=0;
constexpr size_t O_F0 =256;
constexpr size_t O_FW =O_F0+2048;
constexpr size_t O_FWO=O_FW+2048;
constexpr size_t O_FKV=O_FWO+1024;
constexpr size_t O_FO =O_FKV+16*256;
constexpr size_t O_FH =O_FO+16*256;
constexpr size_t O_FA =O_FH+8*256;
constexpr size_t O_FA2=O_FA+8*256;
constexpr size_t O_FX =O_FA2+8*256;
constexpr size_t O_KVP=32768;
constexpr size_t O_XN =O_KVP+(size_t)256*8192;
constexpr size_t O_MX =O_XN+(size_t)S*D*2;
constexpr size_t O_WB =O_MX+(size_t)S*4;
constexpr size_t O_WQ =O_WB+4096;
constexpr size_t O_WO =O_WQ+(size_t)16*144*1024;
constexpr size_t O_O  =O_WO+(size_t)8*120*1024;
constexpr size_t O_MU =O_O+(size_t)128*16*1536;
constexpr size_t O_MH =O_MU+(size_t)S*64;
constexpr size_t O_C  =O_MH+(size_t)S*4;
constexpr size_t O_SSQ=O_C+(size_t)S*D*2;
constexpr size_t O_ACT=O_SSQ+(size_t)S*64;
constexpr size_t O_X  =O_ACT+(size_t)S*F*2;
constexpr size_t O_END=O_X+(size_t)64*160*1024;
struct Params { const u16 *x,*wqkv,*wo,*wg,*wu,*wd; u16* out; char* ws; };

DI void publish(char* ws,size_t off,u64 tag,int w){
  asm volatile("s_waitcnt vmcnt(0) lgkmcnt(0)":::"memory");
  BAR();
  if(w==0 && fresh_lane()==0) __hip_atomic_store((GAS u64*)(ws+off),tag,__ATOMIC_RELAXED,__HIP_MEMORY_SCOPE_AGENT);
}
DI void flag_store(char* ws,size_t off,u64 tag){ if(fresh_lane()==0) __hip_atomic_store((GAS u64*)(ws+off),tag,__ATOMIC_RELAXED,__HIP_MEMORY_SCOPE_AGENT); }
DI void pollp(const GAS u64* p,u64 tag){
  for(unsigned spins=0; spins<(1u<<20); ++spins){
    u64 v=__hip_atomic_load(p,__ATOMIC_RELAXED,__HIP_MEMORY_SCOPE_AGENT);
    if(__all(v==tag)) break;
    __builtin_amdgcn_s_sleep(1);
  }
  __builtin_amdgcn_fence(__ATOMIC_ACQUIRE,"wavefront");
}
template<int N> DI void poll(char* ws,size_t off,u64 tag,int lane){ pollp((const GAS u64*)(ws+off)+(lane<N?lane:0),tag); }

namespace f1 {
constexpr int PF=6, XA=F1_XA, WA=F1_WA;
constexpr int wr(int r){ return r<WA? -1 : (r<12? 5*(r-WA)+3 : 53+5*(r-12)); }
constexpr int roundAt(int i){ for(int r=WA;r<18;r++) if(wr(r)==i) return r; return -1; }
constexpr int vmc(int iw,int xs,int pw){
  int cnt=0;
  for(int k=0;k<XA;k++){ if(k<WA){ if(8*k<pw) cnt=0; else cnt++; } if(k<=xs) cnt=0; else cnt++; }
  for(int i=0;i<iw;i++){
    if(i%5==1 && i/5+XA<18){ if(i/5+XA<=xs) cnt=0; else cnt++; }
    int r=roundAt(i); if(r>=0){ if(8*r<pw) cnt=0; else cnt++; }
  }
  return cnt;
}
constexpr int lpos(int p){ return p<90? p : p+12; }
}
namespace g1 {
constexpr int PF=4, LAK=4;
constexpr int pT(int i){ return i<120? (i%8)/2 : (i<144? (i-120)/6 : 4+(i-144)/36); }
constexpr int pS(int i){ return i<120? i/8 : (i<144? 15+((i-120)%6)/2 : ((i-144)%36)/2); }
constexpr int krow(int kc){ return kc<16? 72*(kc>>1)+32*(kc&1) : 288*(kc-16)+64; }
constexpr int iB(int r){ return r<10? 81+4*r : (r<15? 122+3*(r-10) : 147+2*(r-15)); }
constexpr int iA(int s,int t){ return s<LAK? -1 : ASP*(s-LAK)+1+2*t; }
constexpr int aAt(int i){ for(int s=LAK;s<18;s++) for(int t=0;t<2;t++) if(iA(s,t)==i) return s*2+t; return -1; }
constexpr int nAafter(int i,int s){ int n=0; for(int x=s+1;x<18;x++) for(int t=0;t<2;t++) if(iA(x,t)<i) n++; return n; }
constexpr int iC(int r){ return 183+2*r; }
constexpr int rB(int i){ for(int r=0;r<18;r++) if(iB(r)==i) return r; return -1; }
constexpr int rC(int i){ for(int r=0;r<18;r++) if(iC(r)==i) return r; return -1; }
constexpr int nB(int i){ int n=0; for(int r=0;r<18;r++) if(iB(r)<i) n++; return n; }
constexpr int firstUse(int T,int s){ for(int i=0;i<324;i++) if(pT(i)==T&&pS(i)==s) return i; return -1; }
}
namespace g2 {
constexpr int LW=5, PF=G2_PF, BO=8, NP=162, AR=6;
constexpr int kord(int j){ constexpr int t[18]={0,1,4,5,8,9,12,13, 2,3,6,7,10,11,14,15,16,17}; return t[j]; }
constexpr int PRO=G2_PRO;
constexpr int actStep(int j,int t){ return j<PRO? -1 : (j<6? 5*(j-PRO)+2+2*t : ((j>=8&&j<=10)? 50+2*(j-8)+t : (j<8? 9*(j-4)+2+3*t : 9*(j-5)+2+3*t))); }
constexpr int wStep(int j,int x){ return j<PRO? -1 : (j<8? 5*(j-PRO)+(x==2?3:x) : 9*(j-LW)+(x==0?1:(x==1?3:6))); }
constexpr int vmc(int iw,int ja,int jw){
  int best=63;
  for(int tq=0;tq<4;tq++){
    int cnt=0;
    for(int j=0;j<PRO;j++){ int n=2+((j%4)==tq); if(j<=jw) cnt=0; else cnt+=n; if(j<=ja) cnt=0; else cnt+=2; }
    for(int i=0;i<iw;i++){
      for(int j=PRO;j<18;j++) for(int t=0;t<2;t++) if(actStep(j,t)==i){ if(j<=ja) cnt=0; else cnt++; }
      for(int j=PRO;j<18;j++) for(int x=0;x<3;x++) if(wStep(j,x)==i && (x<2||(j%4)==tq)){ if(j<=jw) cnt=0; else cnt++; }
    }
    if(cnt<best) best=cnt;
  }
  return best;
}
constexpr int wAt(int i){ for(int j=PRO;j<18;j++) for(int x=0;x<3;x++) if(wStep(j,x)==i) return j*3+x; return -1; }
constexpr int actAt(int i){ for(int j=PRO;j<18;j++) for(int t=0;t<2;t++) if(actStep(j,t)==i) return j*2+t; return -1; }
}

__global__ __launch_bounds__(512,2) void fused_k(Params p){
  __shared__ __attribute__((aligned(16))) char sm[160*1024];
  const int tid=threadIdx.x, lane=tid&63, w=__builtin_amdgcn_readfirstlane(tid>>6);
  const int b=blockIdx.x, xcd=b&7, idx=b>>3;
  const unsigned smb=(unsigned)(size_t)(LAS char*)sm;
  const unsigned voff=lane*16;
  const int g=lane>>4, q=(lane>>2)&3, pp=lane&3, c16=lane&15;
  char* const ws=p.ws;
  STAMP(0);
  u64 tag;
  const int hp=(xcd&1)*8+(idx>>2), rt=(xcd>>1)*4+(idx&3);
  unsigned wm[2]={0,0};
  auto touch_gu=[&](int slot,int unit,int lo){
    int ln=unit*64+lane; if(ln>323) ln=323; const int mt=ln>=162; ln-=mt*162;
    const char* src=(const char*)(mt? p.wu : p.wg)+(size_t)b*20736+ln*128;
    asm volatile("s_cmp_lt_u32 %2, %3\n\ts_cbranch_scc1 1f\n\tglobal_load_dword %0, %1, off\n1:" : "+v"(wm[slot]) : "v"(src), "s"(w), "s"(lo) : "memory","scc"); };
  {
    char* const smw=sm+w*16384;
    const unsigned smwb=smb+w*16384;
    if(w<2){
      u32x2 ep; { const unsigned z=0; asm volatile("global_load_dwordx2 %0, %1, %2 sc1" : "=v"(ep) : "v"(z), "s"(ws+(w?(size_t)O_XN:0)) : "memory"); }
      u32x4 v[5];
      { const char* xb=(const char*)p.x+(size_t)(b*8+w*4)*1152; const unsigned v0=g*1152+c16*16, v4=g*1152+(64+(c16&7))*16;
        gload16<0>(v[0],xb,v0); gload16<256>(v[1],xb,v0); gload16<512>(v[2],xb,v0); gload16<768>(v[3],xb,v0); gload16<0>(v[4],xb,v4); }
      asm volatile("s_waitcnt vmcnt(0)" : "+v"(ep),"+v"(v[0]),"+v"(v[1]),"+v"(v[2]),"+v"(v[3]),"+v"(v[4]) :: "memory");
      if(tid==0) *(u32x2*)(sm+TAG_LDS)=ep;
      float s1=0;
      #pragma unroll
      for(int i=0;i<5;i++){ const unsigned one=(i<4||c16<8)?0x3f803f80u:0u;
        #pragma unroll
        for(int j=0;j<4;j++) s1=dot2(v[i][j],one,s1); }
      s1+=__shfl_xor(s1,1); s1+=__shfl_xor(s1,2); s1+=__shfl_xor(s1,4); s1+=__shfl_xor(s1,8);
      const float m=s1*(1.f/576.f);
      float f[5][8]; float s2=0,s2t=0;
      #pragma unroll
      for(int i=0;i<5;i++)
        #pragma unroll
        for(int j=0;j<4;j++){ f[i][2*j]=lo16(v[i][j])-m; f[i][2*j+1]=hi16(v[i][j])-m;
          if(i<4){ s2=fmaf(f[i][2*j],f[i][2*j],s2); s2=fmaf(f[i][2*j+1],f[i][2*j+1],s2); } else { s2t=fmaf(f[i][2*j],f[i][2*j],s2t); s2t=fmaf(f[i][2*j+1],f[i][2*j+1],s2t); } }
      s2+= c16<8? s2t:0.f;
      s2+=__shfl_xor(s2,1); s2+=__shfl_xor(s2,2); s2+=__shfl_xor(s2,4); s2+=__shfl_xor(s2,8);
      const float r=__builtin_amdgcn_rsqf(s2*(1.f/576.f)+1e-5f);
      #pragma unroll
      for(int i=0;i<5;i++){ u32x4 o;
        #pragma unroll
        for(int j=0;j<4;j++) o[j]=pk2(f[i][2*j]*r,f[i][2*j+1]*r);
        *(u32x4*)(smw+(i<4? c16+16*i: 64+(c16&7))*80+g*16)=o; }
      asm volatile("s_waitcnt lgkmcnt(0)":::"memory");
      const rsrc_t rx=mkrs(ws+O_XN+(size_t)(b>>1)*18432+(b&1)*128+w*64,18432);
      #pragma unroll
      for(int i=0;i<5;i++){ const int ch=(lane>>2)+16*i; if(ch<72) ST16(*(const u32x4*)(smw+ch*80+pp*16),rx,ch*256+pp*16,0); }
      const rsrc_t rm=mkrs(ws+O_MX+(size_t)b*32+w*16,16);
      if(c16==0) ST4(m,rm,g*4,0);
    } else if(w<4){
      if(b<144){
        const int s=b>>3, hq=b&7, h2=w-2;
        u32x4 v[8];
        const char* src=(const char*)p.wqkv+(size_t)s*32*3456+hq*144+h2*64;
        int lo_[8];
        #pragma unroll
        for(int i=0;i<8;i++){ const int r=g+4*i; int c=c16<15?c16:14; const int part=c/5, cc=c-part*5; lo_[i]=r*272+c*16; gload16<0>(v[i],src,r*3456+part*1152+cc*16); }
        asm volatile("s_waitcnt vmcnt(0)" : "+v"(v[0]),"+v"(v[1]),"+v"(v[2]),"+v"(v[3]),"+v"(v[4]),"+v"(v[5]),"+v"(v[6]),"+v"(v[7]) :: "memory");
        #pragma unroll
        for(int i=0;i<8;i++) *(u32x4*)(smw+lo_[i])=v[i];
        asm volatile("s_waitcnt lgkmcnt(0)":::"memory");
        const rsrc_t rw=mkrs(ws+O_WQ+(size_t)(hq*2+h2)*144*1024,144*1024);
        #pragma unroll
        for(int t=0;t<8;t++){
          const int vc=(t<3? 16*t:16*(t-3))+4*pp;
          const int col= t<3? (vc<36? vc:0) : (vc<36? 40+vc : (vc<72? 80+vc-36 : 0));
          const unsigned ba=smwb+(8*g+q)*272+(h2*4+col)*2;
          u32x4 o=__builtin_bit_cast(u32x4,cat(trread(ba),trread(ba+4*272)));
          if((t<3? 16*t:16*(t-3))+c16>=(t<3?36:72)) o=(u32x4){0,0,0,0};
          ST16(o,rw,voff,(t<3? 90+s*3+t : s*5+t-3)*1024); }
      } else if(b<240){
        const int jb=b-144, kap=jb>>2, ct=(jb&3)*2+(w-2);
        const int kq=kap/6, kl=kap%6;
        u32x4 v[5];
        const char* src=(const char*)p.wo+ct*144;
        bool okr[5]; int lo_[5];
        #pragma unroll
        for(int i=0;i<5;i++){ int e=lane+64*i; if(e>287) e=287; const int r=e/9, c=e-r*9, row=r&15, hi=r>>4;
          int kr; if(kl<4){ kr=(4*kq+kl)*36+(hi? 12+row: row-4); okr[i]= hi|| row>=4; } else { kr=(4*kq+2*(kl-4)+hi)*36+28+row; okr[i]=row<8; }
          lo_[i]=r*160+c*16; gload16<0>(v[i],src,(okr[i]?kr:0)*1152+c*16); }
        asm volatile("s_waitcnt vmcnt(0)" : "+v"(v[0]),"+v"(v[1]),"+v"(v[2]),"+v"(v[3]),"+v"(v[4]) :: "memory");
        #pragma unroll
        for(int i=0;i<5;i++) *(u32x4*)(smw+lo_[i])= okr[i]? v[i]:(u32x4){0,0,0,0};
        asm volatile("s_waitcnt lgkmcnt(0)":::"memory");
        const rsrc_t rw=mkrs(ws+O_WO+(size_t)ct*120*1024,120*1024);
        #pragma unroll
        for(int nt=0;nt<5;nt++){
          const int col=nt*16+4*((nt==4)?(pp&1):pp);
          const unsigned ba=smwb+(4*g+q)*160+col*2;
          u32x4 o=__builtin_bit_cast(u32x4,cat(trread(ba),trread(ba+16*160)));
          if(nt==4 && c16>=8) o=(u32x4){ (kl<4 && g==0 && (c16&3)==0)? 0x3f803f80u:0u ,0u,0u,0u};
          ST16(o,rw,voff,(kap*5+nt)*1024); }
      }
    } else if(w==4 && rt<9){
      u32x4 v[5];
      const char* rp=(const char*)p.wo+(size_t)(hp*36+rt*4)*1152;
      const unsigned v0=g*1152+c16*16, v4=g*1152+(64+(c16&7))*16;
      gload16<0>(v[0],rp,v0); gload16<256>(v[1],rp,v0); gload16<512>(v[2],rp,v0); gload16<768>(v[3],rp,v0); gload16<0>(v[4],rp,v4);
      asm volatile("s_waitcnt vmcnt(0)" : "+v"(v[0]),"+v"(v[1]),"+v"(v[2]),"+v"(v[3]),"+v"(v[4]) :: "memory");
      float sacc=0;
      #pragma unroll
      for(int i=0;i<5;i++){ const unsigned one=(i<4||c16<8)?0x3f803f80u:0u;
        #pragma unroll
        for(int j=0;j<4;j++) sacc=dot2(v[i][j],one,sacc); }
      sacc+=__shfl_xor(sacc,1); sacc+=__shfl_xor(sacc,2); sacc+=__shfl_xor(sacc,4); sacc+=__shfl_xor(sacc,8);
      if(c16==0) ST4(sacc*(1.f/576.f),mkrs(ws+O_WB+(size_t)(hp*36+rt*4)*4,16),g*4,0);
    }
    if(w>=5){ __builtin_amdgcn_s_sleep(WARM_SLEEP); touch_gu(1,F0_UNITS? (w-5):(w==5?4:5),5); }
    STAMP(1);
    if(w<5) asm volatile("s_waitcnt vmcnt(0) lgkmcnt(0)":::"memory");
    BAR();
    { const u32x2 e=*(const u32x2*)(sm+TAG_LDS); unsigned lo=__builtin_amdgcn_readfirstlane(e[0]), hi=__builtin_amdgcn_readfirstlane(e[1]); tag=(((u64)hi<<32)|lo)+1ull; }
    if(tid<2 && (tid==0 || b<240)){
      const size_t fo= tid==0? O_F0+b*8 : (b<144? O_FW+(b&7)*256+(b>>3)*8 : O_FWO+((b-144)&3)*256+((b-144)>>2)*8);
      __hip_atomic_store((GAS u64*)(ws+fo),tag,__ATOMIC_RELAXED,__HIP_MEMORY_SCOPE_AGENT); }
    STAMP(2);
    if(w==0) pollp((const GAS u64*)(ws+(lane<16? O_F0+rt*128+lane*8 : (lane<34? O_FW+(hp>>1)*256+(lane-16)*8 : O_FWO+((idx&7)>>1)*256+(lane<58?lane-34:0)*8))),tag);
    BAR();
  }
  STAMP(3);
  const int tt=rt*8+w;
  const int ct3=idx&7, r3=xcd*4+(idx>>3);
  constexpr int WOL=8*1024;
  bf16x8 q01; u32x2 q2;
  bool okv[2][4]; bool anyok[2]={false,false};
  {
    using namespace f1;
    const char* wbase=ws+O_WQ+(size_t)hp*144*1024+w*1024;
    const char* abase=ws+O_XN+(size_t)tt*18*1024;
    bf16x8 af[18];
    auto ldW=[&](auto R){ constexpr int r=R.v; const int pc=r*8+w; glds16c(wbase+r*8192,voff,smb+((r<11||(r==11&&0))? pc : (pc<90? pc:pc+12))*1024); };
    auto ldX=[&](auto Sx){ constexpr int s=Sx.v; gload16c<(s&3)*1024>(af[s],abase+(s>>2)*4096,voff); };
    sfor<0,XA>([&](auto K){ if constexpr(K.v<WA) ldW(K); ldX(K); });
    f32x4 acc[8];
    unsigned lwb[4];
    #pragma unroll
    for(int i=0;i<4;i++){ lwb[i]=smb+voff+i*49152; asm volatile("" : "+v"(lwb[i])); }
    bf16x8 wf[PF];
    #define LDW1(i) (*(const LAS bf16x8*)((const LAS char*)(size_t)lwb[lpos(i)/48]+(lpos(i)%48)*1024))
    #define WAITP(m,n) asm volatile("s_waitcnt vmcnt(%2)" : "+v"(af[2*(m)]),"+v"(af[2*(m)+1]) : "i"(n) : "memory")
    WAITP(0,vmc(0,1,10)); BAR();
    STAMP(4);
    sfor<0,PF-1>([&](auto I){ wf[I.v]=LDW1(I.v); });
    sfor<0,90>([&](auto I){ constexpr int i=I.v; constexpr int s=i/5,t=i%5;
      if constexpr(i%10==4 && i/10<8){ constexpr int m=i/10+1; WAITP(m,vmc(i,2*m+1,10*(m+1))); BAR(); }
      if constexpr(i+PF-1<90) wf[(i+PF-1)%PF]=LDW1(i+PF-1);
      SB();
      if constexpr(s==0) acc[3+t]=MF16(af[s],wf[i%PF],(f32x4){}); else acc[3+t]=MF16(af[s],wf[i%PF],acc[3+t]);
      SB();
      if constexpr(t==1 && s+XA<18) ldX(IC<s+XA>{});
      if constexpr(roundAt(i)>=0) ldW(IC<roundAt(i)>{});
      SB();
    });
    STAMP(5);
    u32x2 kvb[5];
    #pragma unroll
    for(int t=0;t<5;t++){ float x[4];
      #pragma unroll
      for(int r4=0;r4<4;r4++){ float a=acc[3+t][r4];
        if(t<2) a=phi(a); else if(t==2) a= c16<4? phi(a):a; else if(t==4) a= c16<8? a : 1.f;
        x[r4]=a; }
      kvb[t][0]=pk2(x[0],x[1]); kvb[t][1]=pk2(x[2],x[3]); }
    BAR();
    { char* xs=sm+w*8192+voff;
      #define KVM(idx,kt,vt) *(f32x4*)(xs+(idx)*1024)=__builtin_amdgcn_mfma_f32_16x16x16bf16_1k(__builtin_bit_cast(s16x4,kvb[kt]),__builtin_bit_cast(s16x4,kvb[vt]),(f32x4){},0,0,0)
      KVM(0,0,2); KVM(1,1,2); KVM(2,0,3); KVM(3,1,3); KVM(4,2,3); KVM(5,0,4); KVM(6,1,4); KVM(7,2,4); }
    waitv<0>();
    LBAR();
    touch_gu(0,w-4,4);
    #pragma unroll
    for(int x=0;x<2;x++){ const int tl=(w&3)+4*x;
      const int kt=(tl==0||tl==2||tl==5)?0:((tl==1||tl==3||tl==6)?1:2), vt= tl<2?2:(tl<5?3:4);
      #pragma unroll
      for(int r4=0;r4<4;r4++){ int d=16*kt+4*g+r4, v=16*vt+c16;
        bool ok; if(v<72) ok= v>=36 && d<36 && ((d<18)==(v-36<18)); else ok=(c16==8||c16==12)? d<18 : ((c16==9||c16==13)? (d>=18&&d<36):false);
        okv[x][r4]=ok; anyok[x]|=ok; } }
    if(w<4){
      const rsrc_t rkv=mkrs(ws+O_KVP+(size_t)hp*16*8192,16*8192);
      #pragma unroll
      for(int x=0;x<2;x++){ const int tl=w+4*x;
        f32x4 sacc=*(const f32x4*)(sm+tl*1024+voff);
        #pragma unroll
        for(int ww=1;ww<8;ww++) sacc+=*(const f32x4*)(sm+ww*8192+tl*1024+voff);
        if(anyok[x]) ST16(sacc,rkv,voff,rt*8192+tl*1024); }
    }
    auto qsteps=[&](auto B,auto E){ constexpr int b0=B.v,e0=E.v;
      sfor<b0,(b0+PF-1<e0?b0+PF-1:e0)>([&](auto I){ wf[I.v%PF]=LDW1(I.v); });
      sfor<b0,e0>([&](auto I){ constexpr int i=I.v; constexpr int s=(i-90)/3,t=(i-90)%3;
        if constexpr(i+PF-1<e0) wf[(i+PF-1)%PF]=LDW1(i+PF-1);
        SB();
        if constexpr(s==0) acc[t]=MF16(wf[i%PF],af[s],(f32x4){}); else acc[t]=MF16(wf[i%PF],af[s],acc[t]);
        SB(); }); };
    qsteps(IC<90>{},IC<90+QSPLIT>{});
    if(w<4) waitv<0>();
    BAR();
    if(tid==0) __hip_atomic_store((GAS u64*)(ws+O_FKV+hp*256+rt*8),tag,__ATOMIC_RELAXED,__HIP_MEMORY_SCOPE_AGENT);
    STAMP(6);
    if(w>=4){
      const char* wob=ws+O_WO+(size_t)ct3*120*1024;
      #pragma unroll
      for(int i=0;i<24;i++){ int pc=(w-4)+4*i; if(pc>93) pc=93; glds16c(wob+pc*1024,voff,smb+WOL+pc*1024); } }
    qsteps(IC<90+QSPLIT>{},IC<144>{});
    { u32x4 v; v[0]=pk2(phi(acc[0][0]),phi(acc[0][1])); v[1]=pk2(phi(acc[0][2]),phi(acc[0][3])); v[2]=pk2(phi(acc[1][0]),phi(acc[1][1])); v[3]=pk2(phi(acc[1][2]),phi(acc[1][3]));
      q01=__builtin_bit_cast(bf16x8,v);
      q2[0]=pk2(phi(acc[2][0]),phi(acc[2][1])); q2[1]=pk2(phi(acc[2][2]),phi(acc[2][3])); }
  }
  if(w==0) poll<16>(ws,O_FKV+hp*256,tag,lane);
  BAR();
  STAMP(22);
  if(w<4){
    u32x4 pv[2][16]; u32x4 wb4;
    { const char* pb=ws+O_KVP+(size_t)hp*16*8192+w*1024;
      #pragma unroll
      for(int x=0;x<2;x++){ const unsigned vo=(anyok[x]?voff:0u)+x*4096;
        #pragma unroll
        for(int i=0;i<16;i++) gload16c<0>(pv[x][i],pb+i*8192,vo); }
      gload16c<0>(wb4,ws+O_WB+hp*144,(lane<9?lane:0)*16); }
    #pragma unroll
    for(int x=0;x<2;x++){
      if(x==0) waitv<17>(); else waitv<0>();
      asm volatile("" : "+v"(pv[x][0]),"+v"(pv[x][1]),"+v"(pv[x][2]),"+v"(pv[x][3]),"+v"(pv[x][4]),"+v"(pv[x][5]),"+v"(pv[x][6]),"+v"(pv[x][7]),
         "+v"(pv[x][8]),"+v"(pv[x][9]),"+v"(pv[x][10]),"+v"(pv[x][11]),"+v"(pv[x][12]),"+v"(pv[x][13]),"+v"(pv[x][14]),"+v"(pv[x][15]) :: "memory");
      f32x4 sacc=__builtin_bit_cast(f32x4,pv[x][0]);
      #pragma unroll
      for(int i=1;i<16;i++) sacc+=__builtin_bit_cast(f32x4,pv[x][i]);
      u32x2 pk; pk[0]=pk2(okv[x][0]?sacc[0]:0.f,okv[x][1]?sacc[1]:0.f); pk[1]=pk2(okv[x][2]?sacc[2]:0.f,okv[x][3]?sacc[3]:0.f);
      *(u32x2*)(sm+(w+4*x)*512+lane*8)=pk; }
    asm volatile("" : "+v"(wb4));
    if(w==0 && lane<9) *(u32x4*)(sm+4096+lane*16)=wb4;
  } else {
    __builtin_amdgcn_s_sleep(8);
    const char* wob=ws+O_WO+(size_t)ct3*120*1024;
    #pragma unroll
    for(int i=0;i<7;i++){ int pc=94+(w-4)+4*i; if(pc>119) pc=119; glds16c(wob+pc*1024,voff,smb+WOL+pc*1024); }
  }
  LBAR();
  STAMP(7);
  { const char* kk=sm+lane*8; const u32x2 z2={0,0};
    #define KF(i) (*(const u32x2*)(kk+(i)*512))
    f32x4 A=MF16(cat2(KF(0),KF(1)),q01,(f32x4){});
    f32x4 B=MF16(cat2(KF(2),KF(3)),q01,(f32x4){}); B=MF16(cat2(KF(4),z2),cat2(q2,z2),B);
    f32x4 C=MF16(cat2(KF(5),KF(6)),q01,(f32x4){}); C=MF16(cat2(KF(7),z2),cat2(q2,z2),C);
    const float ra=__builtin_amdgcn_rcpf(bcast_hi(C[0])+1e-6f), rb=__builtin_amdgcn_rcpf(bcast_hi(C[1])+1e-6f);
    const float b01= g<2? ra:rb, b23= g<1? ra:rb, cc= g<2? rb:0.f;
    A*=ra; B[0]*=b01; B[1]*=b01; B[2]*=b23; B[3]*=b23; C*=cc;
    const char* wbl=sm+4096;
    const f32x4 t4=A*(*(const f32x4*)(wbl+(g>0?g-1:0)*16))+B*(*(const f32x4*)(wbl+48+g*16))+C*(*(const f32x4*)(wbl+112+(g<2?g:0)*16));
    const float mu=sum_g((t4[0]+t4[1])+(t4[2]+t4[3]));
    const float muh=lo16((unsigned)f2bf(mu));
    if(g==0){ A[0]=muh; A[1]=mu-muh; }
    const rsrc_t ro=mkrs(ws+O_O+((size_t)tt*16+hp)*1536,1536);
    ST16(((u32x4){pk2(A[0],A[1]),pk2(A[2],A[3]),pk2(B[0],B[1]),pk2(B[2],B[3])}),ro,voff,0);
    ST8(((u32x2){pk2(C[0],C[1]),pk2(C[2],C[3])}),ro,lane*8,1024);
  }
  publish(ws,O_FO+rt*256+hp*8,tag,w);
  asm volatile("" :: "v"(wm[0]),"v"(wm[1]));
  STAMP(8);
  const int r1=(xcd&1)*4+(idx&3), fs=(xcd>>1)*8+(idx>>2);
  const int L0=9*(fs>>2)+2*(fs&3);
  const char* wsrc=(const char*)((w>>2)? p.wu:p.wg)+(size_t)L0*128;
  const unsigned wdst=smb+w*1024;
  unsigned vA,vC; u64 mC;
  { const int r8=lane>>3, pos=lane&7, sl=(pos>>1)^((r8>>1)&3);
    vA=r8*(F*2)+sl*32+(pos&1)*16;
    mC=__ballot(sl==0); }
  const int w3=w&3;
  const u32x4 rwA=mkrs4(wsrc+(size_t)w3*8*(F*2)), rwB=mkrs4(wsrc+(size_t)w3*72*(F*2));
  auto ldG1=[&](auto R){ constexpr int r=R.v; bl<g1::krow(r)*(F*2),r*8192>(r<16?rwA:rwB,vA,wdst); };
  {
    const int th=w&1, kq=w>>1;
    const int tq=th*2+(kq>>1), hf=kq&1;
    const int t3=r3*4+tq;
    constexpr int XS=116*1024;
    bf16x8 wpre[30];
    { const char* rb=sm+WOL+kq*30720+voff;
      #pragma unroll
      for(int i=0;i<30;i++) wpre[i]=*(const bf16x8*)(rb+i*1024); }
    u32x2 xv[3]; unsigned mxu;
    { const char* xb=(const char*)p.x+(size_t)t3*16*1152+ct3*144;
      #pragma unroll
      for(int i=0;i<3;i++){ int nt= hf? 2+i : (i<2? i:1); int n=nt*16+4*g; if(n>=72) n=68;
        asm volatile("global_load_dwordx2 %0, %1, %2" : "=v"(xv[i]) : "v"(c16*1152+n*2), "s"(xb) : "memory"); } }
    LBAR();
    #pragma unroll
    for(int i=0;i<30;i++) asm volatile("" : "+v"(wpre[i]));
    if(w==7){
      poll<16>(ws,O_FO+(r3>>1)*256,tag,lane);
    } else {
      sfor<0,PA1>(ldG1);
    }
    BAR();
    STAMP(9);
    u32x4 oab[2][4]; u32x2 oc[2][4];
    { const char* ob=ws+O_O+((size_t)(r3*4+th*2)*16+kq*4)*1536;
      #pragma unroll
      for(int i=0;i<4;i++){ gload16c<0>(oab[0][i],ob+i*1536,voff); gload16c<0>(oab[1][i],ob+24576+i*1536,voff); }
      #pragma unroll
      for(int i=0;i<4;i++){ asm volatile("global_load_dwordx2 %0, %1, %2 offset:1024 sc1" : "=v"(oc[0][i]) : "v"(lane*8), "s"(ob+i*1536) : "memory");
                            asm volatile("global_load_dwordx2 %0, %1, %2 offset:1024 sc1" : "=v"(oc[1][i]) : "v"(lane*8), "s"(ob+24576+i*1536) : "memory"); }
      asm volatile("global_load_dword %0, %1, %2 sc1" : "=v"(mxu) : "v"(c16*4), "s"(ws+O_MX+(size_t)t3*64) : "memory"); }
    f32x4 acc[2][5]={};
    sfor<0,4>([&](auto KL){ constexpr int kl=KL.v;
      gwait<15-2*kl>(oab[0][kl],oab[1][kl]);
      #pragma unroll
      for(int t=0;t<2;t++)
        #pragma unroll
        for(int n=0;n<5;n++) acc[t][n]=MF16(wpre[kl*5+n],__builtin_bit_cast(bf16x8,oab[t][kl]),acc[t][n]); });
    asm volatile("s_waitcnt vmcnt(0)" : "+v"(oc[0][0]),"+v"(oc[0][1]),"+v"(oc[0][2]),"+v"(oc[0][3]),"+v"(oc[1][0]),"+v"(oc[1][1]),"+v"(oc[1][2]),"+v"(oc[1][3]),"+v"(mxu),"+v"(xv[0]),"+v"(xv[1]),"+v"(xv[2]) :: "memory");
    #pragma unroll
    for(int kl=0;kl<2;kl++)
      #pragma unroll
      for(int t=0;t<2;t++)
        #pragma unroll
        for(int n=0;n<5;n++) acc[t][n]=MF16(wpre[(4+kl)*5+n],cat2(oc[t][2*kl],oc[t][2*kl+1]),acc[t][n]);
    STAMP(10);
    f32x4 a[5];
    { char* xs=sm+XS+w*5120+voff; const bool hi=kq>>1;
      #pragma unroll
      for(int n=0;n<5;n++){ f32x4 u0=acc[0][n],u1=acc[1][n]; asm volatile("" : "+v"(u0),"+v"(u1)); a[n]=hi?u1:u0; *(f32x4*)(xs+n*1024)=hi?u0:u1; } }
    LBAR();
    { const char* xr=sm+XS+(w^4)*5120+voff;
      #pragma unroll
      for(int n=0;n<5;n++) a[n]+=*(const f32x4*)(xr+n*1024); }
    #pragma unroll
    for(int n=0;n<5;n++) asm volatile("" : "+v"(a[n]));
    f32x4 keep[3];
    { char* xs=sm+XS+(w^4)*5120+voff;
      keep[0]=hf?a[2]:a[0]; keep[1]=hf?a[3]:a[1]; keep[2]=a[4];
      *(f32x4*)(xs)=hf?a[0]:a[2]; *(f32x4*)(xs+1024)=hf?a[1]:a[3]; *(f32x4*)(xs+2048)=a[4]; }
    LBAR();
    { const char* xr=sm+XS+(w^6)*5120+voff;
      const rsrc_t rc=mkrs(ws+O_C+(size_t)t3*18*1024,18*1024), rq=mkrs(ws+O_SSQ+(size_t)t3*1024,1024);
      #pragma unroll
      for(int i=0;i<3;i++) keep[i]+=*(const f32x4*)(xr+i*1024);
      const float m=__uint_as_float(mxu)+bcast_hi(keep[2][0]);
      float qs=0; unsigned cp[6];
      #pragma unroll
      for(int i=0;i<3;i++){
        const f32x4 v=keep[i];
        const float c0=(lo16(xv[i][0])+v[0])-m,c1=(hi16(xv[i][0])+v[1])-m,c2=(lo16(xv[i][1])+v[2])-m,c3=(hi16(xv[i][1])+v[3])-m;
        const float qq=(c0*c0+c1*c1)+(c2*c2+c3*c3);
        qs+= (i<2 || (hf && g<2))? qq:0.f;
        cp[2*i]=pk2(c0,c1); cp[2*i+1]=pk2(c2,c3); }
      ST16(((u32x4){cp[0],cp[1],cp[2],cp[3]}),rc,voff,(2*ct3+hf)*1024);
      if(hf && g<2) ST8(((u32x2){cp[4],cp[5]}),rc,((2*(ct3&1)+g)*16+c16)*16+((ct3>>1)&1)*8,(16+(ct3>>2))*1024);
      qs=sum_g(qs);
      if(lane<16){ ST4(qs,rq,lane*4,(ct3*2+hf)*64); if(ct3==0 && hf==0) ST4(m,mkrs(ws+O_MH+(size_t)t3*64,64),lane*4,0); }
    }
    publish(ws,O_FH+(r3>>2)*256+((r3&3)*8+ct3)*8,tag,w);
    if(b==0 && tid==64) __hip_atomic_store((GAS u64*)(ws+O_EP),tag,__ATOMIC_RELAXED,__HIP_MEMORY_SCOPE_AGENT);
  }
  STAMP(11);
  STAMP(12);
  const int sp=xcd>>1, r2t=(xcd&1)*8+(idx>>2), ct=idx&3;
  const int tq=w&3, kh=w>>2;
  const unsigned rb=smb+kh*73728;
  const u32x4 rw2=mkrs4((const char*)p.wd+(size_t)(sp*36+kh*18)*32*1152+ct*288);
  const u32x4 ra2=mkrs4(ws+O_ACT+((size_t)(r2t*8+tq*2)*144+sp*36+kh*18)*1024);
  const unsigned rbt=rb+tq*1024;
  unsigned wv[3], voff2;
  bf16x8 a2[g2::AR][2];
  auto loadW=[&](auto J,auto X_){ constexpr int j=J.v,x=X_.v;
    if constexpr(x<2) bl<g2::kord(j)*32*1152,(j%8)*9216+4096*x>(rw2,wv[x],rbt);
    else blq<g2::kord(j)*32*1152,(j%8)*9216+8192,j%4>(rw2,wv[2],rb,tq); };
  auto loadA2=[&](auto J,auto T){ constexpr int j=J.v,t=T.v; bload16c<t*144*1024+g2::kord(j)*1024>(a2[j%g2::AR][t],ra2,voff2); };
  auto g2_prologue=[&](){
    const int l2=fresh_lane(); voff2=l2*16;
    #pragma unroll
    for(int i=0;i<3;i++){ const int sub= i<2? tq+4*i:8; const int id=sub*64+l2; const int row=id/18; wv[i]=row*1152+(id-row*18)*16; }
    sfor<0,g2::PRO>([&](auto J){ loadW(J,IC<0>{}); loadW(J,IC<1>{}); loadW(J,IC<2>{}); loadA2(J,IC<0>{}); loadA2(J,IC<1>{}); }); };
  {
    using namespace g1;
    const int t1=r1*16+w*2;
    const char* abase=ws+O_C+(size_t)t1*18*1024;
    if(w==7){ sfor<0,PA1>(ldG1); }
    if(w==0) poll<32>(ws,O_FH+r1*256,tag,lane);
    else sfor<PA1,PA2>(ldG1);
    if(w==7) waitv<PA2-PA1>();
    BAR();
    STAMP(13);
    bf16x8 af[18][2];
    f32x4 sq[2];
    { const char* sp=ws+O_SSQ+(size_t)t1*1024; gload16c<0>(sq[0],sp,voff); gload16c<1024>(sq[1],sp,voff); }
    auto loadA=[&](auto Sx,auto T){ constexpr int s=Sx.v,t=T.v; gload16c<(s&3)*1024>(af[s][t], abase+t*18*1024+(s>>2)*4096, voff); };
    sfor<0,LAK>([&](auto Sx){ loadA(Sx,IC<0>{}); loadA(Sx,IC<1>{}); });
    if(w==0) sfor<PA1,PA2>(ldG1);
    f32x4 acc[4][2][2];
    const unsigned lb0=smb+(4*g+q)*128+((((g&1)<<1)|(q>>1))*32)+pp*8;
    unsigned ab[4];
    bf16x8 wf[PF];
    f32x2 c1; unsigned wmd;
    u32x4 hold[2];
    const rsrc_t ors=mkrs(ws+O_ACT+(size_t)t1*144*1024,2*144*1024);
    const int obase=L0*2*1024;
    const int olast=((fs>>2)*18+16+((fs>>1)&1))*1024+(fs&1)*8;
    auto rdW=[&](auto I){ constexpr int i=I.v, sl=pT(i)&3, s=pS(i), mat=i&1;
      if constexpr(s==0 && mat==0){ if constexpr(i<8) ab[sl]=lb0^(sl*32); else ab[sl]=(ab[(sl+3)&3]-2*57344)^((sl^((sl+3)&3))*32); asm volatile("" : "+v"(ab[sl])); }
      if constexpr((s==7||s==14) && mat==0){ ab[sl]+=57344; asm volatile("" : "+v"(ab[sl])); }
      wf[i%PF]=cat(trread(ab[sl]+(s%7)*8192+mat*4096),trread(ab[sl]+(s%7)*8192+mat*4096+2048)); };
    sfor<0,PF-1>(rdW);
    f32x2 e2,p2;
    auto epi=[&](auto E,auto K){ constexpr int e=E.v,kk=K.v; constexpr int ET=e>>2, t=(e>>1)&1, h=e&1;
      if constexpr(kk==0){ const f32x2 gg={acc[ET&3][0][t][2*h],acc[ET&3][0][t][2*h+1]}, u={acc[ET&3][1][t][2*h],acc[ET&3][1][t][2*h+1]};
        e2=gg*__builtin_shufflevector(c1,c1,t,t); p2=gg*u; asm volatile("" : "+v"(e2),"+v"(p2)); }
      if constexpr(kk==1){ e2[0]=__builtin_amdgcn_exp2f(e2[0]); asm volatile("" : "+v"(e2)); }
      if constexpr(kk==2){ e2[1]=__builtin_amdgcn_exp2f(e2[1]); asm volatile("" : "+v"(e2)); }
      if constexpr(kk==3){ e2=e2+1.f; asm volatile("" : "+v"(e2)); }
      if constexpr(kk==4){ e2[0]=__builtin_amdgcn_rcpf(e2[0]); asm volatile("" : "+v"(e2)); }
      if constexpr(kk==5){ e2[1]=__builtin_amdgcn_rcpf(e2[1]); asm volatile("" : "+v"(e2)); }
      if constexpr(kk==6){
        const f32x2 o=p2*e2;
        constexpr int hi=(ET==8||(ET&1)==0)?0:2;
        hold[t][hi+h]=pk2(o[0],o[1]);
        asm volatile("" : "+v"(hold[t][hi+h]));
        if constexpr(h==1){
          if constexpr(ET==8){ ST8(((u32x2){hold[t][0],hold[t][1]}),ors,voff,olast+t*144*1024); }
          else if constexpr((ET&1)==1){ ST16(hold[t],ors,voff,obase+t*144*1024+(ET>>1)*1024); }
        }
      } };
    auto slot=[&](auto M){ constexpr int m=M.v;
      if constexpr(m>=255 && m<283) epi(IC<(m-255)/7>{},IC<(m-255)%7>{});
      if constexpr(m>=284){ constexpr int e=4+(m-284)/13, d=(m-284)%13; if constexpr(e<32 && d<7) epi(IC<e>{},IC<d>{}); } };
    sfor<0,324>([&](auto I){ constexpr int i=I.v; constexpr int T=pT(i), s=pS(i), mat=i&1;
      if constexpr(i==144) STAMP(14);
      if constexpr(i<126 && i==firstUse(0,s)){
        gwait<nAafter(i,s)+nB(i)>(af[s][0],af[s][1]);
        if constexpr(i==0){ asm volatile("" : "+v"(sq[0]),"+v"(sq[1]));
          #pragma unroll
          for(int t=0;t<2;t++){ const float ss=ssq_sum(sq[t],lane); c1[t]=-1.44269504089f*__builtin_amdgcn_rsqf(ss*(1.f/576.f)+1e-5f); }
          asm volatile("" : "+v"(c1)); } }
      if constexpr(i==40||i==80||i==120||i==146||i==182) BAR();
      if constexpr(i==139){ waitv<nB(139)-10>(); BAR(); }
      if constexpr(i==159){ waitv<nB(159)-15>(); BAR(); }
      if constexpr(i==169||i==240){ waitv<0>(); BAR(); }
      if constexpr(i==240){ if(w==0) flag_store(ws,O_FA+r1*256+fs*8,tag); }
      if constexpr(i==312){ if(w==0) poll<8>(ws,O_FA+((xcd&1)*4+(idx>>3))*256+(xcd>>1)*64,tag,fresh_lane()); }
      if constexpr(i==182){ vC=vA+(8-2*(fs&3))*128+(fs&3)*32; asm volatile("" : "+v"(vC)); }
      if constexpr(i+PF-1<324) rdW(IC<i+PF-1>{});
      SB();
      if constexpr(s==0) acc[T&3][mat][0]=MF16(wf[i%PF],af[s][0],(f32x4){}); else acc[T&3][mat][0]=MF16(wf[i%PF],af[s][0],acc[T&3][mat][0]);
      SB();
      slot(IC<2*i>{});
      SB();
      if constexpr(s==0) acc[T&3][mat][1]=MF16(wf[i%PF],af[s][1],(f32x4){}); else acc[T&3][mat][1]=MF16(wf[i%PF],af[s][1],acc[T&3][mat][1]);
      SB();
      slot(IC<2*i+1>{});
      if constexpr(i==170){ const int l_=fresh_lane(); unsigned o=(w*21+(l_<21?l_:20))*128; if(o>20608) o=20608; gtouch(wmd,(const char*)p.wd+(size_t)b*20736,o); }
      if constexpr(i==241) asm_use(wmd);
      if constexpr(i<40 && i%8==3 && PA2+i/8<18) ldG1(IC<PA2+i/8>{});
      if constexpr(aAt(i)>=0) loadA(IC<aAt(i)/2>{},IC<aAt(i)%2>{});
      if constexpr(rB(i)>=0){ constexpr int r=rB(i); bl<krow(r)*(F*2)+128,r*8192>(r<16?rwA:rwB,vA,wdst); }
      if constexpr(rC(i)>=0){ constexpr int r=rC(i); blm<krow(r)*(F*2),r*8192>(r<16?rwA:rwB,vC,wdst,mC); }
      SB();
    });
    STAMP(15);
    BAR();
    g2_prologue();
    sfor<32,36>([&](auto E){ sfor<0,7>([&](auto K){ epi(E,K); }); });
  }
  STAMP(16);
  {
    using namespace g2;
    const int lane=fresh_lane(); const unsigned voff=lane*16; const int g=lane>>4, q=(lane>>2)&3, pp=lane&3, c16=lane&15;
    f32x4 acc[9][2];
    unsigned lwb[2];
    #pragma unroll
    for(int i=0;i<2;i++){ lwb[i]=rb+(4*g+q)*288+pp*8+i*36864; asm volatile("" : "+v"(lwb[i])); }
    bf16x8 wf[PF];
    #define LDW2(i) cat(trread(lwb[(((i)/9)%8)/4]+((((i)/9)%8)%4)*9216+((i)%9)*32),trread(lwb[(((i)/9)%8)/4]+((((i)/9)%8)%4)*9216+((i)%9)*32+16*288))
    waitv<0>(); BAR();
    if(w==1) flag_store(ws,O_FA2+r1*256+fs*8,tag);
    STAMP(17);
    sfor<0,PF-1>([&](auto I){ wf[I.v]=LDW2(I.v); });
    sfor<0,NP>([&](auto I){ constexpr int i=I.v; constexpr int j=i/9, nt=i%9;
      if constexpr((i+BO)%18==0 && i+BO<NP){
        if constexpr(i+BO==54){ if(w==0) poll<8>(ws,O_FA2+(r2t>>1)*256+sp*64,tag,lane); }
        waitv<vmc(i,-1,(i+BO)/9+1)>();
        BAR(); }
      if constexpr(nt==0) gwait<vmc(i,j,-1)>(a2[j%AR][0],a2[j%AR][1]);
      if constexpr(i+PF-1<NP) wf[(i+PF-1)%PF]=LDW2(i+PF-1);
      SB();
      if constexpr(j==0) acc[nt][0]=MF16(wf[i%PF],a2[j%AR][0],(f32x4){}); else acc[nt][0]=MF16(wf[i%PF],a2[j%AR][0],acc[nt][0]);
      if constexpr(j==0) acc[nt][1]=MF16(wf[i%PF],a2[j%AR][1],(f32x4){}); else acc[nt][1]=MF16(wf[i%PF],a2[j%AR][1],acc[nt][1]);
      SB();
      if constexpr(actAt(i)>=0) loadA2(IC<actAt(i)/2>{},IC<actAt(i)%2>{});
      if constexpr(wAt(i)>=0) loadW(IC<wAt(i)/3>{},IC<wAt(i)%3>{});
      SB();
    });
    STAMP(18);
    BAR();
    { char* xs=sm+(w*9)*1024+voff;
      #pragma unroll
      for(int n=0;n<9;n++){ f32x4 v= kh? acc[n][0]:acc[n][1]; *(f32x4*)(xs+n*1024)=v; } }
    LBAR();
    f32x4 part[10];
    { const char* xr=sm+((w^4)*9)*1024+voff;
      #pragma unroll
      for(int n=0;n<9;n++){ f32x4 mine= kh? acc[n][1]:acc[n][0]; part[n]=mine+*(const f32x4*)(xr+n*1024); }
      part[9]=part[8]; }
    const int grp=r2t*4+ct;
    const rsrc_t xr=mkrs(ws+O_X+(size_t)grp*160*1024,160*1024);
    if(tq!=sp){
      #pragma unroll
      for(int n=0;n<5;n++) ST16(((u32x4){pk2(part[2*n][0],part[2*n][1]),pk2(part[2*n][2],part[2*n][3]),pk2(part[2*n+1][0],part[2*n+1][1]),pk2(part[2*n+1][2],part[2*n+1][3])}),xr,voff,((tq*4+sp)*10+kh*5+n)*1024);
    } else {
      char* xs=sm+(72+kh*10)*1024+voff;
      #pragma unroll
      for(int n=0;n<10;n++) *(f32x4*)(xs+n*1024)=part[n];
    }
    publish(ws,O_FX+grp*128+sp*8,tag,w);
    STAMP(19);
    const int tk0=r2t*128+sp*32;
    const rsrc_t rh=mkrs(ws+O_C+(size_t)(tk0>>4)*18432,2*18432), rq=mkrs(ws+O_SSQ+(size_t)tk0*64,2048), rmh=mkrs(ws+O_MH+(size_t)tk0*4,128);
    u32x2 hv[2][2]; float r2[2], mh[2]; int go[2][2];
    #pragma unroll
    for(int x=0;x<2;x++){ int u=w+8*x; if(u>9) u=9; const int tl=u>=5, pr=u-5*tl;
      #pragma unroll
      for(int y=0;y<2;y++){ int nt=2*pr+y; if(nt>8) nt=8; const int nl=nt*16+g*4;
        go[x][y]=(tl*16+c16)*1152+(ct*144+nl)*2;
        const int c3=ct*2+(nl>=72), o72=nl>=72? nl-72:nl;
        int kc,gp,hfh; if(o72<64){ kc=2*c3+(o72>>5); hfh=(o72>>4)&1; gp=(o72>>2)&3; } else { kc=16+(c3>>2); gp=2*(c3&1)+((o72-64)>>2); hfh=(c3>>1)&1; }
        hv[x][y]=LD8(rh,(tl*18+kc)*1024+(gp*16+c16)*16+hfh*8,0); }
      const f32x4 s4=__builtin_bit_cast(f32x4,LD16(rq,tl*1024+voff,0));
      mh[x]=__uint_as_float(__builtin_amdgcn_raw_buffer_load_b32(rmh,(tl*16+c16)*4,0,16));
      r2[x]=__builtin_amdgcn_rcpf(ssq_sum(s4,lane)*(1.f/576.f)+1e-5f); }
    if(w==0) poll<4>(ws,O_FX+grp*128,tag,lane);
    BAR();
    STAMP(20);
    u32x4 rv[2][3];
    #pragma unroll
    for(int x=0;x<2;x++){ int u=w+8*x; if(u>9) u=9;
      #pragma unroll
      for(int y=0;y<3;y++){ int src=(sp+1+y)&3; rv[x][y]=LD16(xr,voff,((sp*4+src)*10+u)*1024); } }
    char* const ob=(char*)p.out+(size_t)(r2t*128+sp*32)*1152;
    #pragma unroll
    for(int x=0;x<2;x++){ if(x==0 || w<2){ const int u=w+8*x; const int tl=u>=5, pr=u-5*tl;
      #pragma unroll
      for(int y=0;y<2;y++){ if(y==0 || pr<4){
        f32x4 v=*(const f32x4*)(sm+(72+tl*10+2*pr+y)*1024+voff);
        #pragma unroll
        for(int z=0;z<3;z++){ v[0]+=lo16(rv[x][z][2*y]); v[1]+=hi16(rv[x][z][2*y]); v[2]+=lo16(rv[x][z][2*y+1]); v[3]+=hi16(rv[x][z][2*y+1]); }
        u32x2 ov; ov[0]=pk2(v[0]*r2[x]+(lo16(hv[x][y][0])+mh[x]),v[1]*r2[x]+(hi16(hv[x][y][0])+mh[x])); ov[1]=pk2(v[2]*r2[x]+(lo16(hv[x][y][1])+mh[x]),v[3]*r2[x]+(hi16(hv[x][y][1])+mh[x]));
        *(u32x2*)(ob+go[x][y])=ov; } } } }
  }
  STAMP(21);
  STAMP_OUT();
}
}

extern "C" void kernel_launch(void* const* d_in, const int* in_sizes, int n_in,
                              void* d_out, int out_size, void* d_ws, size_t ws_size,
                              hipStream_t stream) {
  using namespace k;
  Params p;
  p.x=(const u16*)d_in[0]; p.wqkv=(const u16*)d_in[1]; p.wo=(const u16*)d_in[2];
  p.wg=(const u16*)d_in[4]; p.wu=(const u16*)d_in[5]; p.wd=(const u16*)d_in[6];
  p.out=(u16*)d_out; p.ws=(char*)d_ws;
  fused_k<<<256,512,0,stream>>>(p);
}
```

```cpp
#include <hip/hip_runtime.h>
#include <hip/hip_bf16.h>
typedef unsigned short u16;
typedef unsigned long long u64;
typedef short s16x4 __attribute__((ext_vector_type(4)));
typedef short s16x8 __attribute__((ext_vector_type(8)));
typedef __bf16 bf16x8 __attribute__((ext_vector_type(8)));
typedef __bf16 bf16x2 __attribute__((ext_vector_type(2)));
typedef float f32x4 __attribute__((ext_vector_type(4)));
typedef float f32x2 __attribute__((ext_vector_type(2)));
typedef unsigned u32x4 __attribute__((ext_vector_type(4)));
typedef unsigned u32x2 __attribute__((ext_vector_type(2)));
#define LAS __attribute__((address_space(3)))
#define GAS __attribute__((address_space(1)))
#define DI __device__ __forceinline__

#define STAMP(i)
#define STAMP_OUT()

#define WARM 21
#define WARM_SLEEP 20
#define STAG 1
#define PRETOUCH 1
#define CODE_KB 48
#define WARM_MODE 1
#define TOUCH_AT 121
#define F0_UNITS 0
#define TOUCH_D 50
#define QSPLIT 24
#define F1_XA 8
#define F1_WA 6
#define P1BAR 36
#define G2_PF 5
#define G2_PRIO 0
#define G2_PRO 4
#define PA1 7
#define PA2 13
#define EPS 1
#define G1_PF 4
#define G1_LAK 5
#define ASP 5
#define F1_LA 6
namespace k {
constexpr int S=2048, D=576, F=4608;
constexpr int STAMP_LDS=156*1024, TAG_LDS=155*1024;
DI int fresh_lane(){ int l; asm volatile("v_mbcnt_lo_u32_b32 %0, -1, 0\n\tv_mbcnt_hi_u32_b32 %0, -1, %0" : "=v"(l)); return l; }
DI u16 f2bf(float f){ __bf16 b=(__bf16)f; return __builtin_bit_cast(u16,b); }
DI unsigned pk2(float lo,float hi){ bf16x2 v; v[0]=(__bf16)lo; v[1]=(__bf16)hi; return __builtin_bit_cast(unsigned,v); }
DI float lo16(unsigned v){ return __uint_as_float(v<<16); }
DI float hi16(unsigned v){ return __uint_as_float(v&0xffff0000u); }
DI float phi(float t){ return (t>0.f? t+1.f: __builtin_amdgcn_exp2f(t*1.44269504089f))+1e-6f; }
DI float bcast_hi(float x){ unsigned a=__float_as_uint(x); auto r=__builtin_amdgcn_permlane32_swap(a,a,false,false); return __uint_as_float(r[1]); }
DI float sum_g(float x){
  unsigned a=__float_as_uint(x); auto r=__builtin_amdgcn_permlane32_swap(a,a,false,false); x=__uint_as_float(r[0])+__uint_as_float(r[1]);
  a=__float_as_uint(x); auto r2=__builtin_amdgcn_permlane16_swap(a,a,false,false); return __uint_as_float(r2[0])+__uint_as_float(r2[1]); }
template<int CTRL> DI float dppf(float x){ return __uint_as_float(__builtin_amdgcn_update_dpp(0u,__float_as_uint(x),CTRL,0xf,0xf,false)); }
DI float ssq_sum(f32x4 v,int){
  const int l=fresh_lane(); const int k=(l>>2)&3;
  #pragma unroll
  for(int r=0;r<4;r++){ float x=v[r]; x+=dppf<0x124>(x); x+=dppf<0x128>(x); v[r]=x; }
  const float t=sum_g(k==0? v[0] : (k==1? v[1] : (k==2? v[2]:v[3])));
  return __uint_as_float(__builtin_amdgcn_ds_bpermute((((l&3)<<2)|k)*4,__float_as_uint(t))); }
DI float dot2(unsigned a,unsigned b,float c){ return __builtin_amdgcn_fdot2_f32_bf16(__builtin_bit_cast(bf16x2,a),__builtin_bit_cast(bf16x2,b),c,false); }
DI s16x4 trread(unsigned a){ return __builtin_amdgcn_ds_read_tr16_b64_v4i16((LAS s16x4*)(size_t)a); }
DI bf16x8 cat(s16x4 a,s16x4 b){ return __builtin_bit_cast(bf16x8,__builtin_shufflevector(a,b,0,1,2,3,4,5,6,7)); }
DI bf16x8 cat2(u32x2 a,u32x2 b){ return __builtin_bit_cast(bf16x8,(u32x4){a[0],a[1],b[0],b[1]}); }
#define BAR() do{ asm volatile("":::"memory"); __builtin_amdgcn_s_barrier(); asm volatile("":::"memory"); }while(0)
#define LBAR() do{ asm volatile("s_waitcnt lgkmcnt(0)":::"memory"); __builtin_amdgcn_s_barrier(); asm volatile("":::"memory"); }while(0)
#define MF16(a,bb,c) __builtin_amdgcn_mfma_f32_16x16x32_bf16(a,bb,c,0,0,0)
#define SB() __builtin_amdgcn_sched_barrier(0)
#define WAITV0() __builtin_amdgcn_s_waitcnt(0x0F70)
template<int I> struct IC{ static constexpr int v=I; };
template<int B,int E,class Fn> DI void sfor(Fn&& f){ if constexpr(B<E){ f(IC<B>{}); sfor<B+1,E>(f); } }
DI void glds16(const void* sbase, unsigned voff, unsigned lds){
  unsigned keep;
  asm volatile("s_mov_b32 %0, m0\n\ts_mov_b32 m0, %3\n\ts_nop 0\n\tglobal_load_lds_dwordx4 %1, %2\n\ts_mov_b32 m0, %0" : "=&s"(keep) : "v"(voff), "s"(sbase), "s"(lds) : "memory");
}
DI void glds16c(const void* sbase, unsigned voff, unsigned lds){
  unsigned keep;
  asm volatile("s_mov_b32 %0, m0\n\ts_mov_b32 m0, %3\n\ts_nop 0\n\tglobal_load_lds_dwordx4 %1, %2 sc1\n\ts_mov_b32 m0, %0" : "=&s"(keep) : "v"(voff), "s"(sbase), "s"(lds) : "memory");
}
DI void glds16m(const void* sbase, unsigned voff, unsigned lds, u64 mask){
  unsigned keep;
  asm volatile("s_mov_b32 %0, m0\n\ts_mov_b32 m0, %3\n\ts_mov_b64 exec, %4\n\tglobal_load_lds_dwordx4 %1, %2\n\ts_mov_b64 exec, -1\n\ts_mov_b32 m0, %0" : "=&s"(keep) : "v"(voff), "s"(sbase), "s"(lds), "s"(mask) : "memory");
}
DI void glds16ms(const void* sbase, unsigned voff, unsigned lds, u64 mask, int skip){
  unsigned keep;
  asm volatile("s_cmp_lg_u32 %5, 0\n\ts_cbranch_scc1 1f\n\ts_mov_b32 %0, m0\n\ts_mov_b32 m0, %3\n\ts_mov_b64 exec, %4\n\tglobal_load_lds_dwordx4 %1, %2\n\ts_mov_b64 exec, -1\n\ts_mov_b32 m0, %0\n1:" : "=&s"(keep) : "v"(voff), "s"(sbase), "s"(lds), "s"(mask), "s"(skip) : "memory","scc");
}
DI u32x4 mkrs4(const void* p){ const u64 a=(u64)p; u32x4 r; r[0]=__builtin_amdgcn_readfirstlane((unsigned)a); r[1]=__builtin_amdgcn_readfirstlane((unsigned)(a>>32)); r[2]=0x7fffffffu; r[3]=0x00020000u; return r; }
template<int SOFF,int LOFF> DI void bl(u32x4 rs,unsigned voff,unsigned lds){ unsigned t;
  asm volatile("s_add_i32 m0, %3, %5\n\ts_mov_b32 %0, %4\n\tbuffer_load_dwordx4 %1, %2, %0 offen lds" : "=&s"(t) : "v"(voff), "s"(rs), "s"(lds), "i"(SOFF), "i"(LOFF) : "memory","scc"); }
template<int SOFF,int LOFF> DI void blm(u32x4 rs,unsigned voff,unsigned lds,u64 mask){ unsigned t;
  asm volatile("s_add_i32 m0, %3, %5\n\ts_mov_b32 %0, %4\n\ts_mov_b64 exec, %6\n\tbuffer_load_dwordx4 %1, %2, %0 offen lds\n\ts_mov_b64 exec, -1" : "=&s"(t) : "v"(voff), "s"(rs), "s"(lds), "i"(SOFF), "i"(LOFF), "s"(mask) : "memory","scc"); }
template<int SOFF,int LOFF,int EQ> DI void blq(u32x4 rs,unsigned voff,unsigned lds,int sel){ unsigned t;
  asm volatile("s_cmp_lg_u32 %6, %7\n\ts_cbranch_scc1 1f\n\ts_add_i32 m0, %3, %5\n\ts_mov_b32 %0, %4\n\tbuffer_load_dwordx4 %1, %2, %0 offen lds\n1:" : "=&s"(t) : "v"(voff), "s"(rs), "s"(lds), "i"(SOFF), "i"(LOFF), "s"(sel), "i"(EQ) : "memory","scc"); }
template<int SOFF,class T> DI void bload16c(T& r,u32x4 rs,unsigned voff){ unsigned t;
  asm volatile("s_mov_b32 %1, %4\n\tbuffer_load_dwordx4 %0, %2, %3, %1 offen sc1" : "=v"(r), "=&s"(t) : "v"(voff), "s"(rs), "i"(SOFF) : "memory"); }
template<int OFF,class T> DI void gload16(T& r,const void* sbase,unsigned voff){ asm volatile("global_load_dwordx4 %0, %1, %2 offset:%3" : "=v"(r) : "v"(voff), "s"(sbase), "i"(OFF) : "memory"); }
template<int OFF,class T> DI void gload16c(T& r,const void* sbase,unsigned voff){ asm volatile("global_load_dwordx4 %0, %1, %2 offset:%3 sc1" : "=v"(r) : "v"(voff), "s"(sbase), "i"(OFF) : "memory"); }
DI void gtouch(unsigned& r,const void* sbase,unsigned voff){ asm volatile("global_load_dword %0, %1, %2" : "=v"(r) : "v"(voff), "s"(sbase) : "memory"); }
DI void asm_use(unsigned r){ asm volatile("" :: "v"(r)); }
template<int N,class T> DI void gwait(T& a,T& b){ asm volatile("s_waitcnt vmcnt(%2)" : "+v"(a),"+v"(b) : "i"(N) : "memory"); }
template<int N> DI void waitv(){ asm volatile("s_waitcnt vmcnt(%0)" :: "i"(N) : "memory"); }
typedef __amdgpu_buffer_rsrc_t rsrc_t;
DI rsrc_t mkrs(const void* p,unsigned bytes){ return __builtin_amdgcn_make_buffer_rsrc((void*)p,0,bytes,0x00020000); }
#define ST16(v,rs,vo,so) __builtin_amdgcn_raw_buffer_store_b128(__builtin_bit_cast(u32x4,v),rs,vo,so,16)
#define ST8(v,rs,vo,so)  __builtin_amdgcn_raw_buffer_store_b64(__builtin_bit_cast(u32x2,v),rs,vo,so,16)
#define ST4(v,rs,vo,so)  __builtin_amdgcn_raw_buffer_store_b32(__float_as_uint(v),rs,vo,so,16)
#define LD16(rs,vo,so) __builtin_amdgcn_raw_buffer_load_b128(rs,vo,so,16)
#define LD8(rs,vo,so)  __builtin_amdgcn_raw_buffer_load_b64(rs,vo,so,16)

constexpr size_t O_EP=0;
constexpr size_t O_F0 =256;
constexpr size_t O_FW =O_F0+2048;
constexpr size_t O_FWO=O_FW+2048;
constexpr size_t O_FKV=O_FWO+1024;
constexpr size_t O_FO =O_FKV+16*256;
constexpr size_t O_FH =O_FO+16*256;
constexpr size_t O_FA =O_FH+8*256;
constexpr size_t O_FA2=O_FA+8*256;
constexpr size_t O_FX =O_FA2+8*256;
constexpr size_t O_KVP=32768;
constexpr size_t O_XN =O_KVP+(size_t)256*8192;
constexpr size_t O_MX =O_XN+(size_t)S*D*2;
constexpr size_t O_WB =O_MX+(size_t)S*4;
constexpr size_t O_WQ =O_WB+4096;
constexpr size_t O_WO =O_WQ+(size_t)16*144*1024;
constexpr size_t O_O  =O_WO+(size_t)8*120*1024;
constexpr size_t O_MU =O_O+(size_t)128*16*1536;
constexpr size_t O_MH =O_MU+(size_t)S*64;
constexpr size_t O_C  =O_MH+(size_t)S*4;
constexpr size_t O_SSQ=O_C+(size_t)S*D*2;
constexpr size_t O_ACT=O_SSQ+(size_t)S*64;
constexpr size_t O_X  =O_ACT+(size_t)S*F*2;
constexpr size_t O_END=O_X+(size_t)64*160*1024;
struct Params { const u16 *x,*wqkv,*wo,*wg,*wu,*wd; u16* out; char* ws; };

DI void publish(char* ws,size_t off,u64 tag,int w){
  asm volatile("s_waitcnt vmcnt(0) lgkmcnt(0)":::"memory");
  BAR();
  if(w==0 && fresh_lane()==0) __hip_atomic_store((GAS u64*)(ws+off),tag,__ATOMIC_RELAXED,__HIP_MEMORY_SCOPE_AGENT);
}
DI void flag_store(char* ws,size_t off,u64 tag){ if(fresh_lane()==0) __hip_atomic_store((GAS u64*)(ws+off),tag,__ATOMIC_RELAXED,__HIP_MEMORY_SCOPE_AGENT); }
DI void pollp(const GAS u64* p,u64 tag){
  for(unsigned spins=0; spins<(1u<<20); ++spins){
    u64 v=__hip_atomic_load(p,__ATOMIC_RELAXED,__HIP_MEMORY_SCOPE_AGENT);
    if(__all(v==tag)) break;
    __builtin_amdgcn_s_sleep(1);
  }
  __builtin_amdgcn_fence(__ATOMIC_ACQUIRE,"wavefront");
}
template<int N> DI void poll(char* ws,size_t off,u64 tag,int lane){ pollp((const GAS u64*)(ws+off)+(lane<N?lane:0),tag); }

namespace f1 {
constexpr int PF=6, XA=F1_XA, WA=F1_WA;
constexpr int wr(int r){ return r<WA? -1 : (r<12? 5*(r-WA)+3 : 53+5*(r-12)); }
constexpr int roundAt(int i){ for(int r=WA;r<18;r++) if(wr(r)==i) return r; return -1; }
constexpr int vmc(int iw,int xs,int pw){
  int cnt=0;
  for(int k=0;k<XA;k++){ if(k<WA){ if(8*k<pw) cnt=0; else cnt++; } if(k<=xs) cnt=0; else cnt++; }
  for(int i=0;i<iw;i++){
    if(i%5==1 && i/5+XA<18){ if(i/5+XA<=xs) cnt=0; else cnt++; }
    int r=roundAt(i); if(r>=0){ if(8*r<pw) cnt=0; else cnt++; }
  }
  return cnt;
}
constexpr int lpos(int p){ return p<90? p : p+12; }
}
namespace g1 {
constexpr int PF=4, LAK=4;
constexpr int pT(int i){ return i<120? (i%8)/2 : (i<144? (i-120)/6 : 4+(i-144)/36); }
constexpr int pS(int i){ return i<120? i/8 : (i<144? 15+((i-120)%6)/2 : ((i-144)%36)/2); }
constexpr int krow(int kc){ return kc<16? 72*(kc>>1)+32*(kc&1) : 288*(kc-16)+64; }
constexpr int iB(int r){ return r<10? 81+4*r : (r<15? 122+3*(r-10) : 147+2*(r-15)); }
constexpr int iA(int s,int t){ return s<LAK? -1 : ASP*(s-LAK)+1+2*t; }
constexpr int aAt(int i){ for(int s=LAK;s<18;s++) for(int t=0;t<2;t++) if(iA(s,t)==i) return s*2+t; return -1; }
constexpr int nAafter(int i,int s){ int n=0; for(int x=s+1;x<18;x++) for(int t=0;t<2;t++) if(iA(x,t)<i) n++; return n; }
constexpr int iC(int r){ return 225+4*r; }
constexpr int rB(int i){ for(int r=0;r<18;r++) if(iB(r)==i) return r; return -1; }
constexpr int rC(int i){ for(int r=0;r<9;r++) if(iC(r)==i) return r; return -1; }
constexpr int nB(int i){ int n=0; for(int r=0;r<18;r++) if(iB(r)<i) n++; return n; }
constexpr int firstUse(int T,int s){ for(int i=0;i<324;i++) if(pT(i)==T&&pS(i)==s) return i; return -1; }
}
namespace g2 {
constexpr int LW=5, PF=G2_PF, BO=8, NP=162, AR=6;
constexpr int kord(int j){ constexpr int t[18]={0,1,4,5,8,9,12,13, 2,3,6,7,10,11,14,15,16,17}; return t[j]; }
constexpr int PRO=G2_PRO;
constexpr int actStep(int j,int t){ return j<PRO? -1 : (j<6? 5*(j-PRO)+2+2*t : ((j>=8&&j<=10)? 50+2*(j-8)+t : (j<8? 9*(j-4)+2+3*t : 9*(j-5)+2+3*t))); }
constexpr int wStep(int j,int x){ return j<PRO? -1 : (j<8? 5*(j-PRO)+(x==2?3:x) : 9*(j-LW)+(x==0?1:(x==1?3:6))); }
constexpr int vmc(int iw,int ja,int jw){
  int best=63;
  for(int tq=0;tq<4;tq++){
    int cnt=0;
    for(int j=0;j<PRO;j++){ int n=2+((j%4)==tq); if(j<=jw) cnt=0; else cnt+=n; if(j<=ja) cnt=0; else cnt+=2; }
    for(int i=0;i<iw;i++){
      for(int j=PRO;j<18;j++) for(int t=0;t<2;t++) if(actStep(j,t)==i){ if(j<=ja) cnt=0; else cnt++; }
      for(int j=PRO;j<18;j++) for(int x=0;x<3;x++) if(wStep(j,x)==i && (x<2||(j%4)==tq)){ if(j<=jw) cnt=0; else cnt++; }
    }
    if(cnt<best) best=cnt;
  }
  return best;
}
constexpr int wAt(int i){ for(int j=PRO;j<18;j++) for(int x=0;x<3;x++) if(wStep(j,x)==i) return j*3+x; return -1; }
constexpr int actAt(int i){ for(int j=PRO;j<18;j++) for(int t=0;t<2;t++) if(actStep(j,t)==i) return j*2+t; return -1; }
}

__global__ __launch_bounds__(512,2) void fused_k(Params p){
  __shared__ __attribute__((aligned(16))) char sm[160*1024];
  u64 pc0; asm volatile("s_getpc_b64 %0" : "=s"(pc0));
  const int tid=threadIdx.x, lane=tid&63, w=__builtin_amdgcn_readfirstlane(tid>>6);
  const int b=blockIdx.x, xcd=b&7, idx=b>>3;
  const unsigned smb=(unsigned)(size_t)(LAS char*)sm;
  const unsigned voff=lane*16;
  const int g=lane>>4, q=(lane>>2)&3, pp=lane&3, c16=lane&15;
  char* const ws=p.ws;
  STAMP(0);
  u64 tag;
  const int hp=(xcd&1)*8+(idx>>2), rt=(xcd>>1)*4+(idx&3);
  unsigned wm[2]={0,0};
  auto touch_gu=[&](int slot,int unit,int lo){
    int ln=unit*64+lane; if(ln>323) ln=323; const int mt=ln>=162; ln-=mt*162;
    const char* src=(const char*)(mt? p.wu : p.wg)+(size_t)b*20736+ln*128;
    asm volatile("s_cmp_lt_u32 %2, %3\n\ts_cbranch_scc1 1f\n\tglobal_load_dword %0, %1, off\n1:" : "+v"(wm[slot]) : "v"(src), "s"(w), "s"(lo) : "memory","scc"); };
  {
    char* const smw=sm+w*16384;
    const unsigned smwb=smb+w*16384;
    if(w<2){
      u32x2 ep; { const unsigned z=0; asm volatile("global_load_dwordx2 %0, %1, %2 sc1" : "=v"(ep) : "v"(z), "s"(ws+(w?(size_t)O_XN:0)) : "memory"); }
      u32x4 v[5];
      { const char* xb=(const char*)p.x+(size_t)(b*8+w*4)*1152; const unsigned v0=g*1152+c16*16, v4=g*1152+(64+(c16&7))*16;
        gload16<0>(v[0],xb,v0); gload16<256>(v[1],xb,v0); gload16<512>(v[2],xb,v0); gload16<768>(v[3],xb,v0); gload16<0>(v[4],xb,v4); }
      asm volatile("s_waitcnt vmcnt(0)" : "+v"(ep),"+v"(v[0]),"+v"(v[1]),"+v"(v[2]),"+v"(v[3]),"+v"(v[4]) :: "memory");
      if(tid==0) *(u32x2*)(sm+TAG_LDS)=ep;
      float s1=0;
      #pragma unroll
      for(int i=0;i<5;i++){ const unsigned one=(i<4||c16<8)?0x3f803f80u:0u;
        #pragma unroll
        for(int j=0;j<4;j++) s1=dot2(v[i][j],one,s1); }
      s1+=__shfl_xor(s1,1); s1+=__shfl_xor(s1,2); s1+=__shfl_xor(s1,4); s1+=__shfl_xor(s1,8);
      const float m=s1*(1.f/576.f);
      float f[5][8]; float s2=0,s2t=0;
      #pragma unroll
      for(int i=0;i<5;i++)
        #pragma unroll
        for(int j=0;j<4;j++){ f[i][2*j]=lo16(v[i][j])-m; f[i][2*j+1]=hi16(v[i][j])-m;
          if(i<4){ s2=fmaf(f[i][2*j],f[i][2*j],s2); s2=fmaf(f[i][2*j+1],f[i][2*j+1],s2); } else { s2t=fmaf(f[i][2*j],f[i][2*j],s2t); s2t=fmaf(f[i][2*j+1],f[i][2*j+1],s2t); } }
      s2+= c16<8? s2t:0.f;
      s2+=__shfl_xor(s2,1); s2+=__shfl_xor(s2,2); s2+=__shfl_xor(s2,4); s2+=__shfl_xor(s2,8);
      const float r=__builtin_amdgcn_rsqf(s2*(1.f/576.f)+1e-5f);
      #pragma unroll
      for(int i=0;i<5;i++){ u32x4 o;
        #pragma unroll
        for(int j=0;j<4;j++) o[j]=pk2(f[i][2*j]*r,f[i][2*j+1]*r);
        *(u32x4*)(smw+(i<4? c16+16*i: 64+(c16&7))*80+g*16)=o; }
      asm volatile("s_waitcnt lgkmcnt(0)":::"memory");
      const rsrc_t rx=mkrs(ws+O_XN+(size_t)(b>>1)*18432+(b&1)*128+w*64,18432);
      #pragma unroll
      for(int i=0;i<5;i++){ const int ch=(lane>>2)+16*i; if(ch<72) ST16(*(const u32x4*)(smw+ch*80+pp*16),rx,ch*256+pp*16,0); }
      const rsrc_t rm=mkrs(ws+O_MX+(size_t)b*32+w*16,16);
      if(c16==0) ST4(m,rm,g*4,0);
    } else if(w<4){
      if(b<144){
        const int s=b>>3, hq=b&7, h2=w-2;
        u32x4 v[8];
        const char* src=(const char*)p.wqkv+(size_t)s*32*3456+hq*144+h2*64;
        int lo_[8];
        #pragma unroll
        for(int i=0;i<8;i++){ const int r=g+4*i; int c=c16<15?c16:14; const int part=c/5, cc=c-part*5; lo_[i]=r*272+c*16; gload16<0>(v[i],src,r*3456+part*1152+cc*16); }
        asm volatile("s_waitcnt vmcnt(0)" : "+v"(v[0]),"+v"(v[1]),"+v"(v[2]),"+v"(v[3]),"+v"(v[4]),"+v"(v[5]),"+v"(v[6]),"+v"(v[7]) :: "memory");
        #pragma unroll
        for(int i=0;i<8;i++) *(u32x4*)(smw+lo_[i])=v[i];
        asm volatile("s_waitcnt lgkmcnt(0)":::"memory");
        const rsrc_t rw=mkrs(ws+O_WQ+(size_t)(hq*2+h2)*144*1024,144*1024);
        #pragma unroll
        for(int t=0;t<8;t++){
          const int vc=(t<3? 16*t:16*(t-3))+4*pp;
          const int col= t<3? (vc<36? vc:0) : (vc<36? 40+vc : (vc<72? 80+vc-36 : 0));
          const unsigned ba=smwb+(8*g+q)*272+(h2*4+col)*2;
          u32x4 o=__builtin_bit_cast(u32x4,cat(trread(ba),trread(ba+4*272)));
          if((t<3? 16*t:16*(t-3))+c16>=(t<3?36:72)) o=(u32x4){0,0,0,0};
          ST16(o,rw,voff,(t<3? 90+s*3+t : s*5+t-3)*1024); }
      } else if(b<240){
        const int jb=b-144, kap=jb>>2, ct=(jb&3)*2+(w-2);
        const int kq=kap/6, kl=kap%6;
        u32x4 v[5];
        const char* src=(const char*)p.wo+ct*144;
        bool okr[5]; int lo_[5];
        #pragma unroll
        for(int i=0;i<5;i++){ int e=lane+64*i; if(e>287) e=287; const int r=e/9, c=e-r*9, row=r&15, hi=r>>4;
          int kr; if(kl<4){ kr=(4*kq+kl)*36+(hi? 12+row: row-4); okr[i]= hi|| row>=4; } else { kr=(4*kq+2*(kl-4)+hi)*36+28+row; okr[i]=row<8; }
          lo_[i]=r*160+c*16; gload16<0>(v[i],src,(okr[i]?kr:0)*1152+c*16); }
        asm volatile("s_waitcnt vmcnt(0)" : "+v"(v[0]),"+v"(v[1]),"+v"(v[2]),"+v"(v[3]),"+v"(v[4]) :: "memory");
        #pragma unroll
        for(int i=0;i<5;i++) *(u32x4*)(smw+lo_[i])= okr[i]? v[i]:(u32x4){0,0,0,0};
        asm volatile("s_waitcnt lgkmcnt(0)":::"memory");
        const rsrc_t rw=mkrs(ws+O_WO+(size_t)ct*120*1024,120*1024);
        #pragma unroll
        for(int nt=0;nt<5;nt++){
          const int col=nt*16+4*((nt==4)?(pp&1):pp);
          const unsigned ba=smwb+(4*g+q)*160+col*2;
          u32x4 o=__builtin_bit_cast(u32x4,cat(trread(ba),trread(ba+16*160)));
          if(nt==4 && c16>=8) o=(u32x4){ (kl<4 && g==0 && (c16&3)==0)? 0x3f803f80u:0u ,0u,0u,0u};
          ST16(o,rw,voff,(kap*5+nt)*1024); }
      }
    } else if(w==4 && rt<9){
      u32x4 v[5];
      const char* rp=(const char*)p.wo+(size_t)(hp*36+rt*4)*1152;
      const unsigned v0=g*1152+c16*16, v4=g*1152+(64+(c16&7))*16;
      gload16<0>(v[0],rp,v0); gload16<256>(v[1],rp,v0); gload16<512>(v[2],rp,v0); gload16<768>(v[3],rp,v0); gload16<0>(v[4],rp,v4);
      asm volatile("s_waitcnt vmcnt(0)" : "+v"(v[0]),"+v"(v[1]),"+v"(v[2]),"+v"(v[3]),"+v"(v[4]) :: "memory");
      float sacc=0;
      #pragma unroll
      for(int i=0;i<5;i++){ const unsigned one=(i<4||c16<8)?0x3f803f80u:0u;
        #pragma unroll
        for(int j=0;j<4;j++) sacc=dot2(v[i][j],one,sacc); }
      sacc+=__shfl_xor(sacc,1); sacc+=__shfl_xor(sacc,2); sacc+=__shfl_xor(sacc,4); sacc+=__shfl_xor(sacc,8);
      if(c16==0) ST4(sacc*(1.f/576.f),mkrs(ws+O_WB+(size_t)(hp*36+rt*4)*4,16),g*4,0);
    }
    if(w==7){
      asm volatile("global_load_dword %0, %1, %2" : "+v"(wm[1]) : "v"(((idx%(CODE_KB/8))*64+lane)*128), "s"(pc0&~(u64)255) : "memory"); }
    else if(w>=5){ __builtin_amdgcn_s_sleep(WARM_SLEEP); touch_gu(1,w==5?4:5,5); }
    STAMP(1);
    if(w<5) asm volatile("s_waitcnt vmcnt(0) lgkmcnt(0)":::"memory");
    BAR();
    { const u32x2 e=*(const u32x2*)(sm+TAG_LDS); unsigned lo=__builtin_amdgcn_readfirstlane(e[0]), hi=__builtin_amdgcn_readfirstlane(e[1]); tag=(((u64)hi<<32)|lo)+1ull; }
    if(tid<2 && (tid==0 || b<240)){
      const size_t fo= tid==0? O_F0+b*8 : (b<144? O_FW+(b&7)*256+(b>>3)*8 : O_FWO+((b-144)&3)*256+((b-144)>>2)*8);
      __hip_atomic_store((GAS u64*)(ws+fo),tag,__ATOMIC_RELAXED,__HIP_MEMORY_SCOPE_AGENT); }
    STAMP(2);
    if(w==0) pollp((const GAS u64*)(ws+(lane<16? O_F0+rt*128+lane*8 : (lane<34? O_FW+(hp>>1)*256+(lane-16)*8 : O_FWO+((idx&7)>>1)*256+(lane<58?lane-34:0)*8))),tag);
    BAR();
  }
  STAMP(3);
  const int tt=rt*8+w;
  const int ct3=idx&7, r3=xcd*4+(idx>>3);
  constexpr int WOL=8*1024;
  bf16x8 q01; u32x2 q2;
  bool okv[2][4]; bool anyok[2]={false,false};
  {
    using namespace f1;
    const char* wbase=ws+O_WQ+(size_t)hp*144*1024+w*1024;
    const char* abase=ws+O_XN+(size_t)tt*18*1024;
    bf16x8 af[18];
    auto ldW=[&](auto R){ constexpr int r=R.v; const int pc=r*8+w; glds16c(wbase+r*8192,voff,smb+((r<11||(r==11&&0))? pc : (pc<90? pc:pc+12))*1024); };
    auto ldX=[&](auto Sx){ constexpr int s=Sx.v; gload16c<(s&3)*1024>(af[s],abase+(s>>2)*4096,voff); };
    sfor<0,XA>([&](auto K){ if constexpr(K.v<WA) ldW(K); ldX(K); });
    f32x4 acc[8];
    unsigned lwb[4];
    #pragma unroll
    for(int i=0;i<4;i++){ lwb[i]=smb+voff+i*49152; asm volatile("" : "+v"(lwb[i])); }
    bf16x8 wf[PF];
    #define LDW1(i) (*(const LAS bf16x8*)((const LAS char*)(size_t)lwb[lpos(i)/48]+(lpos(i)%48)*1024))
    #define WAITP(m,n) asm volatile("s_waitcnt vmcnt(%2)" : "+v"(af[2*(m)]),"+v"(af[2*(m)+1]) : "i"(n) : "memory")
    WAITP(0,vmc(0,1,10)); BAR();
    STAMP(4);
    sfor<0,PF-1>([&](auto I){ wf[I.v]=LDW1(I.v); });
    sfor<0,90>([&](auto I){ constexpr int i=I.v; constexpr int s=i/5,t=i%5;
      if constexpr(i%10==4 && i/10<8){ constexpr int m=i/10+1; WAITP(m,vmc(i,2*m+1,10*(m+1))); BAR(); }
      if constexpr(i+PF-1<90) wf[(i+PF-1)%PF]=LDW1(i+PF-1);
      SB();
      if constexpr(s==0) acc[3+t]=MF16(af[s],wf[i%PF],(f32x4){}); else acc[3+t]=MF16(af[s],wf[i%PF],acc[3+t]);
      SB();
      if constexpr(t==1 && s+XA<18) ldX(IC<s+XA>{});
      if constexpr(roundAt(i)>=0) ldW(IC<roundAt(i)>{});
      SB();
    });
    STAMP(5);
    u32x2 kvb[5];
    #pragma unroll
    for(int t=0;t<5;t++){ float x[4];
      #pragma unroll
      for(int r4=0;r4<4;r4++){ float a=acc[3+t][r4];
        if(t<2) a=phi(a); else if(t==2) a= c16<4? phi(a):a; else if(t==4) a= c16<8? a : 1.f;
        x[r4]=a; }
      kvb[t][0]=pk2(x[0],x[1]); kvb[t][1]=pk2(x[2],x[3]); }
    BAR();
    { char* xs=sm+w*8192+voff;
      #define KVM(idx,kt,vt) *(f32x4*)(xs+(idx)*1024)=__builtin_amdgcn_mfma_f32_16x16x16bf16_1k(__builtin_bit_cast(s16x4,kvb[kt]),__builtin_bit_cast(s16x4,kvb[vt]),(f32x4){},0,0,0)
      KVM(0,0,2); KVM(1,1,2); KVM(2,0,3); KVM(3,1,3); KVM(4,2,3); KVM(5,0,4); KVM(6,1,4); KVM(7,2,4); }
    waitv<0>();
    LBAR();
    touch_gu(0,w-4,4);
    #pragma unroll
    for(int x=0;x<2;x++){ const int tl=(w&3)+4*x;
      const int kt=(tl==0||tl==2||tl==5)?0:((tl==1||tl==3||tl==6)?1:2), vt= tl<2?2:(tl<5?3:4);
      #pragma unroll
      for(int r4=0;r4<4;r4++){ int d=16*kt+4*g+r4, v=16*vt+c16;
        bool ok; if(v<72) ok= v>=36 && d<36 && ((d<18)==(v-36<18)); else ok=(c16==8||c16==12)? d<18 : ((c16==9||c16==13)? (d>=18&&d<36):false);
        okv[x][r4]=ok; anyok[x]|=ok; } }
    if(w<4){
      const rsrc_t rkv=mkrs(ws+O_KVP+(size_t)hp*16*8192,16*8192);
      #pragma unroll
      for(int x=0;x<2;x++){ const int tl=w+4*x;
        f32x4 sacc=*(const f32x4*)(sm+tl*1024+voff);
        #pragma unroll
        for(int ww=1;ww<8;ww++) sacc+=*(const f32x4*)(sm+ww*8192+tl*1024+voff);
        if(anyok[x]) ST16(sacc,rkv,voff,rt*8192+tl*1024); }
    }
    auto qsteps=[&](auto B,auto E){ constexpr int b0=B.v,e0=E.v;
      sfor<b0,(b0+PF-1<e0?b0+PF-1:e0)>([&](auto I){ wf[I.v%PF]=LDW1(I.v); });
      sfor<b0,e0>([&](auto I){ constexpr int i=I.v; constexpr int s=(i-90)/3,t=(i-90)%3;
        if constexpr(i+PF-1<e0) wf[(i+PF-1)%PF]=LDW1(i+PF-1);
        SB();
        if constexpr(s==0) acc[t]=MF16(wf[i%PF],af[s],(f32x4){}); else acc[t]=MF16(wf[i%PF],af[s],acc[t]);
        SB(); }); };
    qsteps(IC<90>{},IC<90+QSPLIT>{});
    if(w<4) waitv<0>();
    BAR();
    if(tid==0) __hip_atomic_store((GAS u64*)(ws+O_FKV+hp*256+rt*8),tag,__ATOMIC_RELAXED,__HIP_MEMORY_SCOPE_AGENT);
    STAMP(6);
    if(w>=4){
      const char* wob=ws+O_WO+(size_t)ct3*120*1024;
      #pragma unroll
      for(int i=0;i<24;i++){ int pc=(w-4)+4*i; if(pc>93) pc=93; glds16c(wob+pc*1024,voff,smb+WOL+pc*1024); } }
    qsteps(IC<90+QSPLIT>{},IC<144>{});
    { u32x4 v; v[0]=pk2(phi(acc[0][0]),phi(acc[0][1])); v[1]=pk2(phi(acc[0][2]),phi(acc[0][3])); v[2]=pk2(phi(acc[1][0]),phi(acc[1][1])); v[3]=pk2(phi(acc[1][2]),phi(acc[1][3]));
      q01=__builtin_bit_cast(bf16x8,v);
      q2[0]=pk2(phi(acc[2][0]),phi(acc[2][1])); q2[1]=pk2(phi(acc[2][2]),phi(acc[2][3])); }
  }
  if(w==0) poll<16>(ws,O_FKV+hp*256,tag,lane);
  BAR();
  STAMP(22);
  if(w<4){
    u32x4 pv[2][16]; u32x4 wb4;
    { const char* pb=ws+O_KVP+(size_t)hp*16*8192+w*1024;
      #pragma unroll
      for(int x=0;x<2;x++){ const unsigned vo=(anyok[x]?voff:0u)+x*4096;
        #pragma unroll
        for(int i=0;i<16;i++) gload16c<0>(pv[x][i],pb+i*8192,vo); }
      gload16c<0>(wb4,ws+O_WB+hp*144,(lane<9?lane:0)*16); }
    #pragma unroll
    for(int x=0;x<2;x++){
      if(x==0) waitv<17>(); else waitv<0>();
      asm volatile("" : "+v"(pv[x][0]),"+v"(pv[x][1]),"+v"(pv[x][2]),"+v"(pv[x][3]),"+v"(pv[x][4]),"+v"(pv[x][5]),"+v"(pv[x][6]),"+v"(pv[x][7]),
         "+v"(pv[x][8]),"+v"(pv[x][9]),"+v"(pv[x][10]),"+v"(pv[x][11]),"+v"(pv[x][12]),"+v"(pv[x][13]),"+v"(pv[x][14]),"+v"(pv[x][15]) :: "memory");
      f32x4 sacc=__builtin_bit_cast(f32x4,pv[x][0]);
      #pragma unroll
      for(int i=1;i<16;i++) sacc+=__builtin_bit_cast(f32x4,pv[x][i]);
      u32x2 pk; pk[0]=pk2(okv[x][0]?sacc[0]:0.f,okv[x][1]?sacc[1]:0.f); pk[1]=pk2(okv[x][2]?sacc[2]:0.f,okv[x][3]?sacc[3]:0.f);
      *(u32x2*)(sm+(w+4*x)*512+lane*8)=pk; }
    asm volatile("" : "+v"(wb4));
    if(w==0 && lane<9) *(u32x4*)(sm+4096+lane*16)=wb4;
  } else {
    __builtin_amdgcn_s_sleep(8);
    const char* wob=ws+O_WO+(size_t)ct3*120*1024;
    #pragma unroll
    for(int i=0;i<7;i++){ int pc=94+(w-4)+4*i; if(pc>119) pc=119; glds16c(wob+pc*1024,voff,smb+WOL+pc*1024); }
  }
  LBAR();
  STAMP(7);
  { const char* kk=sm+lane*8; const u32x2 z2={0,0};
    #define KF(i) (*(const u32x2*)(kk+(i)*512))
    f32x4 A=MF16(cat2(KF(0),KF(1)),q01,(f32x4){});
    f32x4 B=MF16(cat2(KF(2),KF(3)),q01,(f32x4){}); B=MF16(cat2(KF(4),z2),cat2(q2,z2),B);
    f32x4 C=MF16(cat2(KF(5),KF(6)),q01,(f32x4){}); C=MF16(cat2(KF(7),z2),cat2(q2,z2),C);
    const float ra=__builtin_amdgcn_rcpf(bcast_hi(C[0])+1e-6f), rb=__builtin_amdgcn_rcpf(bcast_hi(C[1])+1e-6f);
    const float b01= g<2? ra:rb, b23= g<1? ra:rb, cc= g<2? rb:0.f;
    A*=ra; B[0]*=b01; B[1]*=b01; B[2]*=b23; B[3]*=b23; C*=cc;
    const char* wbl=sm+4096;
    const f32x4 t4=A*(*(const f32x4*)(wbl+(g>0?g-1:0)*16))+B*(*(const f32x4*)(wbl+48+g*16))+C*(*(const f32x4*)(wbl+112+(g<2?g:0)*16));
    const float mu=sum_g((t4[0]+t4[1])+(t4[2]+t4[3]));
    const float muh=lo16((unsigned)f2bf(mu));
    if(g==0){ A[0]=muh; A[1]=mu-muh; }
    const rsrc_t ro=mkrs(ws+O_O+((size_t)tt*16+hp)*1536,1536);
    ST16(((u32x4){pk2(A[0],A[1]),pk2(A[2],A[3]),pk2(B[0],B[1]),pk2(B[2],B[3])}),ro,voff,0);
    ST8(((u32x2){pk2(C[0],C[1]),pk2(C[2],C[3])}),ro,lane*8,1024);
  }
  publish(ws,O_FO+rt*256+hp*8,tag,w);
  asm volatile("" :: "v"(wm[0]),"v"(wm[1]));
  STAMP(8);
  const int r1=(xcd&1)*4+(idx&3), fs=(xcd>>1)*8+(idx>>2);
  const int L0=9*(fs>>2)+2*(fs&3);
  const char* wsrc=(const char*)((w>>2)? p.wu:p.wg)+(size_t)L0*128;
  const unsigned wdst=smb+w*1024;
  unsigned vA,vC; u64 mC;
  { const int r8=lane>>3, pos=lane&7, sl=(pos>>1)^((r8>>1)&3);
    vA=r8*(F*2)+sl*32+(pos&1)*16;
    mC=__ballot(sl<2); }
  const int w3=w&3;
  const u32x4 rwA=mkrs4(wsrc+(size_t)w3*8*(F*2)), rwB=mkrs4(wsrc+(size_t)w3*72*(F*2));
  auto ldG1=[&](auto R){ constexpr int r=R.v; bl<g1::krow(r)*(F*2),r*8192>(r<16?rwA:rwB,vA,wdst); };
  {
    const int th=w&1, kq=w>>1;
    const int tq=th*2+(kq>>1), hf=kq&1;
    const int t3=r3*4+tq;
    constexpr int XS=116*1024;
    bf16x8 wpre[30];
    { const char* rb=sm+WOL+kq*30720+voff;
      #pragma unroll
      for(int i=0;i<30;i++) wpre[i]=*(const bf16x8*)(rb+i*1024); }
    u32x2 xv[3]; unsigned mxu;
    { const char* xb=(const char*)p.x+(size_t)t3*16*1152+ct3*144;
      #pragma unroll
      for(int i=0;i<3;i++){ int nt= hf? 2+i : (i<2? i:1); int n=nt*16+4*g; if(n>=72) n=68;
        asm volatile("global_load_dwordx2 %0, %1, %2" : "=v"(xv[i]) : "v"(c16*1152+n*2), "s"(xb) : "memory"); } }
    LBAR();
    #pragma unroll
    for(int i=0;i<30;i++) asm volatile("" : "+v"(wpre[i]));
    if(w==7){
      poll<16>(ws,O_FO+(r3>>1)*256,tag,lane);
    } else {
      sfor<0,PA1>(ldG1);
    }
    BAR();
    STAMP(9);
    u32x4 oab[2][4]; u32x2 oc[2][4];
    { const char* ob=ws+O_O+((size_t)(r3*4+th*2)*16+kq*4)*1536;
      #pragma unroll
      for(int i=0;i<4;i++){ gload16c<0>(oab[0][i],ob+i*1536,voff); gload16c<0>(oab[1][i],ob+24576+i*1536,voff); }
      #pragma unroll
      for(int i=0;i<4;i++){ asm volatile("global_load_dwordx2 %0, %1, %2 offset:1024 sc1" : "=v"(oc[0][i]) : "v"(lane*8), "s"(ob+i*1536) : "memory");
                            asm volatile("global_load_dwordx2 %0, %1, %2 offset:1024 sc1" : "=v"(oc[1][i]) : "v"(lane*8), "s"(ob+24576+i*1536) : "memory"); }
      asm volatile("global_load_dword %0, %1, %2 sc1" : "=v"(mxu) : "v"(c16*4), "s"(ws+O_MX+(size_t)t3*64) : "memory"); }
    f32x4 acc[2][5]={};
    sfor<0,4>([&](auto KL){ constexpr int kl=KL.v;
      gwait<15-2*kl>(oab[0][kl],oab[1][kl]);
      #pragma unroll
      for(int t=0;t<2;t++)
        #pragma unroll
        for(int n=0;n<5;n++) acc[t][n]=MF16(wpre[kl*5+n],__builtin_bit_cast(bf16x8,oab[t][kl]),acc[t][n]); });
    asm volatile("s_waitcnt vmcnt(0)" : "+v"(oc[0][0]),"+v"(oc[0][1]),"+v"(oc[0][2]),"+v"(oc[0][3]),"+v"(oc[1][0]),"+v"(oc[1][1]),"+v"(oc[1][2]),"+v"(oc[1][3]),"+v"(mxu),"+v"(xv[0]),"+v"(xv[1]),"+v"(xv[2]) :: "memory");
    #pragma unroll
    for(int kl=0;kl<2;kl++)
      #pragma unroll
      for(int t=0;t<2;t++)
        #pragma unroll
        for(int n=0;n<5;n++) acc[t][n]=MF16(wpre[(4+kl)*5+n],cat2(oc[t][2*kl],oc[t][2*kl+1]),acc[t][n]);
    STAMP(10);
    f32x4 a[5];
    { char* xs=sm+XS+w*5120+voff; const bool hi=kq>>1;
      #pragma unroll
      for(int n=0;n<5;n++){ f32x4 u0=acc[0][n],u1=acc[1][n]; asm volatile("" : "+v"(u0),"+v"(u1)); a[n]=hi?u1:u0; *(f32x4*)(xs+n*1024)=hi?u0:u1; } }
    LBAR();
    { const char* xr=sm+XS+(w^4)*5120+voff;
      #pragma unroll
      for(int n=0;n<5;n++) a[n]+=*(const f32x4*)(xr+n*1024); }
    #pragma unroll
    for(int n=0;n<5;n++) asm volatile("" : "+v"(a[n]));
    f32x4 keep[3];
    { char* xs=sm+XS+(w^4)*5120+voff;
      keep[0]=hf?a[2]:a[0]; keep[1]=hf?a[3]:a[1]; keep[2]=a[4];
      *(f32x4*)(xs)=hf?a[0]:a[2]; *(f32x4*)(xs+1024)=hf?a[1]:a[3]; *(f32x4*)(xs+2048)=a[4]; }
    LBAR();
    { const char* xr=sm+XS+(w^6)*5120+voff;
      const rsrc_t rc=mkrs(ws+O_C+(size_t)t3*18*1024,18*1024), rq=mkrs(ws+O_SSQ+(size_t)t3*1024,1024);
      #pragma unroll
      for(int i=0;i<3;i++) keep[i]+=*(const f32x4*)(xr+i*1024);
      const float m=__uint_as_float(mxu)+bcast_hi(keep[2][0]);
      float qs=0; unsigned cp[6];
      #pragma unroll
      for(int i=0;i<3;i++){
        const f32x4 v=keep[i];
        const float c0=(lo16(xv[i][0])+v[0])-m,c1=(hi16(xv[i][0])+v[1])-m,c2=(lo16(xv[i][1])+v[2])-m,c3=(hi16(xv[i][1])+v[3])-m;
        const float qq=(c0*c0+c1*c1)+(c2*c2+c3*c3);
        qs+= (i<2 || (hf && g<2))? qq:0.f;
        cp[2*i]=pk2(c0,c1); cp[2*i+1]=pk2(c2,c3); }
      ST16(((u32x4){cp[0],cp[1],cp[2],cp[3]}),rc,voff,(2*ct3+hf)*1024);
      if(hf && g<2) ST8(((u32x2){cp[4],cp[5]}),rc,((2*(ct3&1)+g)*16+c16)*16+((ct3>>1)&1)*8,(16+(ct3>>2))*1024);
      qs=sum_g(qs);
      if(lane<16){ ST4(qs,rq,lane*4,(ct3*2+hf)*64); if(ct3==0 && hf==0) ST4(m,mkrs(ws+O_MH+(size_t)t3*64,64),lane*4,0); }
    }
    publish(ws,O_FH+(r3>>2)*256+((r3&3)*8+ct3)*8,tag,w);
    if(b==0 && tid==64) __hip_atomic_store((GAS u64*)(ws+O_EP),tag,__ATOMIC_RELAXED,__HIP_MEMORY_SCOPE_AGENT);
  }
  STAMP(11);
  STAMP(12);
  const int sp=xcd>>1, r2t=(xcd&1)*8+(idx>>2), ct=idx&3;
  const int tq=w&3, kh=w>>2;
  const unsigned rb=smb+kh*36864;
  const u32x4 rw2=mkrs4((const char*)p.wd+(size_t)(sp*36+kh*18)*32*1152+ct*288);
  const u32x4 ra2=mkrs4(ws+O_ACT+((size_t)(r2t*8+tq*2)*144+sp*36+kh*18)*1024);
  const unsigned rbt=rb+tq*1024;
  unsigned wv[3], voff2;
  bf16x8 a2[g2::AR][2];
  auto loadW=[&](auto J,auto X_){ constexpr int j=J.v,x=X_.v;
    constexpr int so=((j%8)<4? 73728:0)+(j%4)*9216;
    if constexpr(x<2) bl<g2::kord(j)*32*1152,so+4096*x>(rw2,wv[x],rbt);
    else blq<g2::kord(j)*32*1152,so+8192,j%4>(rw2,wv[2],rb,tq); };
  auto loadA2=[&](auto J,auto T){ constexpr int j=J.v,t=T.v; bload16c<t*144*1024+g2::kord(j)*1024>(a2[j%g2::AR][t],ra2,voff2); };
  auto g2_setup=[&](){
    const int l2=fresh_lane();
    #pragma unroll
    for(int i=0;i<3;i++){ const int sub= i<2? tq+4*i:8; const int id=sub*64+l2; const int row=id/18; wv[i]=row*1152+(id-row*18)*16; } };
  auto g2_prologue=[&](){
    voff2=fresh_lane()*16;
    sfor<0,g2::PRO>([&](auto J){ loadA2(J,IC<0>{}); loadA2(J,IC<1>{}); }); };
  {
    using namespace g1;
    const int t1=r1*16+w*2;
    const char* abase=ws+O_C+(size_t)t1*18*1024;
    if(w==7){ sfor<0,PA1>(ldG1); }
    if(w==0) poll<32>(ws,O_FH+r1*256,tag,lane);
    else sfor<PA1,PA2>(ldG1);
    if(w==7) waitv<PA2-PA1>();
    BAR();
    STAMP(13);
    bf16x8 af[18][2];
    f32x4 sq[2];
    { const char* sp=ws+O_SSQ+(size_t)t1*1024; gload16c<0>(sq[0],sp,voff); gload16c<1024>(sq[1],sp,voff); }
    auto loadA=[&](auto Sx,auto T){ constexpr int s=Sx.v,t=T.v; gload16c<(s&3)*1024>(af[s][t], abase+t*18*1024+(s>>2)*4096, voff); };
    sfor<0,LAK>([&](auto Sx){ loadA(Sx,IC<0>{}); loadA(Sx,IC<1>{}); });
    if(w==0) sfor<PA1,PA2>(ldG1);
    f32x4 acc[4][2][2];
    const unsigned lb0=smb+(4*g+q)*128+((((g&1)<<1)|(q>>1))*32)+pp*8;
    unsigned ab[4];
    bf16x8 wf[PF];
    f32x2 c1; unsigned wmd;
    u32x4 hold[2];
    const rsrc_t ors=mkrs(ws+O_ACT+(size_t)t1*144*1024,2*144*1024);
    const int obase=L0*2*1024;
    const int olast=((fs>>2)*18+16+((fs>>1)&1))*1024+(fs&1)*8;
    auto rdW=[&](auto I){ constexpr int i=I.v, T=pT(i), sl=T&3, s=pS(i), mat=i&1;
      if constexpr(T<8){
        if constexpr(s==0 && mat==0){ if constexpr(i<8) ab[sl]=lb0^(sl*32); else ab[sl]=(ab[(sl+3)&3]-2*57344)^((sl^((sl+3)&3))*32); asm volatile("" : "+v"(ab[sl])); }
        if constexpr((s==7||s==14) && mat==0){ ab[sl]+=57344; asm volatile("" : "+v"(ab[sl])); }
        wf[i%PF]=cat(trread(ab[sl]+(s%7)*8192+mat*4096),trread(ab[sl]+(s%7)*8192+mat*4096+2048));
      } else {
        if constexpr(mat==0){
          if constexpr(s==0){ ab[0]=(ab[3]-2*57344)^96; asm volatile("" : "+v"(ab[0])); }
          if constexpr(s==8||s==17){ ab[0]^=32; asm volatile("" : "+v"(ab[0])); }
          if constexpr(s==16){ ab[0]=(ab[0]^32)+65536; asm volatile("" : "+v"(ab[0])); } }
        constexpr int o=(s<16? (s&7)*8192:0)+mat*4096;
        wf[i%PF]=cat(trread(ab[0]+o),trread(ab[0]+o+2048));
      } };
    sfor<0,PF-1>(rdW);
    f32x2 e2,p2;
    auto epi=[&](auto E,auto K){ constexpr int e=E.v,kk=K.v; constexpr int ET=e>>2, t=(e>>1)&1, h=e&1;
      if constexpr(kk==0){ const f32x2 gg={acc[ET&3][0][t][2*h],acc[ET&3][0][t][2*h+1]}, u={acc[ET&3][1][t][2*h],acc[ET&3][1][t][2*h+1]};
        e2=gg*__builtin_shufflevector(c1,c1,t,t); p2=gg*u; asm volatile("" : "+v"(e2),"+v"(p2)); }
      if constexpr(kk==1){ e2[0]=__builtin_amdgcn_exp2f(e2[0]); asm volatile("" : "+v"(e2)); }
      if constexpr(kk==2){ e2[1]=__builtin_amdgcn_exp2f(e2[1]); asm volatile("" : "+v"(e2)); }
      if constexpr(kk==3){ e2=e2+1.f; asm volatile("" : "+v"(e2)); }
      if constexpr(kk==4){ e2[0]=__builtin_amdgcn_rcpf(e2[0]); asm volatile("" : "+v"(e2)); }
      if constexpr(kk==5){ e2[1]=__builtin_amdgcn_rcpf(e2[1]); asm volatile("" : "+v"(e2)); }
      if constexpr(kk==6){
        const f32x2 o=p2*e2;
        constexpr int hi=(ET==8||(ET&1)==0)?0:2;
        hold[t][hi+h]=pk2(o[0],o[1]);
        asm volatile("" : "+v"(hold[t][hi+h]));
        if constexpr(h==1){
          if constexpr(ET==8){ ST8(((u32x2){hold[t][0],hold[t][1]}),ors,voff,olast+t*144*1024); }
          else if constexpr((ET&1)==1){ ST16(hold[t],ors,voff,obase+t*144*1024+(ET>>1)*1024); }
        }
      } };
    auto slot=[&](auto M){ constexpr int m=M.v;
      if constexpr(m>=255 && m<283) epi(IC<(m-255)/7>{},IC<(m-255)%7>{});
      if constexpr(m>=284){ constexpr int e=4+(m-284)/13, d=(m-284)%13; if constexpr(e<32 && d<7) epi(IC<e>{},IC<d>{}); } };
    sfor<0,324>([&](auto I){ constexpr int i=I.v; constexpr int T=pT(i), s=pS(i), mat=i&1;
      if constexpr(i==144) STAMP(14);
      if constexpr(i<126 && i==firstUse(0,s)){
        gwait<nAafter(i,s)+nB(i)>(af[s][0],af[s][1]);
        if constexpr(i==0){ asm volatile("" : "+v"(sq[0]),"+v"(sq[1]));
          #pragma unroll
          for(int t=0;t<2;t++){ const float ss=ssq_sum(sq[t],lane); c1[t]=-1.44269504089f*__builtin_amdgcn_rsqf(ss*(1.f/576.f)+1e-5f); }
          asm volatile("" : "+v"(c1)); } }
      if constexpr(i==40||i==80||i==120||i==146) BAR();
      if constexpr(i==139){ waitv<nB(139)-10>(); BAR(); }
      if constexpr(i==159){ waitv<nB(159)-15>(); BAR(); }
      if constexpr(i==169||i==224||i==283){ waitv<0>(); BAR(); }
      if constexpr(i==224){ if(w==0) flag_store(ws,O_FA+r1*256+fs*8,tag); }
      if constexpr(i==290){ if(w==0) poll<8>(ws,O_FA+((xcd&1)*4+(idx>>3))*256+(xcd>>1)*64,tag,fresh_lane());
        g2_setup(); BAR(); }
      if constexpr(i==224){ vC=vA+(8-2*(fs&3))*128+(fs&3)*32+((vA&32)? 288*(F*2)-32:0); asm volatile("" : "+v"(vC)); }
      if constexpr(i+PF-1<324) rdW(IC<i+PF-1>{});
      SB();
      if constexpr(s==0) acc[T&3][mat][0]=MF16(wf[i%PF],af[s][0],(f32x4){}); else acc[T&3][mat][0]=MF16(wf[i%PF],af[s][0],acc[T&3][mat][0]);
      SB();
      slot(IC<2*i>{});
      SB();
      if constexpr(s==0) acc[T&3][mat][1]=MF16(wf[i%PF],af[s][1],(f32x4){}); else acc[T&3][mat][1]=MF16(wf[i%PF],af[s][1],acc[T&3][mat][1]);
      SB();
      slot(IC<2*i+1>{});
      if constexpr(i==170){ const int l_=fresh_lane(); unsigned o=(w*21+(l_<21?l_:20))*128; if(o>20608) o=20608; gtouch(wmd,(const char*)p.wd+(size_t)b*20736,o); }
      if constexpr(i==225) asm_use(wmd);
      if constexpr(i>=290 && i<314 && (i&1)==0) loadW(IC<(i-290)/6>{},IC<((i-290)/2)%3>{});
      if constexpr(i<40 && i%8==3 && PA2+i/8<18) ldG1(IC<PA2+i/8>{});
      if constexpr(aAt(i)>=0) loadA(IC<aAt(i)/2>{},IC<aAt(i)%2>{});
      if constexpr(rB(i)>=0){ constexpr int r=rB(i); bl<krow(r)*(F*2)+128,r*8192>(r<16?rwA:rwB,vA,wdst); }
      if constexpr(rC(i)>=0){ constexpr int r=rC(i); blm<krow(r<8?r:16)*(F*2),r*8192>(r<8?rwA:rwB,vC,wdst,mC); }
      SB();
    });
    STAMP(15);
    SB();
    g2_prologue();
    sfor<32,36>([&](auto E){ sfor<0,7>([&](auto K){ epi(E,K); }); });
  }
  STAMP(16);
  {
    using namespace g2;
    const int lane=fresh_lane(); const unsigned voff=lane*16; const int g=lane>>4, q=(lane>>2)&3, pp=lane&3, c16=lane&15;
    f32x4 acc[9][2];
    unsigned lwb[2];
    #pragma unroll
    for(int i=0;i<2;i++){ lwb[i]=rb+(4*g+q)*288+pp*8+(i?0:73728); asm volatile("" : "+v"(lwb[i])); }
    bf16x8 wf[PF];
    #define LDW2(i) cat(trread(lwb[(((i)/9)%8)/4]+((((i)/9)%8)%4)*9216+((i)%9)*32),trread(lwb[(((i)/9)%8)/4]+((((i)/9)%8)%4)*9216+((i)%9)*32+16*288))
    waitv<0>(); BAR();
    if(w==1) flag_store(ws,O_FA2+r1*256+fs*8,tag);
    STAMP(17);
    sfor<0,PF-1>([&](auto I){ wf[I.v]=LDW2(I.v); });
    sfor<0,NP>([&](auto I){ constexpr int i=I.v; constexpr int j=i/9, nt=i%9;
      if constexpr((i+BO)%18==0 && i+BO<NP){
        if constexpr(i+BO==54){ if(w==0) poll<8>(ws,O_FA2+(r2t>>1)*256+sp*64,tag,lane); }
        waitv<vmc(i,-1,(i+BO)/9+1)>();
        BAR(); }
      if constexpr(nt==0) gwait<vmc(i,j,-1)>(a2[j%AR][0],a2[j%AR][1]);
      if constexpr(i+PF-1<NP) wf[(i+PF-1)%PF]=LDW2(i+PF-1);
      SB();
      if constexpr(j==0) acc[nt][0]=MF16(wf[i%PF],a2[j%AR][0],(f32x4){}); else acc[nt][0]=MF16(wf[i%PF],a2[j%AR][0],acc[nt][0]);
      if constexpr(j==0) acc[nt][1]=MF16(wf[i%PF],a2[j%AR][1],(f32x4){}); else acc[nt][1]=MF16(wf[i%PF],a2[j%AR][1],acc[nt][1]);
      SB();
      if constexpr(actAt(i)>=0) loadA2(IC<actAt(i)/2>{},IC<actAt(i)%2>{});
      if constexpr(wAt(i)>=0) loadW(IC<wAt(i)/3>{},IC<wAt(i)%3>{});
      SB();
    });
    STAMP(18);
    BAR();
    { char* xs=sm+(w*9)*1024+voff;
      #pragma unroll
      for(int n=0;n<9;n++){ f32x4 v= kh? acc[n][0]:acc[n][1]; *(f32x4*)(xs+n*1024)=v; } }
    LBAR();
    f32x4 part[10];
    { const char* xr=sm+((w^4)*9)*1024+voff;
      #pragma unroll
      for(int n=0;n<9;n++){ f32x4 mine= kh? acc[n][1]:acc[n][0]; part[n]=mine+*(const f32x4*)(xr+n*1024); }
      part[9]=part[8]; }
    const int grp=r2t*4+ct;
    const rsrc_t xr=mkrs(ws+O_X+(size_t)grp*160*1024,160*1024);
    if(tq!=sp){
      #pragma unroll
      for(int n=0;n<5;n++) ST16(((u32x4){pk2(part[2*n][0],part[2*n][1]),pk2(part[2*n][2],part[2*n][3]),pk2(part[2*n+1][0],part[2*n+1][1]),pk2(part[2*n+1][2],part[2*n+1][3])}),xr,voff,((tq*4+sp)*10+kh*5+n)*1024);
    } else {
      char* xs=sm+(72+kh*10)*1024+voff;
      #pragma unroll
      for(int n=0;n<10;n++) *(f32x4*)(xs+n*1024)=part[n];
    }
    publish(ws,O_FX+grp*128+sp*8,tag,w);
    STAMP(19);
    const int tk0=r2t*128+sp*32;
    const rsrc_t rh=mkrs(ws+O_C+(size_t)(tk0>>4)*18432,2*18432), rq=mkrs(ws+O_SSQ+(size_t)tk0*64,2048), rmh=mkrs(ws+O_MH+(size_t)tk0*4,128);
    u32x2 hv[2][2]; float r2[2], mh[2]; int go[2][2];
    #pragma unroll
    for(int x=0;x<2;x++){ int u=w+8*x; if(u>9) u=9; const int tl=u>=5, pr=u-5*tl;
      #pragma unroll
      for(int y=0;y<2;y++){ int nt=2*pr+y; if(nt>8) nt=8; const int nl=nt*16+g*4;
        go[x][y]=(tl*16+c16)*1152+(ct*144+nl)*2;
        const int c3=ct*2+(nl>=72), o72=nl>=72? nl-72:nl;
        int kc,gp,hfh; if(o72<64){ kc=2*c3+(o72>>5); hfh=(o72>>4)&1; gp=(o72>>2)&3; } else { kc=16+(c3>>2); gp=2*(c3&1)+((o72-64)>>2); hfh=(c3>>1)&1; }
        hv[x][y]=LD8(rh,(tl*18+kc)*1024+(gp*16+c16)*16+hfh*8,0); }
      const f32x4 s4=__builtin_bit_cast(f32x4,LD16(rq,tl*1024+voff,0));
      mh[x]=__uint_as_float(__builtin_amdgcn_raw_buffer_load_b32(rmh,(tl*16+c16)*4,0,16));
      r2[x]=__builtin_amdgcn_rcpf(ssq_sum(s4,lane)*(1.f/576.f)+1e-5f); }
    if(w==0) poll<4>(ws,O_FX+grp*128,tag,lane);
    BAR();
    STAMP(20);
    u32x4 rv[2][3];
    #pragma unroll
    for(int x=0;x<2;x++){ int u=w+8*x; if(u>9) u=9;
      #pragma unroll
      for(int y=0;y<3;y++){ int src=(sp+1+y)&3; rv[x][y]=LD16(xr,voff,((sp*4+src)*10+u)*1024); } }
    char* const ob=(char*)p.out+(size_t)(r2t*128+sp*32)*1152;
    #pragma unroll
    for(int x=0;x<2;x++){ if(x==0 || w<2){ const int u=w+8*x; const int tl=u>=5, pr=u-5*tl;
      #pragma unroll
      for(int y=0;y<2;y++){ if(y==0 || pr<4){
        f32x4 v=*(const f32x4*)(sm+(72+tl*10+2*pr+y)*1024+voff);
        #pragma unroll
        for(int z=0;z<3;z++){ v[0]+=lo16(rv[x][z][2*y]); v[1]+=hi16(rv[x][z][2*y]); v[2]+=lo16(rv[x][z][2*y+1]); v[3]+=hi16(rv[x][z][2*y+1]); }
        u32x2 ov; ov[0]=pk2(v[0]*r2[x]+(lo16(hv[x][y][0])+mh[x]),v[1]*r2[x]+(hi16(hv[x][y][0])+mh[x])); ov[1]=pk2(v[2]*r2[x]+(lo16(hv[x][y][1])+mh[x]),v[3]*r2[x]+(hi16(hv[x][y][1])+mh[x]));
        *(u32x2*)(ob+go[x][y])=ov; } } } }
  }
  STAMP(21);
  STAMP_OUT();
}
}

extern "C" void kernel_launch(void* const* d_in, const int* in_sizes, int n_in,
                              void* d_out, int out_size, void* d_ws, size_t ws_size,
                              hipStream_t stream) {
  using namespace k;
  Params p;
  p.x=(const u16*)d_in[0]; p.wqkv=(const u16*)d_in[1]; p.wo=(const u16*)d_in[2];
  p.wg=(const u16*)d_in[4]; p.wu=(const u16*)d_in[5]; p.wd=(const u16*)d_in[6];
  p.out=(u16*)d_out; p.ws=(char*)d_ws;
  fused_k<<<256,512,0,stream>>>(p);
}
```

```cpp
#include <hip/hip_runtime.h>
#include <hip/hip_bf16.h>
typedef unsigned short u16;
typedef unsigned long long u64;
typedef short s16x4 __attribute__((ext_vector_type(4)));
typedef __bf16 bf16x8 __attribute__((ext_vector_type(8)));
typedef __bf16 bf16x2 __attribute__((ext_vector_type(2)));
typedef float f32x4 __attribute__((ext_vector_type(4)));
typedef float f32x2 __attribute__((ext_vector_type(2)));
typedef unsigned u32x4 __attribute__((ext_vector_type(4)));
typedef unsigned u32x2 __attribute__((ext_vector_type(2)));
#define LAS __attribute__((address_space(3)))
#define GAS __attribute__((address_space(1)))
#define DI __device__ __forceinline__

#define WARM_SLEEP 20
#define QSPLIT 24
#define PA1 7
#define PA2 13

namespace k {
constexpr int S=2048, D=576, F=4608;
constexpr int TAG_LDS=155*1024;
DI int fresh_lane(){ int l; asm volatile("v_mbcnt_lo_u32_b32 %0, -1, 0\n\tv_mbcnt_hi_u32_b32 %0, -1, %0" : "=v"(l)); return l; }
DI u16 f2bf(float f){ __bf16 b=(__bf16)f; return __builtin_bit_cast(u16,b); }
DI unsigned pk2(float lo,float hi){ bf16x2 v; v[0]=(__bf16)lo; v[1]=(__bf16)hi; return __builtin_bit_cast(unsigned,v); }
DI float lo16(unsigned v){ return __uint_as_float(v<<16); }
DI float hi16(unsigned v){ return __uint_as_float(v&0xffff0000u); }
DI float phi(float t){ return (t>0.f? t+1.f: __builtin_amdgcn_exp2f(t*1.44269504089f))+1e-6f; }
DI float bcast_hi(float x){ unsigned a=__float_as_uint(x); auto r=__builtin_amdgcn_permlane32_swap(a,a,false,false); return __uint_as_float(r[1]); }
DI float sum_g(float x){
  unsigned a=__float_as_uint(x); auto r=__builtin_amdgcn_permlane32_swap(a,a,false,false); x=__uint_as_float(r[0])+__uint_as_float(r[1]);
  a=__float_as_uint(x); auto r2=__builtin_amdgcn_permlane16_swap(a,a,false,false); return __uint_as_float(r2[0])+__uint_as_float(r2[1]); }
template<int CTRL> DI float dppf(float x){ return __uint_as_float(__builtin_amdgcn_update_dpp(0u,__float_as_uint(x),CTRL,0xf,0xf,false)); }
DI float ssq_sum(f32x4 v,int){
  const int l=fresh_lane(); const int k=(l>>2)&3;
  #pragma unroll
  for(int r=0;r<4;r++){ float x=v[r]; x+=dppf<0x124>(x); x+=dppf<0x128>(x); v[r]=x; }
  const float t=sum_g(k==0? v[0] : (k==1? v[1] : (k==2? v[2]:v[3])));
  return __uint_as_float(__builtin_amdgcn_ds_bpermute((((l&3)<<2)|k)*4,__float_as_uint(t))); }
DI float dot2(unsigned a,unsigned b,float c){ return __builtin_amdgcn_fdot2_f32_bf16(__builtin_bit_cast(bf16x2,a),__builtin_bit_cast(bf16x2,b),c,false); }
DI s16x4 trread(unsigned a){ return __builtin_amdgcn_ds_read_tr16_b64_v4i16((LAS s16x4*)(size_t)a); }
DI bf16x8 cat(s16x4 a,s16x4 b){ return __builtin_bit_cast(bf16x8,__builtin_shufflevector(a,b,0,1,2,3,4,5,6,7)); }
DI bf16x8 cat2(u32x2 a,u32x2 b){ return __builtin_bit_cast(bf16x8,(u32x4){a[0],a[1],b[0],b[1]}); }
#define BAR() do{ asm volatile("":::"memory"); __builtin_amdgcn_s_barrier(); asm volatile("":::"memory"); }while(0)
#define LBAR() do{ asm volatile("s_waitcnt lgkmcnt(0)":::"memory"); __builtin_amdgcn_s_barrier(); asm volatile("":::"memory"); }while(0)
#define MF16(a,bb,c) __builtin_amdgcn_mfma_f32_16x16x32_bf16(a,bb,c,0,0,0)
#define SB() __builtin_amdgcn_sched_barrier(0)
template<int I> struct IC{ static constexpr int v=I; };
template<int B,int E,class Fn> DI void sfor(Fn&& f){ if constexpr(B<E){ f(IC<B>{}); sfor<B+1,E>(f); } }
DI void glds16c(const void* sbase, unsigned voff, unsigned lds){
  unsigned keep;
  asm volatile("s_mov_b32 %0, m0\n\ts_mov_b32 m0, %3\n\ts_nop 0\n\tglobal_load_lds_dwordx4 %1, %2 sc1\n\ts_mov_b32 m0, %0" : "=&s"(keep) : "v"(voff), "s"(sbase), "s"(lds) : "memory");
}
DI u32x4 mkrs4(const void* p){ const u64 a=(u64)p; u32x4 r; r[0]=__builtin_amdgcn_readfirstlane((unsigned)a); r[1]=__builtin_amdgcn_readfirstlane((unsigned)(a>>32)); r[2]=0x7fffffffu; r[3]=0x00020000u; return r; }
template<int SOFF,int LOFF> DI void bl(u32x4 rs,unsigned voff,unsigned lds){ unsigned t;
  asm volatile("s_add_i32 m0, %3, %5\n\ts_mov_b32 %0, %4\n\tbuffer_load_dwordx4 %1, %2, %0 offen lds" : "=&s"(t) : "v"(voff), "s"(rs), "s"(lds), "i"(SOFF), "i"(LOFF) : "memory","scc"); }
template<int SOFF,int LOFF> DI void blm(u32x4 rs,unsigned voff,unsigned lds,u64 mask){ unsigned t;
  asm volatile("s_add_i32 m0, %3, %5\n\ts_mov_b32 %0, %4\n\ts_mov_b64 exec, %6\n\tbuffer_load_dwordx4 %1, %2, %0 offen lds\n\ts_mov_b64 exec, -1" : "=&s"(t) : "v"(voff), "s"(rs), "s"(lds), "i"(SOFF), "i"(LOFF), "s"(mask) : "memory","scc"); }
template<int SOFF,int LOFF,int EQ> DI void blq(u32x4 rs,unsigned voff,unsigned lds,int sel){ unsigned t;
  asm volatile("s_cmp_lg_u32 %6, %7\n\ts_cbranch_scc1 1f\n\ts_add_i32 m0, %3, %5\n\ts_mov_b32 %0, %4\n\tbuffer_load_dwordx4 %1, %2, %0 offen lds\n1:" : "=&s"(t) : "v"(voff), "s"(rs), "s"(lds), "i"(SOFF), "i"(LOFF), "s"(sel), "i"(EQ) : "memory","scc"); }
template<int SOFF,class T> DI void bload16c(T& r,u32x4 rs,unsigned voff){ unsigned t;
  asm volatile("s_mov_b32 %1, %4\n\tbuffer_load_dwordx4 %0, %2, %3, %1 offen sc1" : "=v"(r), "=&s"(t) : "v"(voff), "s"(rs), "i"(SOFF) : "memory"); }
template<int OFF,class T> DI void gload16(T& r,const void* sbase,unsigned voff){ asm volatile("global_load_dwordx4 %0, %1, %2 offset:%3" : "=v"(r) : "v"(voff), "s"(sbase), "i"(OFF) : "memory"); }
template<int OFF,class T> DI void gload16c(T& r,const void* sbase,unsigned voff){ asm volatile("global_load_dwordx4 %0, %1, %2 offset:%3 sc1" : "=v"(r) : "v"(voff), "s"(sbase), "i"(OFF) : "memory"); }
DI void gtouch(unsigned& r,const void* sbase,unsigned voff){ asm volatile("global_load_dword %0, %1, %2" : "=v"(r) : "v"(voff), "s"(sbase) : "memory"); }
DI void asm_use(unsigned r){ asm volatile("" :: "v"(r)); }
template<int N,class T> DI void gwait(T& a,T& b){ asm volatile("s_waitcnt vmcnt(%2)" : "+v"(a),"+v"(b) : "i"(N) : "memory"); }
template<int N> DI void waitv(){ asm volatile("s_waitcnt vmcnt(%0)" :: "i"(N) : "memory"); }
typedef __amdgpu_buffer_rsrc_t rsrc_t;
DI rsrc_t mkrs(const void* p,unsigned bytes){ return __builtin_amdgcn_make_buffer_rsrc((void*)p,0,bytes,0x00020000); }
#define ST16(v,rs,vo,so) __builtin_amdgcn_raw_buffer_store_b128(__builtin_bit_cast(u32x4,v),rs,vo,so,16)
#define ST8(v,rs,vo,so)  __builtin_amdgcn_raw_buffer_store_b64(__builtin_bit_cast(u32x2,v),rs,vo,so,16)
#define ST4(v,rs,vo,so)  __builtin_amdgcn_raw_buffer_store_b32(__float_as_uint(v),rs,vo,so,16)
#define LD16(rs,vo,so) __builtin_amdgcn_raw_buffer_load_b128(rs,vo,so,16)
#define LD8(rs,vo,so)  __builtin_amdgcn_raw_buffer_load_b64(rs,vo,so,16)

constexpr size_t O_EP=0;
constexpr size_t O_F0 =256;
constexpr size_t O_FW =O_F0+2048;
constexpr size_t O_FWO=O_FW+2048;
constexpr size_t O_FKV=O_FWO+1024;
constexpr size_t O_FO =O_FKV+16*256;
constexpr size_t O_FH =O_FO+16*256;
constexpr size_t O_FA =O_FH+8*256;
constexpr size_t O_FA2=O_FA+8*256;
constexpr size_t O_FX =O_FA2+8*256;
constexpr size_t O_KVP=32768;
constexpr size_t O_XN =O_KVP+(size_t)256*8192;
constexpr size_t O_MX =O_XN+(size_t)S*D*2;
constexpr size_t O_WB =O_MX+(size_t)S*4;
constexpr size_t O_WQ =O_WB+4096;
constexpr size_t O_WO =O_WQ+(size_t)16*144*1024;
constexpr size_t O_O  =O_WO+(size_t)8*120*1024;
constexpr size_t O_MU =O_O+(size_t)128*16*1536;
constexpr size_t O_MH =O_MU+(size_t)S*64;
constexpr size_t O_C  =O_MH+(size_t)S*4;
constexpr size_t O_SSQ=O_C+(size_t)S*D*2;
constexpr size_t O_ACT=O_SSQ+(size_t)S*64;
constexpr size_t O_X  =O_ACT+(size_t)S*F*2;
constexpr size_t O_END=O_X+(size_t)64*160*1024;
struct Params { const u16 *x,*wqkv,*wo,*wg,*wu,*wd; u16* out; char* ws; };

DI void publish(char* ws,size_t off,u64 tag,int w){
  asm volatile("s_waitcnt vmcnt(0) lgkmcnt(0)":::"memory");
  BAR();
  if(w==0 && fresh_lane()==0) __hip_atomic_store((GAS u64*)(ws+off),tag,__ATOMIC_RELAXED,__HIP_MEMORY_SCOPE_AGENT);
}
DI void flag_store(char* ws,size_t off,u64 tag){ if(fresh_lane()==0) __hip_atomic_store((GAS u64*)(ws+off),tag,__ATOMIC_RELAXED,__HIP_MEMORY_SCOPE_AGENT); }
DI void pollp(const GAS u64* p,u64 tag){
  for(unsigned spins=0; spins<(1u<<20); ++spins){
    u64 v=__hip_atomic_load(p,__ATOMIC_RELAXED,__HIP_MEMORY_SCOPE_AGENT);
    if(__all(v==tag)) break;
    __builtin_amdgcn_s_sleep(1);
  }
  __builtin_amdgcn_fence(__ATOMIC_ACQUIRE,"wavefront");
}
template<int N> DI void poll(char* ws,size_t off,u64 tag,int lane){ pollp((const GAS u64*)(ws+off)+(lane<N?lane:0),tag); }

namespace f1 {
constexpr int PF=6, XA=8, WA=6;
constexpr int wr(int r){ return r<WA? -1 : (r<12? 5*(r-WA)+3 : 53+5*(r-12)); }
constexpr int roundAt(int i){ for(int r=WA;r<18;r++) if(wr(r)==i) return r; return -1; }
constexpr int vmc(int iw,int xs,int pw){
  int cnt=0;
  for(int k=0;k<XA;k++){ if(k<WA){ if(8*k<pw) cnt=0; else cnt++; } if(k<=xs) cnt=0; else cnt++; }
  for(int i=0;i<iw;i++){
    if(i%5==1 && i/5+XA<18){ if(i/5+XA<=xs) cnt=0; else cnt++; }
    int r=roundAt(i); if(r>=0){ if(8*r<pw) cnt=0; else cnt++; }
  }
  return cnt;
}
constexpr int lpos(int p){ return p<90? p : p+12; }
}
namespace g1 {
constexpr int PF=4, LAK=4;
constexpr int pT(int i){ return i<120? (i%8)/2 : (i<144? (i-120)/6 : 4+(i-144)/36); }
constexpr int pS(int i){ return i<120? i/8 : (i<144? 15+((i-120)%6)/2 : ((i-144)%36)/2); }
constexpr int krow(int kc){ return kc<16? 72*(kc>>1)+32*(kc&1) : 288*(kc-16)+64; }
constexpr int iB(int r){ return r<10? 81+4*r : (r<15? 122+3*(r-10) : 147+2*(r-15)); }
constexpr int iA(int s,int t){ return s<LAK? -1 : 5*(s-LAK)+1+2*t; }
constexpr int aAt(int i){ for(int s=LAK;s<18;s++) for(int t=0;t<2;t++) if(iA(s,t)==i) return s*2+t; return -1; }
constexpr int nAafter(int i,int s){ int n=0; for(int x=s+1;x<18;x++) for(int t=0;t<2;t++) if(iA(x,t)<i) n++; return n; }
constexpr int iC(int r){ return 225+4*r; }
constexpr int rB(int i){ for(int r=0;r<18;r++) if(iB(r)==i) return r; return -1; }
constexpr int rC(int i){ for(int r=0;r<9;r++) if(iC(r)==i) return r; return -1; }
constexpr int nB(int i){ int n=0; for(int r=0;r<18;r++) if(iB(r)<i) n++; return n; }
constexpr int firstUse(int T,int s){ for(int i=0;i<324;i++) if(pT(i)==T&&pS(i)==s) return i; return -1; }
}
namespace g2 {
constexpr int LW=5, PF=5, BO=8, NP=162, AR=6;
constexpr int kord(int j){ constexpr int t[18]={0,1,4,5,8,9,12,13, 2,3,6,7,10,11,14,15,16,17}; return t[j]; }
constexpr int PRO=4;
constexpr int actStep(int j,int t){ return j<PRO? -1 : (j<6? 5*(j-PRO)+2+2*t : ((j>=8&&j<=10)? 50+2*(j-8)+t : (j<8? 9*(j-4)+2+3*t : 9*(j-5)+2+3*t))); }
constexpr int wStep(int j,int x){ return j<PRO? -1 : (j<8? 5*(j-PRO)+(x==2?3:x) : 9*(j-LW)+(x==0?1:(x==1?3:6))); }
constexpr int vmc(int iw,int ja,int jw){
  int best=63;
  for(int tq=0;tq<4;tq++){
    int cnt=0;
    for(int j=0;j<PRO;j++){ int n=2+((j%4)==tq); if(j<=jw) cnt=0; else cnt+=n; if(j<=ja) cnt=0; else cnt+=2; }
    for(int i=0;i<iw;i++){
      for(int j=PRO;j<18;j++) for(int t=0;t<2;t++) if(actStep(j,t)==i){ if(j<=ja) cnt=0; else cnt++; }
      for(int j=PRO;j<18;j++) for(int x=0;x<3;x++) if(wStep(j,x)==i && (x<2||(j%4)==tq)){ if(j<=jw) cnt=0; else cnt++; }
    }
    if(cnt<best) best=cnt;
  }
  return best;
}
constexpr int wAt(int i){ for(int j=PRO;j<18;j++) for(int x=0;x<3;x++) if(wStep(j,x)==i) return j*3+x; return -1; }
constexpr int actAt(int i){ for(int j=PRO;j<18;j++) for(int t=0;t<2;t++) if(actStep(j,t)==i) return j*2+t; return -1; }
}

__global__ __launch_bounds__(512,2) void fused_k(Params p){
  __shared__ __attribute__((aligned(16))) char sm[160*1024];
  const int tid=threadIdx.x, lane=tid&63, w=__builtin_amdgcn_readfirstlane(tid>>6);
  const int b=blockIdx.x, xcd=b&7, idx=b>>3;
  const unsigned smb=(unsigned)(size_t)(LAS char*)sm;
  const unsigned voff=lane*16;
  const int g=lane>>4, q=(lane>>2)&3, pp=lane&3, c16=lane&15;
  char* const ws=p.ws;
  u64 tag;
  const int hp=(xcd&1)*8+(idx>>2), rt=(xcd>>1)*4+(idx&3);
  unsigned wm[2]={0,0};
  auto touch_gu=[&](int slot,int unit,int lo){
    int ln=unit*64+lane; if(ln>323) ln=323; const int mt=ln>=162; ln-=mt*162;
    const char* src=(const char*)(mt? p.wu : p.wg)+(size_t)b*20736+ln*128;
    asm volatile("s_cmp_lt_u32 %2, %3\n\ts_cbranch_scc1 1f\n\tglobal_load_dword %0, %1, off\n1:" : "+v"(wm[slot]) : "v"(src), "s"(w), "s"(lo) : "memory","scc"); };
  {
    char* const smw=sm+w*16384;
    const unsigned smwb=smb+w*16384;
    if(w<2){
      u32x2 ep; { const unsigned z=0; asm volatile("global_load_dwordx2 %0, %1, %2 sc1" : "=v"(ep) : "v"(z), "s"(ws+(w?(size_t)O_XN:0)) : "memory"); }
      u32x4 v[5];
      { const char* xb=(const char*)p.x+(size_t)(b*8+w*4)*1152; const unsigned v0=g*1152+c16*16, v4=g*1152+(64+(c16&7))*16;
        gload16<0>(v[0],xb,v0); gload16<256>(v[1],xb,v0); gload16<512>(v[2],xb,v0); gload16<768>(v[3],xb,v0); gload16<0>(v[4],xb,v4); }
      asm volatile("s_waitcnt vmcnt(0)" : "+v"(ep),"+v"(v[0]),"+v"(v[1]),"+v"(v[2]),"+v"(v[3]),"+v"(v[4]) :: "memory");
      if(tid==0) *(u32x2*)(sm+TAG_LDS)=ep;
      float s1=0;
      #pragma unroll
      for(int i=0;i<5;i++){ const unsigned one=(i<4||c16<8)?0x3f803f80u:0u;
        #pragma unroll
        for(int j=0;j<4;j++) s1=dot2(v[i][j],one,s1); }
      s1+=__shfl_xor(s1,1); s1+=__shfl_xor(s1,2); s1+=__shfl_xor(s1,4); s1+=__shfl_xor(s1,8);
      const float m=s1*(1.f/576.f);
      float f[5][8]; float s2=0,s2t=0;
      #pragma unroll
      for(int i=0;i<5;i++)
        #pragma unroll
        for(int j=0;j<4;j++){ f[i][2*j]=lo16(v[i][j])-m; f[i][2*j+1]=hi16(v[i][j])-m;
          if(i<4){ s2=fmaf(f[i][2*j],f[i][2*j],s2); s2=fmaf(f[i][2*j+1],f[i][2*j+1],s2); } else { s2t=fmaf(f[i][2*j],f[i][2*j],s2t); s2t=fmaf(f[i][2*j+1],f[i][2*j+1],s2t); } }
      s2+= c16<8? s2t:0.f;
      s2+=__shfl_xor(s2,1); s2+=__shfl_xor(s2,2); s2+=__shfl_xor(s2,4); s2+=__shfl_xor(s2,8);
      const float r=__builtin_amdgcn_rsqf(s2*(1.f/576.f)+1e-5f);
      #pragma unroll
      for(int i=0;i<5;i++){ u32x4 o;
        #pragma unroll
        for(int j=0;j<4;j++) o[j]=pk2(f[i][2*j]*r,f[i][2*j+1]*r);
        *(u32x4*)(smw+(i<4? c16+16*i: 64+(c16&7))*80+g*16)=o; }
      asm volatile("s_waitcnt lgkmcnt(0)":::"memory");
      const rsrc_t rx=mkrs(ws+O_XN+(size_t)(b>>1)*18432+(b&1)*128+w*64,18432);
      #pragma unroll
      for(int i=0;i<5;i++){ const int ch=(lane>>2)+16*i; if(ch<72) ST16(*(const u32x4*)(smw+ch*80+pp*16),rx,ch*256+pp*16,0); }
      const rsrc_t rm=mkrs(ws+O_MX+(size_t)b*32+w*16,16);
      if(c16==0) ST4(m,rm,g*4,0);
    } else if(w<4){
      if(b<144){
        const int s=b>>3, hq=b&7, h2=w-2;
        u32x4 v[8];
        const char* src=(const char*)p.wqkv+(size_t)s*32*3456+hq*144+h2*64;
        int lo_[8];
        #pragma unroll
        for(int i=0;i<8;i++){ const int r=g+4*i; int c=c16<15?c16:14; const int part=c/5, cc=c-part*5; lo_[i]=r*272+c*16; gload16<0>(v[i],src,r*3456+part*1152+cc*16); }
        asm volatile("s_waitcnt vmcnt(0)" : "+v"(v[0]),"+v"(v[1]),"+v"(v[2]),"+v"(v[3]),"+v"(v[4]),"+v"(v[5]),"+v"(v[6]),"+v"(v[7]) :: "memory");
        #pragma unroll
        for(int i=0;i<8;i++) *(u32x4*)(smw+lo_[i])=v[i];
        asm volatile("s_waitcnt lgkmcnt(0)":::"memory");
        const rsrc_t rw=mkrs(ws+O_WQ+(size_t)(hq*2+h2)*144*1024,144*1024);
        #pragma unroll
        for(int t=0;t<8;t++){
          const int vc=(t<3? 16*t:16*(t-3))+4*pp;
          const int col= t<3? (vc<36? vc:0) : (vc<36? 40+vc : (vc<72? 80+vc-36 : 0));
          const unsigned ba=smwb+(8*g+q)*272+(h2*4+col)*2;
          u32x4 o=__builtin_bit_cast(u32x4,cat(trread(ba),trread(ba+4*272)));
          if((t<3? 16*t:16*(t-3))+c16>=(t<3?36:72)) o=(u32x4){0,0,0,0};
          ST16(o,rw,voff,(t<3? 90+s*3+t : s*5+t-3)*1024); }
      } else if(b<240){
        const int jb=b-144, kap=jb>>2, ct=(jb&3)*2+(w-2);
        const int kq=kap/6, kl=kap%6;
        u32x4 v[5];
        const char* src=(const char*)p.wo+ct*144;
        bool okr[5]; int lo_[5];
        #pragma unroll
        for(int i=0;i<5;i++){ int e=lane+64*i; if(e>287) e=287; const int r=e/9, c=e-r*9, row=r&15, hi=r>>4;
          int kr; if(kl<4){ kr=(4*kq+kl)*36+(hi? 12+row: row-4); okr[i]= hi|| row>=4; } else { kr=(4*kq+2*(kl-4)+hi)*36+28+row; okr[i]=row<8; }
          lo_[i]=r*160+c*16; gload16<0>(v[i],src,(okr[i]?kr:0)*1152+c*16); }
        asm volatile("s_waitcnt vmcnt(0)" : "+v"(v[0]),"+v"(v[1]),"+v"(v[2]),"+v"(v[3]),"+v"(v[4]) :: "memory");
        #pragma unroll
        for(int i=0;i<5;i++) *(u32x4*)(smw+lo_[i])= okr[i]? v[i]:(u32x4){0,0,0,0};
        asm volatile("s_waitcnt lgkmcnt(0)":::"memory");
        const rsrc_t rw=mkrs(ws+O_WO+(size_t)ct*120*1024,120*1024);
        #pragma unroll
        for(int nt=0;nt<5;nt++){
          const int col=nt*16+4*((nt==4)?(pp&1):pp);
          const unsigned ba=smwb+(4*g+q)*160+col*2;
          u32x4 o=__builtin_bit_cast(u32x4,cat(trread(ba),trread(ba+16*160)));
          if(nt==4 && c16>=8) o=(u32x4){ (kl<4 && g==0 && (c16&3)==0)? 0x3f803f80u:0u ,0u,0u,0u};
          ST16(o,rw,voff,(kap*5+nt)*1024); }
      }
    } else if(w==4 && rt<9){
      u32x4 v[5];
      const char* rp=(const char*)p.wo+(size_t)(hp*36+rt*4)*1152;
      const unsigned v0=g*1152+c16*16, v4=g*1152+(64+(c16&7))*16;
      gload16<0>(v[0],rp,v0); gload16<256>(v[1],rp,v0); gload16<512>(v[2],rp,v0); gload16<768>(v[3],rp,v0); gload16<0>(v[4],rp,v4);
      asm volatile("s_waitcnt vmcnt(0)" : "+v"(v[0]),"+v"(v[1]),"+v"(v[2]),"+v"(v[3]),"+v"(v[4]) :: "memory");
      float sacc=0;
      #pragma unroll
      for(int i=0;i<5;i++){ const unsigned one=(i<4||c16<8)?0x3f803f80u:0u;
        #pragma unroll
        for(int j=0;j<4;j++) sacc=dot2(v[i][j],one,sacc); }
      sacc+=__shfl_xor(sacc,1); sacc+=__shfl_xor(sacc,2); sacc+=__shfl_xor(sacc,4); sacc+=__shfl_xor(sacc,8);
      if(c16==0) ST4(sacc*(1.f/576.f),mkrs(ws+O_WB+(size_t)(hp*36+rt*4)*4,16),g*4,0);
    }
    if(w>=5){ __builtin_amdgcn_s_sleep(WARM_SLEEP); touch_gu(1,w==5?4:5,5); }
    if(w<5) asm volatile("s_waitcnt vmcnt(0) lgkmcnt(0)":::"memory");
    BAR();
    { const u32x2 e=*(const u32x2*)(sm+TAG_LDS); unsigned lo=__builtin_amdgcn_readfirstlane(e[0]), hi=__builtin_amdgcn_readfirstlane(e[1]); tag=(((u64)hi<<32)|lo)+1ull; }
    if(tid<2 && (tid==0 || b<240)){
      const size_t fo= tid==0? O_F0+b*8 : (b<144? O_FW+(b&7)*256+(b>>3)*8 : O_FWO+((b-144)&3)*256+((b-144)>>2)*8);
      __hip_atomic_store((GAS u64*)(ws+fo),tag,__ATOMIC_RELAXED,__HIP_MEMORY_SCOPE_AGENT); }
    if(w==0) pollp((const GAS u64*)(ws+(lane<16? O_F0+rt*128+lane*8 : (lane<34? O_FW+(hp>>1)*256+(lane-16)*8 : O_FWO+((idx&7)>>1)*256+(lane<58?lane-34:0)*8))),tag);
    BAR();
  }
  const int tt=rt*8+w;
  const int ct3=idx&7, r3=xcd*4+(idx>>3);
  constexpr int WOL=8*1024;
  bf16x8 q01; u32x2 q2;
  bool okv[2][4]; bool anyok[2]={false,false};
  {
    using namespace f1;
    const char* wbase=ws+O_WQ+(size_t)hp*144*1024+w*1024;
    const char* abase=ws+O_XN+(size_t)tt*18*1024;
    bf16x8 af[18];
    auto ldW=[&](auto R){ constexpr int r=R.v; const int pc=r*8+w; glds16c(wbase+r*8192,voff,smb+((r<11||(r==11&&0))? pc : (pc<90? pc:pc+12))*1024); };
    auto ldX=[&](auto Sx){ constexpr int s=Sx.v; gload16c<(s&3)*1024>(af[s],abase+(s>>2)*4096,voff); };
    sfor<0,XA>([&](auto K){ if constexpr(K.v<WA) ldW(K); ldX(K); });
    f32x4 acc[8];
    unsigned lwb[4];
    #pragma unroll
    for(int i=0;i<4;i++){ lwb[i]=smb+voff+i*49152; asm volatile("" : "+v"(lwb[i])); }
    bf16x8 wf[PF];
    #define LDW1(i) (*(const LAS bf16x8*)((const LAS char*)(size_t)lwb[lpos(i)/48]+(lpos(i)%48)*1024))
    #define WAITP(m,n) asm volatile("s_waitcnt vmcnt(%2)" : "+v"(af[2*(m)]),"+v"(af[2*(m)+1]) : "i"(n) : "memory")
    WAITP(0,vmc(0,1,10)); BAR();
    sfor<0,PF-1>([&](auto I){ wf[I.v]=LDW1(I.v); });
    sfor<0,90>([&](auto I){ constexpr int i=I.v; constexpr int s=i/5,t=i%5;
      if constexpr(i%10==4 && i/10<8){ constexpr int m=i/10+1; WAITP(m,vmc(i,2*m+1,10*(m+1))); BAR(); }
      if constexpr(i+PF-1<90) wf[(i+PF-1)%PF]=LDW1(i+PF-1);
      SB();
      if constexpr(s==0) acc[3+t]=MF16(af[s],wf[i%PF],(f32x4){}); else acc[3+t]=MF16(af[s],wf[i%PF],acc[3+t]);
      SB();
      if constexpr(t==1 && s+XA<18) ldX(IC<s+XA>{});
      if constexpr(roundAt(i)>=0) ldW(IC<roundAt(i)>{});
      SB();
    });
    u32x2 kvb[5];
    #pragma unroll
    for(int t=0;t<5;t++){ float x[4];
      #pragma unroll
      for(int r4=0;r4<4;r4++){ float a=acc[3+t][r4];
        if(t<2) a=phi(a); else if(t==2) a= c16<4? phi(a):a; else if(t==4) a= c16<8? a : 1.f;
        x[r4]=a; }
      kvb[t][0]=pk2(x[0],x[1]); kvb[t][1]=pk2(x[2],x[3]); }
    BAR();
    { char* xs=sm+w*8192+voff;
      #define KVM(idx,kt,vt) *(f32x4*)(xs+(idx)*1024)=__builtin_amdgcn_mfma_f32_16x16x16bf16_1k(__builtin_bit_cast(s16x4,kvb[kt]),__builtin_bit_cast(s16x4,kvb[vt]),(f32x4){},0,0,0)
      KVM(0,0,2); KVM(1,1,2); KVM(2,0,3); KVM(3,1,3); KVM(4,2,3); KVM(5,0,4); KVM(6,1,4); KVM(7,2,4); }
    waitv<0>();
    LBAR();
    touch_gu(0,w-4,4);
    #pragma unroll
    for(int x=0;x<2;x++){ const int tl=(w&3)+4*x;
      const int kt=(tl==0||tl==2||tl==5)?0:((tl==1||tl==3||tl==6)?1:2), vt= tl<2?2:(tl<5?3:4);
      #pragma unroll
      for(int r4=0;r4<4;r4++){ int d=16*kt+4*g+r4, v=16*vt+c16;
        bool ok; if(v<72) ok= v>=36 && d<36 && ((d<18)==(v-36<18)); else ok=(c16==8||c16==12)? d<18 : ((c16==9||c16==13)? (d>=18&&d<36):false);
        okv[x][r4]=ok; anyok[x]|=ok; } }
    if(w<4){
      const rsrc_t rkv=mkrs(ws+O_KVP+(size_t)hp*16*8192,16*8192);
      #pragma unroll
      for(int x=0;x<2;x++){ const int tl=w+4*x;
        f32x4 sacc=*(const f32x4*)(sm+tl*1024+voff);
        #pragma unroll
        for(int ww=1;ww<8;ww++) sacc+=*(const f32x4*)(sm+ww*8192+tl*1024+voff);
        if(anyok[x]) ST16(sacc,rkv,voff,rt*8192+tl*1024); }
    }
    auto qsteps=[&](auto B,auto E){ constexpr int b0=B.v,e0=E.v;
      sfor<b0,(b0+PF-1<e0?b0+PF-1:e0)>([&](auto I){ wf[I.v%PF]=LDW1(I.v); });
      sfor<b0,e0>([&](auto I){ constexpr int i=I.v; constexpr int s=(i-90)/3,t=(i-90)%3;
        if constexpr(i+PF-1<e0) wf[(i+PF-1)%PF]=LDW1(i+PF-1);
        SB();
        if constexpr(s==0) acc[t]=MF16(wf[i%PF],af[s],(f32x4){}); else acc[t]=MF16(wf[i%PF],af[s],acc[t]);
        SB(); }); };
    qsteps(IC<90>{},IC<90+QSPLIT>{});
    if(w<4) waitv<0>();
    BAR();
    if(tid==0) __hip_atomic_store((GAS u64*)(ws+O_FKV+hp*256+rt*8),tag,__ATOMIC_RELAXED,__HIP_MEMORY_SCOPE_AGENT);
    if(w>=4){
      const char* wob=ws+O_WO+(size_t)ct3*120*1024;
      #pragma unroll
      for(int i=0;i<24;i++){ int pc=(w-4)+4*i; if(pc>93) pc=93; glds16c(wob+pc*1024,voff,smb+WOL+pc*1024); } }
    qsteps(IC<90+QSPLIT>{},IC<144>{});
    { u32x4 v; v[0]=pk2(phi(acc[0][0]),phi(acc[0][1])); v[1]=pk2(phi(acc[0][2]),phi(acc[0][3])); v[2]=pk2(phi(acc[1][0]),phi(acc[1][1])); v[3]=pk2(phi(acc[1][2]),phi(acc[1][3]));
      q01=__builtin_bit_cast(bf16x8,v);
      q2[0]=pk2(phi(acc[2][0]),phi(acc[2][1])); q2[1]=pk2(phi(acc[2][2]),phi(acc[2][3])); }
  }
  if(w==0) poll<16>(ws,O_FKV+hp*256,tag,lane);
  BAR();
  if(w<4){
    u32x4 pv[2][16]; u32x4 wb4;
    { const char* pb=ws+O_KVP+(size_t)hp*16*8192+w*1024;
      #pragma unroll
      for(int x=0;x<2;x++){ const unsigned vo=(anyok[x]?voff:0u)+x*4096;
        #pragma unroll
        for(int i=0;i<16;i++) gload16c<0>(pv[x][i],pb+i*8192,vo); }
      gload16c<0>(wb4,ws+O_WB+hp*144,(lane<9?lane:0)*16); }
    #pragma unroll
    for(int x=0;x<2;x++){
      if(x==0) waitv<17>(); else waitv<0>();
      asm volatile("" : "+v"(pv[x][0]),"+v"(pv[x][1]),"+v"(pv[x][2]),"+v"(pv[x][3]),"+v"(pv[x][4]),"+v"(pv[x][5]),"+v"(pv[x][6]),"+v"(pv[x][7]),
         "+v"(pv[x][8]),"+v"(pv[x][9]),"+v"(pv[x][10]),"+v"(pv[x][11]),"+v"(pv[x][12]),"+v"(pv[x][13]),"+v"(pv[x][14]),"+v"(pv[x][15]) :: "memory");
      f32x4 sacc=__builtin_bit_cast(f32x4,pv[x][0]);
      #pragma unroll
      for(int i=1;i<16;i++) sacc+=__builtin_bit_cast(f32x4,pv[x][i]);
      u32x2 pk; pk[0]=pk2(okv[x][0]?sacc[0]:0.f,okv[x][1]?sacc[1]:0.f); pk[1]=pk2(okv[x][2]?sacc[2]:0.f,okv[x][3]?sacc[3]:0.f);
      *(u32x2*)(sm+(w+4*x)*512+lane*8)=pk; }
    asm volatile("" : "+v"(wb4));
    if(w==0 && lane<9) *(u32x4*)(sm+4096+lane*16)=wb4;
  } else {
    __builtin_amdgcn_s_sleep(8);
    const char* wob=ws+O_WO+(size_t)ct3*120*1024;
    #pragma unroll
    for(int i=0;i<7;i++){ int pc=94+(w-4)+4*i; if(pc>119) pc=119; glds16c(wob+pc*1024,voff,smb+WOL+pc*1024); }
  }
  LBAR();
  { const char* kk=sm+lane*8; const u32x2 z2={0,0};
    #define KF(i) (*(const u32x2*)(kk+(i)*512))
    f32x4 A=MF16(cat2(KF(0),KF(1)),q01,(f32x4){});
    f32x4 B=MF16(cat2(KF(2),KF(3)),q01,(f32x4){}); B=MF16(cat2(KF(4),z2),cat2(q2,z2),B);
    f32x4 C=MF16(cat2(KF(5),KF(6)),q01,(f32x4){}); C=MF16(cat2(KF(7),z2),cat2(q2,z2),C);
    const float ra=__builtin_amdgcn_rcpf(bcast_hi(C[0])+1e-6f), rb=__builtin_amdgcn_rcpf(bcast_hi(C[1])+1e-6f);
    const float b01= g<2? ra:rb, b23= g<1? ra:rb, cc= g<2? rb:0.f;
    A*=ra; B[0]*=b01; B[1]*=b01; B[2]*=b23; B[3]*=b23; C*=cc;
    const char* wbl=sm+4096;
    const f32x4 t4=A*(*(const f32x4*)(wbl+(g>0?g-1:0)*16))+B*(*(const f32x4*)(wbl+48+g*16))+C*(*(const f32x4*)(wbl+112+(g<2?g:0)*16));
    const float mu=sum_g((t4[0]+t4[1])+(t4[2]+t4[3]));
    const float muh=lo16((unsigned)f2bf(mu));
    if(g==0){ A[0]=muh; A[1]=mu-muh; }
    const rsrc_t ro=mkrs(ws+O_O+((size_t)tt*16+hp)*1536,1536);
    ST16(((u32x4){pk2(A[0],A[1]),pk2(A[2],A[3]),pk2(B[0],B[1]),pk2(B[2],B[3])}),ro,voff,0);
    ST8(((u32x2){pk2(C[0],C[1]),pk2(C[2],C[3])}),ro,lane*8,1024);
  }
  publish(ws,O_FO+rt*256+hp*8,tag,w);
  asm volatile("" :: "v"(wm[0]),"v"(wm[1]));
  const int r1=(xcd&1)*4+(idx&3), fs=(xcd>>1)*8+(idx>>2);
  const int L0=9*(fs>>2)+2*(fs&3);
  const char* wsrc=(const char*)((w>>2)? p.wu:p.wg)+(size_t)L0*128;
  const unsigned wdst=smb+w*1024;
  unsigned vA,vC; u64 mC;
  { const int r8=lane>>3, pos=lane&7, sl=(pos>>1)^((r8>>1)&3);
    vA=r8*(F*2)+sl*32+(pos&1)*16;
    mC=__ballot(sl<2); }
  const int w3=w&3;
  const u32x4 rwA=mkrs4(wsrc+(size_t)w3*8*(F*2)), rwB=mkrs4(wsrc+(size_t)w3*72*(F*2));
  auto ldG1=[&](auto R){ constexpr int r=R.v; bl<g1::krow(r)*(F*2),r*8192>(r<16?rwA:rwB,vA,wdst); };
  {
    const int th=w&1, kq=w>>1;
    const int tq=th*2+(kq>>1), hf=kq&1;
    const int t3=r3*4+tq;
    constexpr int XS=116*1024;
    bf16x8 wpre[30];
    { const char* rb=sm+WOL+kq*30720+voff;
      #pragma unroll
      for(int i=0;i<30;i++) wpre[i]=*(const bf16x8*)(rb+i*1024); }
    u32x2 xv[3]; unsigned mxu;
    { const char* xb=(const char*)p.x+(size_t)t3*16*1152+ct3*144;
      #pragma unroll
      for(int i=0;i<3;i++){ int nt= hf? 2+i : (i<2? i:1); int n=nt*16+4*g; if(n>=72) n=68;
        asm volatile("global_load_dwordx2 %0, %1, %2" : "=v"(xv[i]) : "v"(c16*1152+n*2), "s"(xb) : "memory"); } }
    LBAR();
    #pragma unroll
    for(int i=0;i<30;i++) asm volatile("" : "+v"(wpre[i]));
    if(w==7){
      poll<16>(ws,O_FO+(r3>>1)*256,tag,lane);
    } else {
      sfor<0,PA1>(ldG1);
    }
    BAR();
    u32x4 oab[2][4]; u32x2 oc[2][4];
    { const char* ob=ws+O_O+((size_t)(r3*4+th*2)*16+kq*4)*1536;
      #pragma unroll
      for(int i=0;i<4;i++){ gload16c<0>(oab[0][i],ob+i*1536,voff); gload16c<0>(oab[1][i],ob+24576+i*1536,voff); }
      #pragma unroll
      for(int i=0;i<4;i++){ asm volatile("global_load_dwordx2 %0, %1, %2 offset:1024 sc1" : "=v"(oc[0][i]) : "v"(lane*8), "s"(ob+i*1536) : "memory");
                            asm volatile("global_load_dwordx2 %0, %1, %2 offset:1024 sc1" : "=v"(oc[1][i]) : "v"(lane*8), "s"(ob+24576+i*1536) : "memory"); }
      asm volatile("global_load_dword %0, %1, %2 sc1" : "=v"(mxu) : "v"(c16*4), "s"(ws+O_MX+(size_t)t3*64) : "memory"); }
    f32x4 acc[2][5]={};
    sfor<0,4>([&](auto KL){ constexpr int kl=KL.v;
      gwait<15-2*kl>(oab[0][kl],oab[1][kl]);
      #pragma unroll
      for(int t=0;t<2;t++)
        #pragma unroll
        for(int n=0;n<5;n++) acc[t][n]=MF16(wpre[kl*5+n],__builtin_bit_cast(bf16x8,oab[t][kl]),acc[t][n]); });
    asm volatile("s_waitcnt vmcnt(0)" : "+v"(oc[0][0]),"+v"(oc[0][1]),"+v"(oc[0][2]),"+v"(oc[0][3]),"+v"(oc[1][0]),"+v"(oc[1][1]),"+v"(oc[1][2]),"+v"(oc[1][3]),"+v"(mxu),"+v"(xv[0]),"+v"(xv[1]),"+v"(xv[2]) :: "memory");
    #pragma unroll
    for(int kl=0;kl<2;kl++)
      #pragma unroll
      for(int t=0;t<2;t++)
        #pragma unroll
        for(int n=0;n<5;n++) acc[t][n]=MF16(wpre[(4+kl)*5+n],cat2(oc[t][2*kl],oc[t][2*kl+1]),acc[t][n]);
    f32x4 a[5];
    { char* xs=sm+XS+w*5120+voff; const bool hi=kq>>1;
      #pragma unroll
      for(int n=0;n<5;n++){ f32x4 u0=acc[0][n],u1=acc[1][n]; asm volatile("" : "+v"(u0),"+v"(u1)); a[n]=hi?u1:u0; *(f32x4*)(xs+n*1024)=hi?u0:u1; } }
    LBAR();
    { const char* xr=sm+XS+(w^4)*5120+voff;
      #pragma unroll
      for(int n=0;n<5;n++) a[n]+=*(const f32x4*)(xr+n*1024); }
    #pragma unroll
    for(int n=0;n<5;n++) asm volatile("" : "+v"(a[n]));
    f32x4 keep[3];
    { char* xs=sm+XS+(w^4)*5120+voff;
      keep[0]=hf?a[2]:a[0]; keep[1]=hf?a[3]:a[1]; keep[2]=a[4];
      *(f32x4*)(xs)=hf?a[0]:a[2]; *(f32x4*)(xs+1024)=hf?a[1]:a[3]; *(f32x4*)(xs+2048)=a[4]; }
    LBAR();
    { const char* xr=sm+XS+(w^6)*5120+voff;
      const rsrc_t rc=mkrs(ws+O_C+(size_t)t3*18*1024,18*1024), rq=mkrs(ws+O_SSQ+(size_t)t3*1024,1024);
      #pragma unroll
      for(int i=0;i<3;i++) keep[i]+=*(const f32x4*)(xr+i*1024);
      const float m=__uint_as_float(mxu)+bcast_hi(keep[2][0]);
      float qs=0; unsigned cp[6];
      #pragma unroll
      for(int i=0;i<3;i++){
        const f32x4 v=keep[i];
        const float c0=(lo16(xv[i][0])+v[0])-m,c1=(hi16(xv[i][0])+v[1])-m,c2=(lo16(xv[i][1])+v[2])-m,c3=(hi16(xv[i][1])+v[3])-m;
        const float qq=(c0*c0+c1*c1)+(c2*c2+c3*c3);
        qs+= (i<2 || (hf && g<2))? qq:0.f;
        cp[2*i]=pk2(c0,c1); cp[2*i+1]=pk2(c2,c3); }
      ST16(((u32x4){cp[0],cp[1],cp[2],cp[3]}),rc,voff,(2*ct3+hf)*1024);
      if(hf && g<2) ST8(((u32x2){cp[4],cp[5]}),rc,((2*(ct3&1)+g)*16+c16)*16+((ct3>>1)&1)*8,(16+(ct3>>2))*1024);
      qs=sum_g(qs);
      if(lane<16){ ST4(qs,rq,lane*4,(ct3*2+hf)*64); if(ct3==0 && hf==0) ST4(m,mkrs(ws+O_MH+(size_t)t3*64,64),lane*4,0); }
    }
    publish(ws,O_FH+(r3>>2)*256+((r3&3)*8+ct3)*8,tag,w);
    if(b==0 && tid==64) __hip_atomic_store((GAS u64*)(ws+O_EP),tag,__ATOMIC_RELAXED,__HIP_MEMORY_SCOPE_AGENT);
  }
  const int sp=xcd>>1, r2t=(xcd&1)*8+(idx>>2), ct=idx&3;
  const int tq=w&3, kh=w>>2;
  const unsigned rb=smb+kh*36864;
  const u32x4 rw2=mkrs4((const char*)p.wd+(size_t)(sp*36+kh*18)*32*1152+ct*288);
  const u32x4 ra2=mkrs4(ws+O_ACT+((size_t)(r2t*8+tq*2)*144+sp*36+kh*18)*1024);
  const unsigned rbt=rb+tq*1024;
  unsigned wv[3], voff2;
  bf16x8 a2[g2::AR][2];
  auto loadW=[&](auto J,auto X_){ constexpr int j=J.v,x=X_.v;
    constexpr int so=((j%8)<4? 73728:0)+(j%4)*9216;
    if constexpr(x<2) bl<g2::kord(j)*32*1152,so+4096*x>(rw2,wv[x],rbt);
    else blq<g2::kord(j)*32*1152,so+8192,j%4>(rw2,wv[2],rb,tq); };
  auto loadA2=[&](auto J,auto T){ constexpr int j=J.v,t=T.v; bload16c<t*144*1024+g2::kord(j)*1024>(a2[j%g2::AR][t],ra2,voff2); };
  auto g2_setup=[&](){
    const int l2=fresh_lane();
    #pragma unroll
    for(int i=0;i<3;i++){ const int sub= i<2? tq+4*i:8; const int id=sub*64+l2; const int row=id/18; wv[i]=row*1152+(id-row*18)*16; } };
  auto g2_prologue=[&](){
    voff2=fresh_lane()*16;
    sfor<0,g2::PRO>([&](auto J){ loadA2(J,IC<0>{}); loadA2(J,IC<1>{}); }); };
  {
    using namespace g1;
    const int t1=r1*16+w*2;
    const char* abase=ws+O_C+(size_t)t1*18*1024;
    if(w==7){ sfor<0,PA1>(ldG1); }
    if(w==0) poll<32>(ws,O_FH+r1*256,tag,lane);
    else sfor<PA1,PA2>(ldG1);
    if(w==7) waitv<PA2-PA1>();
    BAR();
    bf16x8 af[18][2];
    f32x4 sq[2];
    { const char* sp=ws+O_SSQ+(size_t)t1*1024; gload16c<0>(sq[0],sp,voff); gload16c<1024>(sq[1],sp,voff); }
    auto loadA=[&](auto Sx,auto T){ constexpr int s=Sx.v,t=T.v; gload16c<(s&3)*1024>(af[s][t], abase+t*18*1024+(s>>2)*4096, voff); };
    sfor<0,LAK>([&](auto Sx){ loadA(Sx,IC<0>{}); loadA(Sx,IC<1>{}); });
    if(w==0) sfor<PA1,PA2>(ldG1);
    f32x4 acc[4][2][2];
    const unsigned lb0=smb+(4*g+q)*128+((((g&1)<<1)|(q>>1))*32)+pp*8;
    unsigned ab[4];
    bf16x8 wf[PF];
    f32x2 c1; unsigned wmd;
    u32x4 hold[2];
    const rsrc_t ors=mkrs(ws+O_ACT+(size_t)t1*144*1024,2*144*1024);
    const int obase=L0*2*1024;
    const int olast=((fs>>2)*18+16+((fs>>1)&1))*1024+(fs&1)*8;
    auto rdW=[&](auto I){ constexpr int i=I.v, T=pT(i), sl=T&3, s=pS(i), mat=i&1;
      if constexpr(T<8){
        if constexpr(s==0 && mat==0){ if constexpr(i<8) ab[sl]=lb0^(sl*32); else ab[sl]=(ab[(sl+3)&3]-2*57344)^((sl^((sl+3)&3))*32); asm volatile("" : "+v"(ab[sl])); }
        if constexpr((s==7||s==14) && mat==0){ ab[sl]+=57344; asm volatile("" : "+v"(ab[sl])); }
        wf[i%PF]=cat(trread(ab[sl]+(s%7)*8192+mat*4096),trread(ab[sl]+(s%7)*8192+mat*4096+2048));
      } else {
        if constexpr(mat==0){
          if constexpr(s==0){ ab[0]=(ab[3]-2*57344)^96; asm volatile("" : "+v"(ab[0])); }
          if constexpr(s==8||s==17){ ab[0]^=32; asm volatile("" : "+v"(ab[0])); }
          if constexpr(s==16){ ab[0]=(ab[0]^32)+65536; asm volatile("" : "+v"(ab[0])); } }
        constexpr int o=(s<16? (s&7)*8192:0)+mat*4096;
        wf[i%PF]=cat(trread(ab[0]+o),trread(ab[0]+o+2048));
      } };
    sfor<0,PF-1>(rdW);
    f32x2 e2,p2;
    auto epi=[&](auto E,auto K){ constexpr int e=E.v,kk=K.v; constexpr int ET=e>>2, t=(e>>1)&1, h=e&1;
      if constexpr(kk==0){ const f32x2 gg={acc[ET&3][0][t][2*h],acc[ET&3][0][t][2*h+1]}, u={acc[ET&3][1][t][2*h],acc[ET&3][1][t][2*h+1]};
        e2=gg*__builtin_shufflevector(c1,c1,t,t); p2=gg*u; asm volatile("" : "+v"(e2),"+v"(p2)); }
      if constexpr(kk==1){ e2[0]=__builtin_amdgcn_exp2f(e2[0]); asm volatile("" : "+v"(e2)); }
      if constexpr(kk==2){ e2[1]=__builtin_amdgcn_exp2f(e2[1]); asm volatile("" : "+v"(e2)); }
      if constexpr(kk==3){ e2=e2+1.f; asm volatile("" : "+v"(e2)); }
      if constexpr(kk==4){ e2[0]=__builtin_amdgcn_rcpf(e2[0]); asm volatile("" : "+v"(e2)); }
      if constexpr(kk==5){ e2[1]=__builtin_amdgcn_rcpf(e2[1]); asm volatile("" : "+v"(e2)); }
      if constexpr(kk==6){
        const f32x2 o=p2*e2;
        constexpr int hi=(ET==8||(ET&1)==0)?0:2;
        hold[t][hi+h]=pk2(o[0],o[1]);
        asm volatile("" : "+v"(hold[t][hi+h]));
        if constexpr(h==1){
          if constexpr(ET==8){ ST8(((u32x2){hold[t][0],hold[t][1]}),ors,voff,olast+t*144*1024); }
          else if constexpr((ET&1)==1){ ST16(hold[t],ors,voff,obase+t*144*1024+(ET>>1)*1024); }
        }
      } };
    auto slot=[&](auto M){ constexpr int m=M.v;
      if constexpr(m>=255 && m<283) epi(IC<(m-255)/7>{},IC<(m-255)%7>{});
      if constexpr(m>=284){ constexpr int e=4+(m-284)/13, d=(m-284)%13; if constexpr(e<32 && d<7) epi(IC<e>{},IC<d>{}); } };
    sfor<0,324>([&](auto I){ constexpr int i=I.v; constexpr int T=pT(i), s=pS(i), mat=i&1;
      if constexpr(i<126 && i==firstUse(0,s)){
        gwait<nAafter(i,s)+nB(i)>(af[s][0],af[s][1]);
        if constexpr(i==0){ asm volatile("" : "+v"(sq[0]),"+v"(sq[1]));
          #pragma unroll
          for(int t=0;t<2;t++){ const float ss=ssq_sum(sq[t],lane); c1[t]=-1.44269504089f*__builtin_amdgcn_rsqf(ss*(1.f/576.f)+1e-5f); }
          asm volatile("" : "+v"(c1)); } }
      if constexpr(i==40||i==80||i==120||i==146) BAR();
      if constexpr(i==139){ waitv<nB(139)-10>(); BAR(); }
      if constexpr(i==159){ waitv<nB(159)-15>(); BAR(); }
      if constexpr(i==169||i==224||i==283){ waitv<0>(); BAR(); }
      if constexpr(i==224){ if(w==0) flag_store(ws,O_FA+r1*256+fs*8,tag); }
      if constexpr(i==266){ if(w==0) poll<8>(ws,O_FA+((xcd&1)*4+(idx>>3))*256+(xcd>>1)*64,tag,fresh_lane()); }
      if constexpr(i==276) g2_setup();
      if constexpr(i==290) BAR();
      if constexpr(i==224){ vC=vA+(8-2*(fs&3))*128+(fs&3)*32+((vA&32)? 288*(F*2)-32:0); asm volatile("" : "+v"(vC)); }
      if constexpr(i+PF-1<324) rdW(IC<i+PF-1>{});
      SB();
      if constexpr(s==0) acc[T&3][mat][0]=MF16(wf[i%PF],af[s][0],(f32x4){}); else acc[T&3][mat][0]=MF16(wf[i%PF],af[s][0],acc[T&3][mat][0]);
      SB();
      slot(IC<2*i>{});
      SB();
      if constexpr(s==0) acc[T&3][mat][1]=MF16(wf[i%PF],af[s][1],(f32x4){}); else acc[T&3][mat][1]=MF16(wf[i%PF],af[s][1],acc[T&3][mat][1]);
      SB();
      slot(IC<2*i+1>{});
      if constexpr(i==170){ const int l_=fresh_lane(); unsigned o=(w*21+(l_<21?l_:20))*128; if(o>20608) o=20608; gtouch(wmd,(const char*)p.wd+(size_t)b*20736,o); }
      if constexpr(i==225) asm_use(wmd);
      if constexpr(i>=290 && i<314 && (i&1)==0) loadW(IC<(i-290)/6>{},IC<((i-290)/2)%3>{});
      if constexpr(i<40 && i%8==3 && PA2+i/8<18) ldG1(IC<PA2+i/8>{});
      if constexpr(aAt(i)>=0) loadA(IC<aAt(i)/2>{},IC<aAt(i)%2>{});
      if constexpr(rB(i)>=0){ constexpr int r=rB(i); bl<krow(r)*(F*2)+128,r*8192>(r<16?rwA:rwB,vA,wdst); }
      if constexpr(rC(i)>=0){ constexpr int r=rC(i); blm<krow(r<8?r:16)*(F*2),r*8192>(r<8?rwA:rwB,vC,wdst,mC); }
      SB();
    });
    SB();
    g2_prologue();
    sfor<32,36>([&](auto E){ sfor<0,7>([&](auto K){ epi(E,K); }); });
  }
  {
    using namespace g2;
    const int lane=fresh_lane(); const unsigned voff=lane*16; const int g=lane>>4, q=(lane>>2)&3, pp=lane&3, c16=lane&15;
    f32x4 acc[9][2];
    unsigned lwb[2];
    #pragma unroll
    for(int i=0;i<2;i++){ lwb[i]=rb+(4*g+q)*288+pp*8+(i?0:73728); asm volatile("" : "+v"(lwb[i])); }
    bf16x8 wf[PF];
    #define LDW2(i) cat(trread(lwb[(((i)/9)%8)/4]+((((i)/9)%8)%4)*9216+((i)%9)*32),trread(lwb[(((i)/9)%8)/4]+((((i)/9)%8)%4)*9216+((i)%9)*32+16*288))
    waitv<0>(); BAR();
    if(w==1) flag_store(ws,O_FA2+r1*256+fs*8,tag);
    sfor<0,PF-1>([&](auto I){ wf[I.v]=LDW2(I.v); });
    sfor<0,NP>([&](auto I){ constexpr int i=I.v; constexpr int j=i/9, nt=i%9;
      if constexpr((i+BO)%18==0 && i+BO<NP){
        waitv<vmc(i,-1,(i+BO)/9+1)>();
        BAR(); }
      if constexpr(i==36){ if(w==0) poll<8>(ws,O_FA2+(r2t>>1)*256+sp*64,tag,lane); }
      if constexpr(nt==0) gwait<vmc(i,j,-1)>(a2[j%AR][0],a2[j%AR][1]);
      if constexpr(i+PF-1<NP) wf[(i+PF-1)%PF]=LDW2(i+PF-1);
      SB();
      if constexpr(j==0) acc[nt][0]=MF16(wf[i%PF],a2[j%AR][0],(f32x4){}); else acc[nt][0]=MF16(wf[i%PF],a2[j%AR][0],acc[nt][0]);
      if constexpr(j==0) acc[nt][1]=MF16(wf[i%PF],a2[j%AR][1],(f32x4){}); else acc[nt][1]=MF16(wf[i%PF],a2[j%AR][1],acc[nt][1]);
      SB();
      if constexpr(actAt(i)>=0) loadA2(IC<actAt(i)/2>{},IC<actAt(i)%2>{});
      if constexpr(wAt(i)>=0) loadW(IC<wAt(i)/3>{},IC<wAt(i)%3>{});
      SB();
    });
    BAR();
    { char* xs=sm+(w*9)*1024+voff;
      #pragma unroll
      for(int n=0;n<9;n++){ f32x4 v= kh? acc[n][0]:acc[n][1]; *(f32x4*)(xs+n*1024)=v; } }
    LBAR();
    f32x4 part[10];
    { const char* xr=sm+((w^4)*9)*1024+voff;
      #pragma unroll
      for(int n=0;n<9;n++){ f32x4 mine= kh? acc[n][1]:acc[n][0]; part[n]=mine+*(const f32x4*)(xr+n*1024); }
      part[9]=part[8]; }
    const int grp=r2t*4+ct;
    const rsrc_t xr=mkrs(ws+O_X+(size_t)grp*160*1024,160*1024);
    if(tq!=sp){
      #pragma unroll
      for(int n=0;n<5;n++) ST16(((u32x4){pk2(part[2*n][0],part[2*n][1]),pk2(part[2*n][2],part[2*n][3]),pk2(part[2*n+1][0],part[2*n+1][1]),pk2(part[2*n+1][2],part[2*n+1][3])}),xr,voff,((tq*4+sp)*10+kh*5+n)*1024);
    } else {
      char* xs=sm+(72+kh*10)*1024+voff;
      #pragma unroll
      for(int n=0;n<10;n++) *(f32x4*)(xs+n*1024)=part[n];
    }
    publish(ws,O_FX+grp*128+sp*8,tag,w);
    const int tk0=r2t*128+sp*32;
    const rsrc_t rh=mkrs(ws+O_C+(size_t)(tk0>>4)*18432,2*18432), rq=mkrs(ws+O_SSQ+(size_t)tk0*64,2048), rmh=mkrs(ws+O_MH+(size_t)tk0*4,128);
    u32x2 hv[2][2]; float r2[2], mh[2]; int go[2][2];
    #pragma unroll
    for(int x=0;x<2;x++){ int u=w+8*x; if(u>9) u=9; const int tl=u>=5, pr=u-5*tl;
      #pragma unroll
      for(int y=0;y<2;y++){ int nt=2*pr+y; if(nt>8) nt=8; const int nl=nt*16+g*4;
        go[x][y]=(tl*16+c16)*1152+(ct*144+nl)*2;
        const int c3=ct*2+(nl>=72), o72=nl>=72? nl-72:nl;
        int kc,gp,hfh; if(o72<64){ kc=2*c3+(o72>>5); hfh=(o72>>4)&1; gp=(o72>>2)&3; } else { kc=16+(c3>>2); gp=2*(c3&1)+((o72-64)>>2); hfh=(c3>>1)&1; }
        hv[x][y]=LD8(rh,(tl*18+kc)*1024+(gp*16+c16)*16+hfh*8,0); }
      const f32x4 s4=__builtin_bit_cast(f32x4,LD16(rq,tl*1024+voff,0));
      mh[x]=__uint_as_float(__builtin_amdgcn_raw_buffer_load_b32(rmh,(tl*16+c16)*4,0,16));
      r2[x]=__builtin_amdgcn_rcpf(ssq_sum(s4,lane)*(1.f/576.f)+1e-5f); }
    if(w==0) poll<4>(ws,O_FX+grp*128,tag,lane);
    BAR();
    u32x4 rv[2][3];
    #pragma unroll
    for(int x=0;x<2;x++){ int u=w+8*x; if(u>9) u=9;
      #pragma unroll
      for(int y=0;y<3;y++){ int src=(sp+1+y)&3; rv[x][y]=LD16(xr,voff,((sp*4+src)*10+u)*1024); } }
    char* const ob=(char*)p.out+(size_t)(r2t*128+sp*32)*1152;
    #pragma unroll
    for(int x=0;x<2;x++){ if(x==0 || w<2){ const int u=w+8*x; const int tl=u>=5, pr=u-5*tl;
      #pragma unroll
      for(int y=0;y<2;y++){ if(y==0 || pr<4){
        f32x4 v=*(const f32x4*)(sm+(72+tl*10+2*pr+y)*1024+voff);
        #pragma unroll
        for(int z=0;z<3;z++){ v[0]+=lo16(rv[x][z][2*y]); v[1]+=hi16(rv[x][z][2*y]); v[2]+=lo16(rv[x][z][2*y+1]); v[3]+=hi16(rv[x][z][2*y+1]); }
        u32x2 ov; ov[0]=pk2(v[0]*r2[x]+(lo16(hv[x][y][0])+mh[x]),v[1]*r2[x]+(hi16(hv[x][y][0])+mh[x])); ov[1]=pk2(v[2]*r2[x]+(lo16(hv[x][y][1])+mh[x]),v[3]*r2[x]+(hi16(hv[x][y][1])+mh[x]));
        *(u32x2*)(ob+go[x][y])=ov; } } } }
  }
}
}

extern "C" void kernel_launch(void* const* d_in, const int* in_sizes, int n_in,
                              void* d_out, int out_size, void* d_ws, size_t ws_size,
                              hipStream_t stream) {
  using namespace k;
  Params p;
  p.x=(const u16*)d_in[0]; p.wqkv=(const u16*)d_in[1]; p.wo=(const u16*)d_in[2];
  p.wg=(const u16*)d_in[4]; p.wu=(const u16*)d_in[5]; p.wd=(const u16*)d_in[6];
  p.out=(u16*)d_out; p.ws=(char*)d_ws;
  fused_k<<<256,512,0,stream>>>(p);
}
```

```cpp
#include <hip/hip_runtime.h>
#include <hip/hip_bf16.h>
typedef unsigned short u16;
typedef unsigned long long u64;
typedef short s16x4 __attribute__((ext_vector_type(4)));
typedef __bf16 bf16x8 __attribute__((ext_vector_type(8)));
typedef __bf16 bf16x2 __attribute__((ext_vector_type(2)));
typedef float f32x4 __attribute__((ext_vector_type(4)));
typedef float f32x2 __attribute__((ext_vector_type(2)));
typedef unsigned u32x4 __attribute__((ext_vector_type(4)));
typedef unsigned u32x2 __attribute__((ext_vector_type(2)));
#define LAS __attribute__((address_space(3)))
#define GAS __attribute__((address_space(1)))
#define DI __device__ __forceinline__

#define WARM_SLEEP 20
#define QSPLIT 24
#define PA1 7
#define PA2 13

namespace k {
constexpr int S=2048, D=576, F=4608;
constexpr int TAG_LDS=155*1024;
DI int fresh_lane(){ int l; asm volatile("v_mbcnt_lo_u32_b32 %0, -1, 0\n\tv_mbcnt_hi_u32_b32 %0, -1, %0" : "=v"(l)); return l; }
DI u16 f2bf(float f){ __bf16 b=(__bf16)f; return __builtin_bit_cast(u16,b); }
DI unsigned pk2(float lo,float hi){ bf16x2 v; v[0]=(__bf16)lo; v[1]=(__bf16)hi; return __builtin_bit_cast(unsigned,v); }
DI float lo16(unsigned v){ return __uint_as_float(v<<16); }
DI float hi16(unsigned v){ return __uint_as_float(v&0xffff0000u); }
DI float phi(float t){ return (t>0.f? t+1.f: __builtin_amdgcn_exp2f(t*1.44269504089f))+1e-6f; }
DI float bcast_hi(float x){ unsigned a=__float_as_uint(x); auto r=__builtin_amdgcn_permlane32_swap(a,a,false,false); return __uint_as_float(r[1]); }
DI float sum_g(float x){
  unsigned a=__float_as_uint(x); auto r=__builtin_amdgcn_permlane32_swap(a,a,false,false); x=__uint_as_float(r[0])+__uint_as_float(r[1]);
  a=__float_as_uint(x); auto r2=__builtin_amdgcn_permlane16_swap(a,a,false,false); return __uint_as_float(r2[0])+__uint_as_float(r2[1]); }
template<int CTRL> DI float dppf(float x){ return __uint_as_float(__builtin_amdgcn_update_dpp(0u,__float_as_uint(x),CTRL,0xf,0xf,false)); }
DI float ssq_sum(f32x4 v,int){
  const int l=fresh_lane(); const int k=(l>>2)&3;
  #pragma unroll
  for(int r=0;r<4;r++){ float x=v[r]; x+=dppf<0x124>(x); x+=dppf<0x128>(x); v[r]=x; }
  const float t=sum_g(k==0? v[0] : (k==1? v[1] : (k==2? v[2]:v[3])));
  return __uint_as_float(__builtin_amdgcn_ds_bpermute((((l&3)<<2)|k)*4,__float_as_uint(t))); }
DI float dot2(unsigned a,unsigned b,float c){ return __builtin_amdgcn_fdot2_f32_bf16(__builtin_bit_cast(bf16x2,a),__builtin_bit_cast(bf16x2,b),c,false); }
DI s16x4 trread(unsigned a){ return __builtin_amdgcn_ds_read_tr16_b64_v4i16((LAS s16x4*)(size_t)a); }
DI bf16x8 cat(s16x4 a,s16x4 b){ return __builtin_bit_cast(bf16x8,__builtin_shufflevector(a,b,0,1,2,3,4,5,6,7)); }
DI bf16x8 cat2(u32x2 a,u32x2 b){ return __builtin_bit_cast(bf16x8,(u32x4){a[0],a[1],b[0],b[1]}); }
#define BAR() do{ asm volatile("":::"memory"); __builtin_amdgcn_s_barrier(); asm volatile("":::"memory"); }while(0)
#define LBAR() do{ asm volatile("s_waitcnt lgkmcnt(0)":::"memory"); __builtin_amdgcn_s_barrier(); asm volatile("":::"memory"); }while(0)
#define MF16(a,bb,c) __builtin_amdgcn_mfma_f32_16x16x32_bf16(a,bb,c,0,0,0)
#define SB() __builtin_amdgcn_sched_barrier(0)
template<int I> struct IC{ static constexpr int v=I; };
template<int B,int E,class Fn> DI void sfor(Fn&& f){ if constexpr(B<E){ f(IC<B>{}); sfor<B+1,E>(f); } }
DI void glds16c(const void* sbase, unsigned voff, unsigned lds){
  unsigned keep;
  asm volatile("s_mov_b32 %0, m0\n\ts_mov_b32 m0, %3\n\ts_nop 0\n\tglobal_load_lds_dwordx4 %1, %2 sc1\n\ts_mov_b32 m0, %0" : "=&s"(keep) : "v"(voff), "s"(sbase), "s"(lds) : "memory");
}
DI u32x4 mkrs4(const void* p){ const u64 a=(u64)p; u32x4 r; r[0]=__builtin_amdgcn_readfirstlane((unsigned)a); r[1]=__builtin_amdgcn_readfirstlane((unsigned)(a>>32)); r[2]=0x7fffffffu; r[3]=0x00020000u; return r; }
template<int SOFF,int LOFF> DI void bl(u32x4 rs,unsigned voff,unsigned lds){ unsigned t;
  asm volatile("s_add_i32 m0, %3, %5\n\ts_mov_b32 %0, %4\n\tbuffer_load_dwordx4 %1, %2, %0 offen lds" : "=&s"(t) : "v"(voff), "s"(rs), "s"(lds), "i"(SOFF), "i"(LOFF) : "memory","scc"); }
template<int SOFF,int LOFF> DI void blm(u32x4 rs,unsigned voff,unsigned lds,u64 mask){ unsigned t;
  asm volatile("s_add_i32 m0, %3, %5\n\ts_mov_b32 %0, %4\n\ts_mov_b64 exec, %6\n\tbuffer_load_dwordx4 %1, %2, %0 offen lds\n\ts_mov_b64 exec, -1" : "=&s"(t) : "v"(voff), "s"(rs), "s"(lds), "i"(SOFF), "i"(LOFF), "s"(mask) : "memory","scc"); }
template<int SOFF,int LOFF,int EQ> DI void blq(u32x4 rs,unsigned voff,unsigned lds,int sel){ unsigned t;
  asm volatile("s_cmp_lg_u32 %6, %7\n\ts_cbranch_scc1 1f\n\ts_add_i32 m0, %3, %5\n\ts_mov_b32 %0, %4\n\tbuffer_load_dwordx4 %1, %2, %0 offen lds\n1:" : "=&s"(t) : "v"(voff), "s"(rs), "s"(lds), "i"(SOFF), "i"(LOFF), "s"(sel), "i"(EQ) : "memory","scc"); }
template<int SOFF,class T> DI void bload16c(T& r,u32x4 rs,unsigned voff){ unsigned t;
  asm volatile("s_mov_b32 %1, %4\n\tbuffer_load_dwordx4 %0, %2, %3, %1 offen sc1" : "=v"(r), "=&s"(t) : "v"(voff), "s"(rs), "i"(SOFF) : "memory"); }
template<int OFF,class T> DI void gload16(T& r,const void* sbase,unsigned voff){ asm volatile("global_load_dwordx4 %0, %1, %2 offset:%3" : "=v"(r) : "v"(voff), "s"(sbase), "i"(OFF) : "memory"); }
template<int OFF,class T> DI void gload16c(T& r,const void* sbase,unsigned voff){ asm volatile("global_load_dwordx4 %0, %1, %2 offset:%3 sc1" : "=v"(r) : "v"(voff), "s"(sbase), "i"(OFF) : "memory"); }
DI void gload8c(u32x2& r,const void* sbase,unsigned voff){ asm volatile("global_load_dwordx2 %0, %1, %2 sc1" : "=v"(r) : "v"(voff), "s"(sbase) : "memory"); }
DI void gload4c(unsigned& r,const void* sbase,unsigned voff){ asm volatile("global_load_dword %0, %1, %2 sc1" : "=v"(r) : "v"(voff), "s"(sbase) : "memory"); }
DI void gtouch(unsigned& r,const void* sbase,unsigned voff){ asm volatile("global_load_dword %0, %1, %2" : "=v"(r) : "v"(voff), "s"(sbase) : "memory"); }
DI void asm_use(unsigned r){ asm volatile("" :: "v"(r)); }
template<int N,class T> DI void gwait(T& a,T& b){ asm volatile("s_waitcnt vmcnt(%2)" : "+v"(a),"+v"(b) : "i"(N) : "memory"); }
template<int N> DI void waitv(){ asm volatile("s_waitcnt vmcnt(%0)" :: "i"(N) : "memory"); }
typedef __amdgpu_buffer_rsrc_t rsrc_t;
DI rsrc_t mkrs(const void* p,unsigned bytes){ return __builtin_amdgcn_make_buffer_rsrc((void*)p,0,bytes,0x00020000); }
#define ST16(v,rs,vo,so) __builtin_amdgcn_raw_buffer_store_b128(__builtin_bit_cast(u32x4,v),rs,vo,so,16)
#define ST8(v,rs,vo,so)  __builtin_amdgcn_raw_buffer_store_b64(__builtin_bit_cast(u32x2,v),rs,vo,so,16)
#define ST4(v,rs,vo,so)  __builtin_amdgcn_raw_buffer_store_b32(__float_as_uint(v),rs,vo,so,16)
#define LD16(rs,vo,so) __builtin_amdgcn_raw_buffer_load_b128(rs,vo,so,16)
#define LD8(rs,vo,so)  __builtin_amdgcn_raw_buffer_load_b64(rs,vo,so,16)

constexpr size_t O_EP=0;
constexpr size_t O_F0 =256;
constexpr size_t O_FW =O_F0+2048;
constexpr size_t O_FWO=O_FW+2048;
constexpr size_t O_FKV=O_FWO+1024;
constexpr size_t O_FO =O_FKV+16*256;
constexpr size_t O_FH =O_FO+16*256;
constexpr size_t O_FA =O_FH+8*256;
constexpr size_t O_FA2=O_FA+8*256;
constexpr size_t O_FX =O_FA2+8*256;
constexpr size_t O_KVP=32768;
constexpr size_t O_XN =O_KVP+(size_t)256*8192;
constexpr size_t O_MX =O_XN+(size_t)S*D*2;
constexpr size_t O_WB =O_MX+(size_t)S*4;
constexpr size_t O_WQ =O_WB+4096;
constexpr size_t O_WO =O_WQ+(size_t)16*144*1024;
constexpr size_t O_O  =O_WO+(size_t)8*120*1024;
constexpr size_t O_MU =O_O+(size_t)128*16*1536;
constexpr size_t O_MH =O_MU+(size_t)S*64;
constexpr size_t O_C  =O_MH+(size_t)S*4;
constexpr size_t O_SSQ=O_C+(size_t)S*D*2;
constexpr size_t O_ACT=O_SSQ+(size_t)S*64;
constexpr size_t O_X  =O_ACT+(size_t)S*F*2;
constexpr size_t O_END=O_X+(size_t)64*160*1024;
struct Params { const u16 *x,*wqkv,*wo,*wg,*wu,*wd; u16* out; char* ws; };

DI void publish(char* ws,size_t off,u64 tag,int w){
  asm volatile("s_waitcnt vmcnt(0) lgkmcnt(0)":::"memory");
  BAR();
  if(w==0 && fresh_lane()==0) __hip_atomic_store((GAS u64*)(ws+off),tag,__ATOMIC_RELAXED,__HIP_MEMORY_SCOPE_AGENT);
}
DI void flag_store(char* ws,size_t off,u64 tag){ if(fresh_lane()==0) __hip_atomic_store((GAS u64*)(ws+off),tag,__ATOMIC_RELAXED,__HIP_MEMORY_SCOPE_AGENT); }
DI void pollp(const GAS u64* p,u64 tag){
  for(unsigned spins=0; spins<(1u<<20); ++spins){
    u64 v=__hip_atomic_load(p,__ATOMIC_RELAXED,__HIP_MEMORY_SCOPE_AGENT);
    if(__all(v==tag)) break;
    __builtin_amdgcn_s_sleep(1);
  }
  __builtin_amdgcn_fence(__ATOMIC_ACQUIRE,"wavefront");
}
template<int N> DI void poll(char* ws,size_t off,u64 tag,int lane){ pollp((const GAS u64*)(ws+off)+(lane<N?lane:0),tag); }

namespace f1 {
constexpr int PF=6, XA=8, WA=6;
constexpr int wr(int r){ return r<WA? -1 : (r<12? 5*(r-WA)+3 : 53+5*(r-12)); }
constexpr int roundAt(int i){ for(int r=WA;r<18;r++) if(wr(r)==i) return r; return -1; }
constexpr int vmc(int iw,int xs,int pw){
  int cnt=0;
  for(int k=0;k<XA;k++){ if(k<WA){ if(8*k<pw) cnt=0; else cnt++; } if(k<=xs) cnt=0; else cnt++; }
  for(int i=0;i<iw;i++){
    if(i%5==1 && i/5+XA<18){ if(i/5+XA<=xs) cnt=0; else cnt++; }
    int r=roundAt(i); if(r>=0){ if(8*r<pw) cnt=0; else cnt++; }
  }
  return cnt;
}
constexpr int lpos(int p){ return p<90? p : p+12; }
}
namespace g1 {
constexpr int PF=4, LAK=4;
constexpr int pT(int i){ return i<120? (i%8)/2 : (i<144? (i-120)/6 : 4+(i-144)/36); }
constexpr int pS(int i){ return i<120? i/8 : (i<144? 15+((i-120)%6)/2 : ((i-144)%36)/2); }
constexpr int krow(int kc){ return kc<16? 72*(kc>>1)+32*(kc&1) : 288*(kc-16)+64; }
constexpr int iB(int r){ return r<10? 81+4*r : (r<15? 122+3*(r-10) : 147+2*(r-15)); }
constexpr int iA(int s,int t){ return s<LAK? -1 : 5*(s-LAK)+1+2*t; }
constexpr int aAt(int i){ for(int s=LAK;s<18;s++) for(int t=0;t<2;t++) if(iA(s,t)==i) return s*2+t; return -1; }
constexpr int nAafter(int i,int s){ int n=0; for(int x=s+1;x<18;x++) for(int t=0;t<2;t++) if(iA(x,t)<i) n++; return n; }
constexpr int iC(int r){ return 225+4*r; }
constexpr int rB(int i){ for(int r=0;r<18;r++) if(iB(r)==i) return r; return -1; }
constexpr int rC(int i){ for(int r=0;r<9;r++) if(iC(r)==i) return r; return -1; }
constexpr int nB(int i){ int n=0; for(int r=0;r<18;r++) if(iB(r)<i) n++; return n; }
constexpr int firstUse(int T,int s){ for(int i=0;i<324;i++) if(pT(i)==T&&pS(i)==s) return i; return -1; }
}
namespace g2 {
constexpr int LW=5, PF=5, BO=8, NP=162, AR=6;
constexpr int kord(int j){ constexpr int t[18]={0,1,4,5,8,9,12,13, 2,3,6,7,10,11,14,15,16,17}; return t[j]; }
constexpr int PRO=4;
constexpr int actStep(int j,int t){ return j<PRO? -1 : (j<6? 5*(j-PRO)+2+2*t : ((j>=8&&j<=10)? 50+2*(j-8)+t : (j<8? 9*(j-4)+2+3*t : 9*(j-5)+2+3*t))); }
constexpr int wStep(int j,int x){ return j<PRO? -1 : (j<8? 5*(j-PRO)+(x==2?3:x) : 9*(j-LW)+(x==0?1:(x==1?3:6))); }
constexpr int vmc(int iw,int ja,int jw){
  int best=63;
  for(int tq=0;tq<4;tq++){
    int cnt=0;
    for(int j=0;j<PRO;j++){ int n=2+((j%4)==tq); if(j<=jw) cnt=0; else cnt+=n; if(j<=ja) cnt=0; else cnt+=2; }
    for(int i=0;i<iw;i++){
      for(int j=PRO;j<18;j++) for(int t=0;t<2;t++) if(actStep(j,t)==i){ if(j<=ja) cnt=0; else cnt++; }
      for(int j=PRO;j<18;j++) for(int x=0;x<3;x++) if(wStep(j,x)==i && (x<2||(j%4)==tq)){ if(j<=jw) cnt=0; else cnt++; }
    }
    if(cnt<best) best=cnt;
  }
  return best;
}
constexpr int wAt(int i){ for(int j=PRO;j<18;j++) for(int x=0;x<3;x++) if(wStep(j,x)==i) return j*3+x; return -1; }
constexpr int actAt(int i){ for(int j=PRO;j<18;j++) for(int t=0;t<2;t++) if(actStep(j,t)==i) return j*2+t; return -1; }
}

__global__ __launch_bounds__(512,2) void fused_k(Params p){
  __shared__ __attribute__((aligned(16))) char sm[160*1024];
  const int tid=threadIdx.x, lane=tid&63, w=__builtin_amdgcn_readfirstlane(tid>>6);
  const int b=blockIdx.x, xcd=b&7, idx=b>>3;
  const unsigned smb=(unsigned)(size_t)(LAS char*)sm;
  const unsigned voff=lane*16;
  const int g=lane>>4, q=(lane>>2)&3, pp=lane&3, c16=lane&15;
  char* const ws=p.ws;
  u64 tag;
  const int hp=(xcd&1)*8+(idx>>2), rt=(xcd>>1)*4+(idx&3);
  unsigned wm[2]={0,0};
  auto touch_gu=[&](int slot,int unit,int lo){
    int ln=unit*64+lane; if(ln>323) ln=323; const int mt=ln>=162; ln-=mt*162;
    const char* src=(const char*)(mt? p.wu : p.wg)+(size_t)b*20736+ln*128;
    asm volatile("s_cmp_lt_u32 %2, %3\n\ts_cbranch_scc1 1f\n\tglobal_load_dword %0, %1, off\n1:" : "+v"(wm[slot]) : "v"(src), "s"(w), "s"(lo) : "memory","scc"); };
  {
    char* const smw=sm+w*16384;
    const unsigned smwb=smb+w*16384;
    if(w<2){
      u32x2 ep; { const unsigned z=0; asm volatile("global_load_dwordx2 %0, %1, %2 sc1" : "=v"(ep) : "v"(z), "s"(ws+(w?(size_t)O_XN:0)) : "memory"); }
      u32x4 v[5];
      { const char* xb=(const char*)p.x+(size_t)(b*8+w*4)*1152; const unsigned v0=g*1152+c16*16, v4=g*1152+(64+(c16&7))*16;
        gload16<0>(v[0],xb,v0); gload16<256>(v[1],xb,v0); gload16<512>(v[2],xb,v0); gload16<768>(v[3],xb,v0); gload16<0>(v[4],xb,v4); }
      asm volatile("s_waitcnt vmcnt(0)" : "+v"(ep),"+v"(v[0]),"+v"(v[1]),"+v"(v[2]),"+v"(v[3]),"+v"(v[4]) :: "memory");
      if(tid==0) *(u32x2*)(sm+TAG_LDS)=ep;
      float s1=0;
      #pragma unroll
      for(int i=0;i<5;i++){ const unsigned one=(i<4||c16<8)?0x3f803f80u:0u;
        #pragma unroll
        for(int j=0;j<4;j++) s1=dot2(v[i][j],one,s1); }
      s1+=__shfl_xor(s1,1); s1+=__shfl_xor(s1,2); s1+=__shfl_xor(s1,4); s1+=__shfl_xor(s1,8);
      const float m=s1*(1.f/576.f);
      float f[5][8]; float s2=0,s2t=0;
      #pragma unroll
      for(int i=0;i<5;i++)
        #pragma unroll
        for(int j=0;j<4;j++){ f[i][2*j]=lo16(v[i][j])-m; f[i][2*j+1]=hi16(v[i][j])-m;
          if(i<4){ s2=fmaf(f[i][2*j],f[i][2*j],s2); s2=fmaf(f[i][2*j+1],f[i][2*j+1],s2); } else { s2t=fmaf(f[i][2*j],f[i][2*j],s2t); s2t=fmaf(f[i][2*j+1],f[i][2*j+1],s2t); } }
      s2+= c16<8? s2t:0.f;
      s2+=__shfl_xor(s2,1); s2+=__shfl_xor(s2,2); s2+=__shfl_xor(s2,4); s2+=__shfl_xor(s2,8);
      const float r=__builtin_amdgcn_rsqf(s2*(1.f/576.f)+1e-5f);
      #pragma unroll
      for(int i=0;i<5;i++){ u32x4 o;
        #pragma unroll
        for(int j=0;j<4;j++) o[j]=pk2(f[i][2*j]*r,f[i][2*j+1]*r);
        *(u32x4*)(smw+(i<4? c16+16*i: 64+(c16&7))*80+g*16)=o; }
      asm volatile("s_waitcnt lgkmcnt(0)":::"memory");
      const rsrc_t rx=mkrs(ws+O_XN+(size_t)(b>>1)*18432+(b&1)*128+w*64,18432);
      #pragma unroll
      for(int i=0;i<5;i++){ const int ch=(lane>>2)+16*i; if(ch<72) ST16(*(const u32x4*)(smw+ch*80+pp*16),rx,ch*256+pp*16,0); }
      const rsrc_t rm=mkrs(ws+O_MX+(size_t)b*32+w*16,16);
      if(c16==0) ST4(m,rm,g*4,0);
    } else if(w<4){
      if(b<144){
        const int s=b>>3, hq=b&7, h2=w-2;
        u32x4 v[8];
        const char* src=(const char*)p.wqkv+(size_t)s*32*3456+hq*144+h2*64;
        int lo_[8];
        #pragma unroll
        for(int i=0;i<8;i++){ const int r=g+4*i; int c=c16<15?c16:14; const int part=c/5, cc=c-part*5; lo_[i]=r*272+c*16; gload16<0>(v[i],src,r*3456+part*1152+cc*16); }
        asm volatile("s_waitcnt vmcnt(0)" : "+v"(v[0]),"+v"(v[1]),"+v"(v[2]),"+v"(v[3]),"+v"(v[4]),"+v"(v[5]),"+v"(v[6]),"+v"(v[7]) :: "memory");
        #pragma unroll
        for(int i=0;i<8;i++) *(u32x4*)(smw+lo_[i])=v[i];
        asm volatile("s_waitcnt lgkmcnt(0)":::"memory");
        const rsrc_t rw=mkrs(ws+O_WQ+(size_t)(hq*2+h2)*144*1024,144*1024);
        #pragma unroll
        for(int t=0;t<8;t++){
          const int vc=(t<3? 16*t:16*(t-3))+4*pp;
          const int col= t<3? (vc<36? vc:0) : (vc<36? 40+vc : (vc<72? 80+vc-36 : 0));
          const unsigned ba=smwb+(8*g+q)*272+(h2*4+col)*2;
          u32x4 o=__builtin_bit_cast(u32x4,cat(trread(ba),trread(ba+4*272)));
          if((t<3? 16*t:16*(t-3))+c16>=(t<3?36:72)) o=(u32x4){0,0,0,0};
          ST16(o,rw,voff,(t<3? 90+s*3+t : s*5+t-3)*1024); }
      } else if(b<240){
        const int jb=b-144, kap=jb>>2, ct=(jb&3)*2+(w-2);
        const int kq=kap/6, kl=kap%6;
        u32x4 v[5];
        const char* src=(const char*)p.wo+ct*144;
        bool okr[5]; int lo_[5];
        #pragma unroll
        for(int i=0;i<5;i++){ int e=lane+64*i; if(e>287) e=287; const int r=e/9, c=e-r*9, row=r&15, hi=r>>4;
          int kr; if(kl<4){ kr=(4*kq+kl)*36+(hi? 12+row: row-4); okr[i]= hi|| row>=4; } else { kr=(4*kq+2*(kl-4)+hi)*36+28+row; okr[i]=row<8; }
          lo_[i]=r*160+c*16; gload16<0>(v[i],src,(okr[i]?kr:0)*1152+c*16); }
        asm volatile("s_waitcnt vmcnt(0)" : "+v"(v[0]),"+v"(v[1]),"+v"(v[2]),"+v"(v[3]),"+v"(v[4]) :: "memory");
        #pragma unroll
        for(int i=0;i<5;i++) *(u32x4*)(smw+lo_[i])= okr[i]? v[i]:(u32x4){0,0,0,0};
        asm volatile("s_waitcnt lgkmcnt(0)":::"memory");
        const rsrc_t rw=mkrs(ws+O_WO+(size_t)ct*120*1024,120*1024);
        #pragma unroll
        for(int nt=0;nt<5;nt++){
          const int col=nt*16+4*((nt==4)?(pp&1):pp);
          const unsigned ba=smwb+(4*g+q)*160+col*2;
          u32x4 o=__builtin_bit_cast(u32x4,cat(trread(ba),trread(ba+16*160)));
          if(nt==4 && c16>=8) o=(u32x4){ (kl<4 && g==0 && (c16&3)==0)? 0x3f803f80u:0u ,0u,0u,0u};
          ST16(o,rw,voff,(kap*5+nt)*1024); }
      }
    } else if(w==4 && rt<9){
      u32x4 v[5];
      const char* rp=(const char*)p.wo+(size_t)(hp*36+rt*4)*1152;
      const unsigned v0=g*1152+c16*16, v4=g*1152+(64+(c16&7))*16;
      gload16<0>(v[0],rp,v0); gload16<256>(v[1],rp,v0); gload16<512>(v[2],rp,v0); gload16<768>(v[3],rp,v0); gload16<0>(v[4],rp,v4);
      asm volatile("s_waitcnt vmcnt(0)" : "+v"(v[0]),"+v"(v[1]),"+v"(v[2]),"+v"(v[3]),"+v"(v[4]) :: "memory");
      float sacc=0;
      #pragma unroll
      for(int i=0;i<5;i++){ const unsigned one=(i<4||c16<8)?0x3f803f80u:0u;
        #pragma unroll
        for(int j=0;j<4;j++) sacc=dot2(v[i][j],one,sacc); }
      sacc+=__shfl_xor(sacc,1); sacc+=__shfl_xor(sacc,2); sacc+=__shfl_xor(sacc,4); sacc+=__shfl_xor(sacc,8);
      if(c16==0) ST4(sacc*(1.f/576.f),mkrs(ws+O_WB+(size_t)(hp*36+rt*4)*4,16),g*4,0);
    }
    if(w>=5){ __builtin_amdgcn_s_sleep(WARM_SLEEP); touch_gu(1,w==5?4:5,5); }
    if(w<5) asm volatile("s_waitcnt vmcnt(0) lgkmcnt(0)":::"memory");
    BAR();
    { const u32x2 e=*(const u32x2*)(sm+TAG_LDS); unsigned lo=__builtin_amdgcn_readfirstlane(e[0]), hi=__builtin_amdgcn_readfirstlane(e[1]); tag=(((u64)hi<<32)|lo)+1ull; }
    if(tid<2 && (tid==0 || b<240)){
      const size_t fo= tid==0? O_F0+b*8 : (b<144? O_FW+(b&7)*256+(b>>3)*8 : O_FWO+((b-144)&3)*256+((b-144)>>2)*8);
      __hip_atomic_store((GAS u64*)(ws+fo),tag,__ATOMIC_RELAXED,__HIP_MEMORY_SCOPE_AGENT); }
    if(w==0) pollp((const GAS u64*)(ws+(lane<16? O_F0+rt*128+lane*8 : (lane<34? O_FW+(hp>>1)*256+(lane-16)*8 : O_FWO+((idx&7)>>1)*256+(lane<58?lane-34:0)*8))),tag);
    BAR();
  }
  const int tt=rt*8+w;
  const int ct3=idx&7, r3=xcd*4+(idx>>3);
  constexpr int WOL=8*1024;
  bf16x8 q01; u32x2 q2;
  bool okv[2][4]; bool anyok[2]={false,false};
  {
    using namespace f1;
    const char* wbase=ws+O_WQ+(size_t)hp*144*1024+w*1024;
    const char* abase=ws+O_XN+(size_t)tt*18*1024;
    bf16x8 af[18];
    auto ldW=[&](auto R){ constexpr int r=R.v; const int pc=r*8+w; glds16c(wbase+r*8192,voff,smb+((r<11||(r==11&&0))? pc : (pc<90? pc:pc+12))*1024); };
    auto ldX=[&](auto Sx){ constexpr int s=Sx.v; gload16c<(s&3)*1024>(af[s],abase+(s>>2)*4096,voff); };
    sfor<0,XA>([&](auto K){ if constexpr(K.v<WA) ldW(K); ldX(K); });
    f32x4 acc[8];
    unsigned lwb[4];
    #pragma unroll
    for(int i=0;i<4;i++){ lwb[i]=smb+voff+i*49152; asm volatile("" : "+v"(lwb[i])); }
    bf16x8 wf[PF];
    #define LDW1(i) (*(const LAS bf16x8*)((const LAS char*)(size_t)lwb[lpos(i)/48]+(lpos(i)%48)*1024))
    #define WAITP(m,n) asm volatile("s_waitcnt vmcnt(%2)" : "+v"(af[2*(m)]),"+v"(af[2*(m)+1]) : "i"(n) : "memory")
    WAITP(0,vmc(0,1,10)); BAR();
    sfor<0,PF-1>([&](auto I){ wf[I.v]=LDW1(I.v); });
    sfor<0,90>([&](auto I){ constexpr int i=I.v; constexpr int s=i/5,t=i%5;
      if constexpr(i%10==4 && i/10<8){ constexpr int m=i/10+1; WAITP(m,vmc(i,2*m+1,10*(m+1))); BAR(); }
      if constexpr(i+PF-1<90) wf[(i+PF-1)%PF]=LDW1(i+PF-1);
      SB();
      if constexpr(s==0) acc[3+t]=MF16(af[s],wf[i%PF],(f32x4){}); else acc[3+t]=MF16(af[s],wf[i%PF],acc[3+t]);
      SB();
      if constexpr(t==1 && s+XA<18) ldX(IC<s+XA>{});
      if constexpr(roundAt(i)>=0) ldW(IC<roundAt(i)>{});
      SB();
    });
    u32x2 kvb[5];
    #pragma unroll
    for(int t=0;t<5;t++){ float x[4];
      #pragma unroll
      for(int r4=0;r4<4;r4++){ float a=acc[3+t][r4];
        if(t<2) a=phi(a); else if(t==2) a= c16<4? phi(a):a; else if(t==4) a= c16<8? a : 1.f;
        x[r4]=a; }
      kvb[t][0]=pk2(x[0],x[1]); kvb[t][1]=pk2(x[2],x[3]); }
    BAR();
    { char* xs=sm+w*8192+voff;
      #define KVM(idx,kt,vt) *(f32x4*)(xs+(idx)*1024)=__builtin_amdgcn_mfma_f32_16x16x16bf16_1k(__builtin_bit_cast(s16x4,kvb[kt]),__builtin_bit_cast(s16x4,kvb[vt]),(f32x4){},0,0,0)
      KVM(0,0,2); KVM(1,1,2); KVM(2,0,3); KVM(3,1,3); KVM(4,2,3); KVM(5,0,4); KVM(6,1,4); KVM(7,2,4); }
    waitv<0>();
    LBAR();
    touch_gu(0,w-4,4);
    #pragma unroll
    for(int x=0;x<2;x++){ const int tl=(w&3)+4*x;
      const int kt=(tl==0||tl==2||tl==5)?0:((tl==1||tl==3||tl==6)?1:2), vt= tl<2?2:(tl<5?3:4);
      #pragma unroll
      for(int r4=0;r4<4;r4++){ int d=16*kt+4*g+r4, v=16*vt+c16;
        bool ok; if(v<72) ok= v>=36 && d<36 && ((d<18)==(v-36<18)); else ok=(c16==8||c16==12)? d<18 : ((c16==9||c16==13)? (d>=18&&d<36):false);
        okv[x][r4]=ok; anyok[x]|=ok; } }
    if(w<4){
      const rsrc_t rkv=mkrs(ws+O_KVP+(size_t)hp*16*8192,16*8192);
      #pragma unroll
      for(int x=0;x<2;x++){ const int tl=w+4*x;
        f32x4 sacc=*(const f32x4*)(sm+tl*1024+voff);
        #pragma unroll
        for(int ww=1;ww<8;ww++) sacc+=*(const f32x4*)(sm+ww*8192+tl*1024+voff);
        if(anyok[x]) ST16(sacc,rkv,voff,rt*8192+tl*1024); }
    }
    auto qsteps=[&](auto B,auto E){ constexpr int b0=B.v,e0=E.v;
      sfor<b0,(b0+PF-1<e0?b0+PF-1:e0)>([&](auto I){ wf[I.v%PF]=LDW1(I.v); });
      sfor<b0,e0>([&](auto I){ constexpr int i=I.v; constexpr int s=(i-90)/3,t=(i-90)%3;
        if constexpr(i+PF-1<e0) wf[(i+PF-1)%PF]=LDW1(i+PF-1);
        SB();
        if constexpr(s==0) acc[t]=MF16(wf[i%PF],af[s],(f32x4){}); else acc[t]=MF16(wf[i%PF],af[s],acc[t]);
        SB(); }); };
    qsteps(IC<90>{},IC<90+QSPLIT>{});
    if(w<4) waitv<0>();
    BAR();
    if(tid==0) __hip_atomic_store((GAS u64*)(ws+O_FKV+hp*256+rt*8),tag,__ATOMIC_RELAXED,__HIP_MEMORY_SCOPE_AGENT);
    if(w>=4){
      const char* wob=ws+O_WO+(size_t)ct3*120*1024;
      #pragma unroll
      for(int i=0;i<24;i++){ int pc=(w-4)+4*i; if(pc>93) pc=93; glds16c(wob+pc*1024,voff,smb+WOL+pc*1024); } }
    qsteps(IC<90+QSPLIT>{},IC<144>{});
    { u32x4 v; v[0]=pk2(phi(acc[0][0]),phi(acc[0][1])); v[1]=pk2(phi(acc[0][2]),phi(acc[0][3])); v[2]=pk2(phi(acc[1][0]),phi(acc[1][1])); v[3]=pk2(phi(acc[1][2]),phi(acc[1][3]));
      q01=__builtin_bit_cast(bf16x8,v);
      q2[0]=pk2(phi(acc[2][0]),phi(acc[2][1])); q2[1]=pk2(phi(acc[2][2]),phi(acc[2][3])); }
  }
  if(w==0) poll<16>(ws,O_FKV+hp*256,tag,lane);
  BAR();
  if(w<4){
    u32x4 pv[2][16]; u32x4 wb4;
    { const char* pb=ws+O_KVP+(size_t)hp*16*8192+w*1024;
      #pragma unroll
      for(int x=0;x<2;x++){ const unsigned vo=(anyok[x]?voff:0u)+x*4096;
        #pragma unroll
        for(int i=0;i<16;i++) gload16c<0>(pv[x][i],pb+i*8192,vo); }
      gload16c<0>(wb4,ws+O_WB+hp*144,(lane<9?lane:0)*16); }
    #pragma unroll
    for(int x=0;x<2;x++){
      if(x==0) waitv<17>(); else waitv<0>();
      asm volatile("" : "+v"(pv[x][0]),"+v"(pv[x][1]),"+v"(pv[x][2]),"+v"(pv[x][3]),"+v"(pv[x][4]),"+v"(pv[x][5]),"+v"(pv[x][6]),"+v"(pv[x][7]),
         "+v"(pv[x][8]),"+v"(pv[x][9]),"+v"(pv[x][10]),"+v"(pv[x][11]),"+v"(pv[x][12]),"+v"(pv[x][13]),"+v"(pv[x][14]),"+v"(pv[x][15]) :: "memory");
      f32x4 sacc=__builtin_bit_cast(f32x4,pv[x][0]);
      #pragma unroll
      for(int i=1;i<16;i++) sacc+=__builtin_bit_cast(f32x4,pv[x][i]);
      u32x2 pk; pk[0]=pk2(okv[x][0]?sacc[0]:0.f,okv[x][1]?sacc[1]:0.f); pk[1]=pk2(okv[x][2]?sacc[2]:0.f,okv[x][3]?sacc[3]:0.f);
      *(u32x2*)(sm+(w+4*x)*512+lane*8)=pk; }
    asm volatile("" : "+v"(wb4));
    if(w==0 && lane<9) *(u32x4*)(sm+4096+lane*16)=wb4;
  } else {
    __builtin_amdgcn_s_sleep(8);
    const char* wob=ws+O_WO+(size_t)ct3*120*1024;
    #pragma unroll
    for(int i=0;i<7;i++){ int pc=94+(w-4)+4*i; if(pc>119) pc=119; glds16c(wob+pc*1024,voff,smb+WOL+pc*1024); }
  }
  LBAR();
  { const char* kk=sm+lane*8; const u32x2 z2={0,0};
    #define KF(i) (*(const u32x2*)(kk+(i)*512))
    f32x4 A=MF16(cat2(KF(0),KF(1)),q01,(f32x4){});
    f32x4 B=MF16(cat2(KF(2),KF(3)),q01,(f32x4){}); B=MF16(cat2(KF(4),z2),cat2(q2,z2),B);
    f32x4 C=MF16(cat2(KF(5),KF(6)),q01,(f32x4){}); C=MF16(cat2(KF(7),z2),cat2(q2,z2),C);
    const float ra=__builtin_amdgcn_rcpf(bcast_hi(C[0])+1e-6f), rb=__builtin_amdgcn_rcpf(bcast_hi(C[1])+1e-6f);
    const float b01= g<2? ra:rb, b23= g<1? ra:rb, cc= g<2? rb:0.f;
    A*=ra; B[0]*=b01; B[1]*=b01; B[2]*=b23; B[3]*=b23; C*=cc;
    const char* wbl=sm+4096;
    const f32x4 t4=A*(*(const f32x4*)(wbl+(g>0?g-1:0)*16))+B*(*(const f32x4*)(wbl+48+g*16))+C*(*(const f32x4*)(wbl+112+(g<2?g:0)*16));
    const float mu=sum_g((t4[0]+t4[1])+(t4[2]+t4[3]));
    const float muh=lo16((unsigned)f2bf(mu));
    if(g==0){ A[0]=muh; A[1]=mu-muh; }
    const rsrc_t ro=mkrs(ws+O_O+((size_t)tt*16+hp)*1536,1536);
    ST16(((u32x4){pk2(A[0],A[1]),pk2(A[2],A[3]),pk2(B[0],B[1]),pk2(B[2],B[3])}),ro,voff,0);
    ST8(((u32x2){pk2(C[0],C[1]),pk2(C[2],C[3])}),ro,lane*8,1024);
  }
  publish(ws,O_FO+rt*256+hp*8,tag,w);
  asm volatile("" :: "v"(wm[0]),"v"(wm[1]));
  const int r1=(xcd&1)*4+(idx&3), fs=(xcd>>1)*8+(idx>>2);
  const int L0=9*(fs>>2)+2*(fs&3);
  const char* wsrc=(const char*)((w>>2)? p.wu:p.wg)+(size_t)L0*128;
  const unsigned wdst=smb+w*1024;
  unsigned vA,vC; u64 mC;
  { const int r8=lane>>3, pos=lane&7, sl=(pos>>1)^((r8>>1)&3);
    vA=r8*(F*2)+sl*32+(pos&1)*16;
    mC=__ballot(sl<2); }
  const int w3=w&3;
  const u32x4 rwA=mkrs4(wsrc+(size_t)w3*8*(F*2)), rwB=mkrs4(wsrc+(size_t)w3*72*(F*2));
  auto ldG1=[&](auto R){ constexpr int r=R.v; bl<g1::krow(r)*(F*2),r*8192>(r<16?rwA:rwB,vA,wdst); };
  {
    const int th=w&1, kq=w>>1;
    const int tq=th*2+(kq>>1), hf=kq&1;
    const int t3=r3*4+tq;
    constexpr int XS=116*1024;
    bf16x8 wpre[30];
    { const char* rb=sm+WOL+kq*30720+voff;
      #pragma unroll
      for(int i=0;i<30;i++) wpre[i]=*(const bf16x8*)(rb+i*1024); }
    u32x2 xv[3]; unsigned mxu;
    { const char* xb=(const char*)p.x+(size_t)t3*16*1152+ct3*144;
      #pragma unroll
      for(int i=0;i<3;i++){ int nt= hf? 2+i : (i<2? i:1); int n=nt*16+4*g; if(n>=72) n=68;
        asm volatile("global_load_dwordx2 %0, %1, %2" : "=v"(xv[i]) : "v"(c16*1152+n*2), "s"(xb) : "memory"); } }
    LBAR();
    #pragma unroll
    for(int i=0;i<30;i++) asm volatile("" : "+v"(wpre[i]));
    if(w==7){
      poll<16>(ws,O_FO+(r3>>1)*256,tag,lane);
    } else {
      sfor<0,PA1>(ldG1);
    }
    BAR();
    u32x4 oab[2][4]; u32x2 oc[2][4];
    { const char* ob=ws+O_O+((size_t)(r3*4+th*2)*16+kq*4)*1536;
      #pragma unroll
      for(int i=0;i<4;i++){ gload16c<0>(oab[0][i],ob+i*1536,voff); gload16c<0>(oab[1][i],ob+24576+i*1536,voff); }
      #pragma unroll
      for(int i=0;i<4;i++){ asm volatile("global_load_dwordx2 %0, %1, %2 offset:1024 sc1" : "=v"(oc[0][i]) : "v"(lane*8), "s"(ob+i*1536) : "memory");
                            asm volatile("global_load_dwordx2 %0, %1, %2 offset:1024 sc1" : "=v"(oc[1][i]) : "v"(lane*8), "s"(ob+24576+i*1536) : "memory"); }
      asm volatile("global_load_dword %0, %1, %2 sc1" : "=v"(mxu) : "v"(c16*4), "s"(ws+O_MX+(size_t)t3*64) : "memory"); }
    f32x4 acc[2][5]={};
    sfor<0,4>([&](auto KL){ constexpr int kl=KL.v;
      gwait<15-2*kl>(oab[0][kl],oab[1][kl]);
      #pragma unroll
      for(int t=0;t<2;t++)
        #pragma unroll
        for(int n=0;n<5;n++) acc[t][n]=MF16(wpre[kl*5+n],__builtin_bit_cast(bf16x8,oab[t][kl]),acc[t][n]); });
    asm volatile("s_waitcnt vmcnt(0)" : "+v"(oc[0][0]),"+v"(oc[0][1]),"+v"(oc[0][2]),"+v"(oc[0][3]),"+v"(oc[1][0]),"+v"(oc[1][1]),"+v"(oc[1][2]),"+v"(oc[1][3]),"+v"(mxu),"+v"(xv[0]),"+v"(xv[1]),"+v"(xv[2]) :: "memory");
    #pragma unroll
    for(int kl=0;kl<2;kl++)
      #pragma unroll
      for(int t=0;t<2;t++)
        #pragma unroll
        for(int n=0;n<5;n++) acc[t][n]=MF16(wpre[(4+kl)*5+n],cat2(oc[t][2*kl],oc[t][2*kl+1]),acc[t][n]);
    f32x4 a[5];
    { char* xs=sm+XS+w*5120+voff; const bool hi=kq>>1;
      #pragma unroll
      for(int n=0;n<5;n++){ f32x4 u0=acc[0][n],u1=acc[1][n]; asm volatile("" : "+v"(u0),"+v"(u1)); a[n]=hi?u1:u0; *(f32x4*)(xs+n*1024)=hi?u0:u1; } }
    LBAR();
    { const char* xr=sm+XS+(w^4)*5120+voff;
      #pragma unroll
      for(int n=0;n<5;n++) a[n]+=*(const f32x4*)(xr+n*1024); }
    #pragma unroll
    for(int n=0;n<5;n++) asm volatile("" : "+v"(a[n]));
    f32x4 keep[3];
    { char* xs=sm+XS+(w^4)*5120+voff;
      keep[0]=hf?a[2]:a[0]; keep[1]=hf?a[3]:a[1]; keep[2]=a[4];
      *(f32x4*)(xs)=hf?a[0]:a[2]; *(f32x4*)(xs+1024)=hf?a[1]:a[3]; *(f32x4*)(xs+2048)=a[4]; }
    LBAR();
    { const char* xr=sm+XS+(w^6)*5120+voff;
      const rsrc_t rc=mkrs(ws+O_C+(size_t)t3*18*1024,18*1024), rq=mkrs(ws+O_SSQ+(size_t)t3*1024,1024);
      #pragma unroll
      for(int i=0;i<3;i++) keep[i]+=*(const f32x4*)(xr+i*1024);
      const float m=__uint_as_float(mxu)+bcast_hi(keep[2][0]);
      float qs=0; unsigned cp[6];
      #pragma unroll
      for(int i=0;i<3;i++){
        const f32x4 v=keep[i];
        const float c0=(lo16(xv[i][0])+v[0])-m,c1=(hi16(xv[i][0])+v[1])-m,c2=(lo16(xv[i][1])+v[2])-m,c3=(hi16(xv[i][1])+v[3])-m;
        const float qq=(c0*c0+c1*c1)+(c2*c2+c3*c3);
        qs+= (i<2 || (hf && g<2))? qq:0.f;
        cp[2*i]=pk2(c0,c1); cp[2*i+1]=pk2(c2,c3); }
      ST16(((u32x4){cp[0],cp[1],cp[2],cp[3]}),rc,voff,(2*ct3+hf)*1024);
      if(hf && g<2) ST8(((u32x2){cp[4],cp[5]}),rc,((2*(ct3&1)+g)*16+c16)*16+((ct3>>1)&1)*8,(16+(ct3>>2))*1024);
      qs=sum_g(qs);
      if(lane<16){ ST4(qs,rq,lane*4,(ct3*2+hf)*64); if(ct3==0 && hf==0) ST4(m,mkrs(ws+O_MH+(size_t)t3*64,64),lane*4,0); }
    }
    publish(ws,O_FH+(r3>>2)*256+((r3&3)*8+ct3)*8,tag,w);
    if(b==0 && tid==64) __hip_atomic_store((GAS u64*)(ws+O_EP),tag,__ATOMIC_RELAXED,__HIP_MEMORY_SCOPE_AGENT);
  }
  const int sp=xcd>>1, r2t=(xcd&1)*8+(idx>>2), ct=idx&3;
  const int tq=w&3, kh=w>>2;
  const unsigned rb=smb+kh*36864;
  const u32x4 rw2=mkrs4((const char*)p.wd+(size_t)(sp*36+kh*18)*32*1152+ct*288);
  const u32x4 ra2=mkrs4(ws+O_ACT+((size_t)(r2t*8+tq*2)*144+sp*36+kh*18)*1024);
  const unsigned rbt=rb+tq*1024;
  unsigned wv[3], voff2;
  bf16x8 a2[g2::AR][2];
  auto loadW=[&](auto J,auto X_){ constexpr int j=J.v,x=X_.v;
    constexpr int so=((j%8)<4? 73728:0)+(j%4)*9216;
    if constexpr(x<2) bl<g2::kord(j)*32*1152,so+4096*x>(rw2,wv[x],rbt);
    else blq<g2::kord(j)*32*1152,so+8192,j%4>(rw2,wv[2],rb,tq); };
  auto loadA2=[&](auto J,auto T){ constexpr int j=J.v,t=T.v; bload16c<t*144*1024+g2::kord(j)*1024>(a2[j%g2::AR][t],ra2,voff2); };
  auto g2_setup=[&](){
    const int l2=fresh_lane();
    #pragma unroll
    for(int i=0;i<3;i++){ const int sub= i<2? tq+4*i:8; const int id=sub*64+l2; const int row=id/18; wv[i]=row*1152+(id-row*18)*16; } };
  auto g2_prologue=[&](){
    voff2=fresh_lane()*16;
    sfor<0,g2::PRO>([&](auto J){ loadA2(J,IC<0>{}); loadA2(J,IC<1>{}); }); };
  {
    using namespace g1;
    const int t1=r1*16+w*2;
    const char* abase=ws+O_C+(size_t)t1*18*1024;
    if(w==7){ sfor<0,PA1>(ldG1); }
    if(w==0) poll<32>(ws,O_FH+r1*256,tag,lane);
    else sfor<PA1,PA2>(ldG1);
    if(w==7) waitv<PA2-PA1>();
    BAR();
    bf16x8 af[18][2];
    f32x4 sq[2];
    auto loadA=[&](auto Sx,auto T){ constexpr int s=Sx.v,t=T.v; gload16c<(s&3)*1024>(af[s][t], abase+t*18*1024+(s>>2)*4096, voff); };
    sfor<0,LAK>([&](auto Sx){ loadA(Sx,IC<0>{}); loadA(Sx,IC<1>{}); });
    { const char* sp=ws+O_SSQ+(size_t)t1*1024; gload16c<0>(sq[0],sp,voff); gload16c<1024>(sq[1],sp,voff); }
    if(w==0) sfor<PA1,PA2>(ldG1);
    f32x4 acc[4][2][2];
    const unsigned lb0=smb+(4*g+q)*128+((((g&1)<<1)|(q>>1))*32)+pp*8;
    unsigned ab[4];
    bf16x8 wf[PF];
    f32x2 c1; unsigned wmd;
    u32x4 hold[2];
    const rsrc_t ors=mkrs(ws+O_ACT+(size_t)t1*144*1024,2*144*1024);
    const int obase=L0*2*1024;
    const int olast=((fs>>2)*18+16+((fs>>1)&1))*1024+(fs&1)*8;
    auto rdW=[&](auto I){ constexpr int i=I.v, T=pT(i), sl=T&3, s=pS(i), mat=i&1;
      if constexpr(T<8){
        if constexpr(s==0 && mat==0){ if constexpr(i<8) ab[sl]=lb0^(sl*32); else ab[sl]=(ab[(sl+3)&3]-2*57344)^((sl^((sl+3)&3))*32); asm volatile("" : "+v"(ab[sl])); }
        if constexpr((s==7||s==14) && mat==0){ ab[sl]+=57344; asm volatile("" : "+v"(ab[sl])); }
        wf[i%PF]=cat(trread(ab[sl]+(s%7)*8192+mat*4096),trread(ab[sl]+(s%7)*8192+mat*4096+2048));
      } else {
        if constexpr(mat==0){
          if constexpr(s==0){ ab[0]=(ab[3]-2*57344)^96; asm volatile("" : "+v"(ab[0])); }
          if constexpr(s==8||s==17){ ab[0]^=32; asm volatile("" : "+v"(ab[0])); }
          if constexpr(s==16){ ab[0]=(ab[0]^32)+65536; asm volatile("" : "+v"(ab[0])); } }
        constexpr int o=(s<16? (s&7)*8192:0)+mat*4096;
        wf[i%PF]=cat(trread(ab[0]+o),trread(ab[0]+o+2048));
      } };
    sfor<0,PF-1>(rdW);
    f32x2 e2,p2;
    auto epi=[&](auto E,auto K){ constexpr int e=E.v,kk=K.v; constexpr int ET=e>>2, t=(e>>1)&1, h=e&1;
      if constexpr(kk==0){ const f32x2 gg={acc[ET&3][0][t][2*h],acc[ET&3][0][t][2*h+1]}, u={acc[ET&3][1][t][2*h],acc[ET&3][1][t][2*h+1]};
        e2=gg*__builtin_shufflevector(c1,c1,t,t); p2=gg*u; asm volatile("" : "+v"(e2),"+v"(p2)); }
      if constexpr(kk==1){ e2[0]=__builtin_amdgcn_exp2f(e2[0]); asm volatile("" : "+v"(e2)); }
      if constexpr(kk==2){ e2[1]=__builtin_amdgcn_exp2f(e2[1]); asm volatile("" : "+v"(e2)); }
      if constexpr(kk==3){ e2=e2+1.f; asm volatile("" : "+v"(e2)); }
      if constexpr(kk==4){ e2[0]=__builtin_amdgcn_rcpf(e2[0]); asm volatile("" : "+v"(e2)); }
      if constexpr(kk==5){ e2[1]=__builtin_amdgcn_rcpf(e2[1]); asm volatile("" : "+v"(e2)); }
      if constexpr(kk==6){
        const f32x2 o=p2*e2;
        constexpr int hi=(ET==8||(ET&1)==0)?0:2;
        hold[t][hi+h]=pk2(o[0],o[1]);
        asm volatile("" : "+v"(hold[t][hi+h]));
        if constexpr(h==1){
          if constexpr(ET==8){ ST8(((u32x2){hold[t][0],hold[t][1]}),ors,voff,olast+t*144*1024); }
          else if constexpr((ET&1)==1){ ST16(hold[t],ors,voff,obase+t*144*1024+(ET>>1)*1024); }
        }
      } };
    auto slot=[&](auto M){ constexpr int m=M.v;
      if constexpr(m>=255 && m<283) epi(IC<(m-255)/7>{},IC<(m-255)%7>{});
      if constexpr(m>=284){ constexpr int e=4+(m-284)/13, d=(m-284)%13; if constexpr(e<32 && d<7) epi(IC<e>{},IC<d>{}); } };
    sfor<0,324>([&](auto I){ constexpr int i=I.v; constexpr int T=pT(i), s=pS(i), mat=i&1;
      if constexpr(i<126 && i==firstUse(0,s)){
        gwait<nAafter(i,s)+nB(i)+(s<LAK?2:0)>(af[s][0],af[s][1]);
        if constexpr(s==LAK){ asm volatile("" : "+v"(sq[0]),"+v"(sq[1]));
          #pragma unroll
          for(int t=0;t<2;t++){ const float ss=ssq_sum(sq[t],lane); c1[t]=-1.44269504089f*__builtin_amdgcn_rsqf(ss*(1.f/576.f)+1e-5f); }
          asm volatile("" : "+v"(c1)); } }
      if constexpr(i==40||i==80||i==120||i==146) BAR();
      if constexpr(i==139){ waitv<nB(139)-10>(); BAR(); }
      if constexpr(i==159){ waitv<nB(159)-15>(); BAR(); }
      if constexpr(i==169||i==224||i==283){ waitv<0>(); BAR(); }
      if constexpr(i==224){ if(w==0) flag_store(ws,O_FA+r1*256+fs*8,tag); }
      if constexpr(i==266){ if(w==0) poll<8>(ws,O_FA+((xcd&1)*4+(idx>>3))*256+(xcd>>1)*64,tag,fresh_lane()); }
      if constexpr(i==276) g2_setup();
      if constexpr(i==290) BAR();
      if constexpr(i==224){ vC=vA+(8-2*(fs&3))*128+(fs&3)*32+((vA&32)? 288*(F*2)-32:0); asm volatile("" : "+v"(vC)); }
      if constexpr(i+PF-1<324) rdW(IC<i+PF-1>{});
      SB();
      if constexpr(s==0) acc[T&3][mat][0]=MF16(wf[i%PF],af[s][0],(f32x4){}); else acc[T&3][mat][0]=MF16(wf[i%PF],af[s][0],acc[T&3][mat][0]);
      SB();
      slot(IC<2*i>{});
      SB();
      if constexpr(s==0) acc[T&3][mat][1]=MF16(wf[i%PF],af[s][1],(f32x4){}); else acc[T&3][mat][1]=MF16(wf[i%PF],af[s][1],acc[T&3][mat][1]);
      SB();
      slot(IC<2*i+1>{});
      if constexpr(i==170){ const int l_=fresh_lane(); unsigned o=(w*21+(l_<21?l_:20))*128; if(o>20608) o=20608; gtouch(wmd,(const char*)p.wd+(size_t)b*20736,o); }
      if constexpr(i==225) asm_use(wmd);
      if constexpr(i>=290 && i<314 && (i&1)==0) loadW(IC<(i-290)/6>{},IC<((i-290)/2)%3>{});
      if constexpr(i<40 && i%8==3 && PA2+i/8<18) ldG1(IC<PA2+i/8>{});
      if constexpr(aAt(i)>=0) loadA(IC<aAt(i)/2>{},IC<aAt(i)%2>{});
      if constexpr(rB(i)>=0){ constexpr int r=rB(i); bl<krow(r)*(F*2)+128,r*8192>(r<16?rwA:rwB,vA,wdst); }
      if constexpr(rC(i)>=0){ constexpr int r=rC(i); blm<krow(r<8?r:16)*(F*2),r*8192>(r<8?rwA:rwB,vC,wdst,mC); }
      SB();
    });
    SB();
    g2_prologue();
    sfor<32,36>([&](auto E){ sfor<0,7>([&](auto K){ epi(E,K); }); });
  }
  {
    using namespace g2;
    const int lane=fresh_lane(); const unsigned voff=lane*16; const int g=lane>>4, q=(lane>>2)&3, pp=lane&3, c16=lane&15;
    const int tl=w>>2, pr=w&3;
    const bool extra= pr==3;
    const int tk0=r2t*128+sp*32+tl*16;
    u32x2 hv[3]; int go[3]; u32x4 s4; unsigned mhu;
    auto resid=[&](){
      const char* hb=ws+O_C+(size_t)(tk0>>4)*18432;
      #pragma unroll
      for(int y=0;y<3;y++){ const int nt= y<2? 2*pr+y:8; const int nl=nt*16+g*4;
        go[y]=c16*1152+(ct*144+nl)*2;
        const int c3=ct*2+(nl>=72), o72=nl>=72? nl-72:nl;
        int kc,gp,hfh; if(o72<64){ kc=2*c3+(o72>>5); hfh=(o72>>4)&1; gp=(o72>>2)&3; } else { kc=16+(c3>>2); gp=2*(c3&1)+((o72-64)>>2); hfh=(c3>>1)&1; }
        gload8c(hv[y],hb,kc*1024+(gp*16+c16)*16+hfh*8); }
      gload16c<0>(s4,ws+O_SSQ+(size_t)tk0*64,voff);
      gload4c(mhu,ws+O_MH+(size_t)tk0*4,c16*4); };
    f32x4 acc[9][2];
    unsigned lwb[2];
    #pragma unroll
    for(int i=0;i<2;i++){ lwb[i]=rb+(4*g+q)*288+pp*8+(i?0:73728); asm volatile("" : "+v"(lwb[i])); }
    bf16x8 wf[PF];
    #define LDW2(i) cat(trread(lwb[(((i)/9)%8)/4]+((((i)/9)%8)%4)*9216+((i)%9)*32),trread(lwb[(((i)/9)%8)/4]+((((i)/9)%8)%4)*9216+((i)%9)*32+16*288))
    waitv<0>(); BAR();
    if(w==1) flag_store(ws,O_FA2+r1*256+fs*8,tag);
    sfor<0,PF-1>([&](auto I){ wf[I.v]=LDW2(I.v); });
    sfor<0,NP>([&](auto I){ constexpr int i=I.v; constexpr int j=i/9, nt=i%9;
      if constexpr((i+BO)%18==0 && i+BO<NP){
        waitv<vmc(i,-1,(i+BO)/9+1)>();
        BAR(); }
      if constexpr(i==36){ if(w==0) poll<8>(ws,O_FA2+(r2t>>1)*256+sp*64,tag,lane); }
      if constexpr(nt==0) gwait<vmc(i,j,-1)>(a2[j%AR][0],a2[j%AR][1]);
      if constexpr(i+PF-1<NP) wf[(i+PF-1)%PF]=LDW2(i+PF-1);
      SB();
      if constexpr(j==0) acc[nt][0]=MF16(wf[i%PF],a2[j%AR][0],(f32x4){}); else acc[nt][0]=MF16(wf[i%PF],a2[j%AR][0],acc[nt][0]);
      if constexpr(j==0) acc[nt][1]=MF16(wf[i%PF],a2[j%AR][1],(f32x4){}); else acc[nt][1]=MF16(wf[i%PF],a2[j%AR][1],acc[nt][1]);
      SB();
      if constexpr(actAt(i)>=0) loadA2(IC<actAt(i)/2>{},IC<actAt(i)%2>{});
      if constexpr(wAt(i)>=0) loadW(IC<wAt(i)/3>{},IC<wAt(i)%3>{});
      if constexpr(i==116) resid();
      SB();
    });
    asm volatile("s_waitcnt vmcnt(0)" : "+v"(hv[0]),"+v"(hv[1]),"+v"(hv[2]),"+v"(s4),"+v"(mhu) :: "memory");
    BAR();
    { char* xs=sm+(w*9)*1024+voff;
      #pragma unroll
      for(int n=0;n<9;n++){ f32x4 v= kh? acc[n][0]:acc[n][1]; *(f32x4*)(xs+n*1024)=v; } }
    LBAR();
    f32x4 part[10];
    { const char* xr=sm+((w^4)*9)*1024+voff;
      #pragma unroll
      for(int n=0;n<9;n++){ f32x4 mine= kh? acc[n][1]:acc[n][0]; part[n]=mine+*(const f32x4*)(xr+n*1024); }
      part[9]=part[8]; }
    const int grp=r2t*4+ct;
    const rsrc_t xr=mkrs(ws+O_X+(size_t)grp*160*1024,160*1024);
    if(tq!=sp){
      #pragma unroll
      for(int n=0;n<5;n++) ST16(((u32x4){pk2(part[2*n][0],part[2*n][1]),pk2(part[2*n][2],part[2*n][3]),pk2(part[2*n+1][0],part[2*n+1][1]),pk2(part[2*n+1][2],part[2*n+1][3])}),xr,voff,((tq*4+sp)*10+kh*5+n)*1024);
    } else {
      char* xs=sm+(72+kh*10)*1024+voff;
      #pragma unroll
      for(int n=0;n<9;n++) *(f32x4*)(xs+n*1024)=part[n];
    }
    float r2=__builtin_amdgcn_rcpf(ssq_sum(__builtin_bit_cast(f32x4,s4),lane)*(1.f/576.f)+1e-5f);
    f32x4 hr[3];
    #pragma unroll
    for(int y=0;y<3;y++){ const float mh=__uint_as_float(mhu); hr[y]=(f32x4){lo16(hv[y][0])+mh,hi16(hv[y][0])+mh,lo16(hv[y][1])+mh,hi16(hv[y][1])+mh}; }
    asm volatile("" : "+v"(r2),"+v"(hr[0]),"+v"(hr[1]),"+v"(hr[2]));
    publish(ws,O_FX+grp*128+sp*8,tag,w);
    if(w==0) poll<4>(ws,O_FX+grp*128,tag,lane);
    f32x4 own[3];
    #pragma unroll
    for(int y=0;y<3;y++) own[y]=*(const f32x4*)(sm+(72+tl*10+(y<2?2*pr+y:8))*1024+voff);
    BAR();
    u32x4 rv[3]; u32x2 rx[3];
    #pragma unroll
    for(int z=0;z<3;z++){ const int src=(sp+1+z)&3; rv[z]=LD16(xr,voff,((sp*4+src)*10+tl*5+pr)*1024); }
    if(extra){
      #pragma unroll
      for(int z=0;z<3;z++){ const int src=(sp+1+z)&3; rx[z]=LD8(xr,voff,((sp*4+src)*10+tl*5+4)*1024); } }
    char* const ob=(char*)p.out+(size_t)tk0*1152;
    #pragma unroll
    for(int y=0;y<3;y++){ if(y<2 || extra){
      f32x4 v=own[y];
      #pragma unroll
      for(int z=0;z<3;z++){ const unsigned a0= y<2? rv[z][2*(y&1)]:rx[z][0], a1= y<2? rv[z][2*(y&1)+1]:rx[z][1]; v[0]+=lo16(a0); v[1]+=hi16(a0); v[2]+=lo16(a1); v[3]+=hi16(a1); }
      v=v*r2+hr[y];
      *(u32x2*)(ob+go[y])=(u32x2){pk2(v[0],v[1]),pk2(v[2],v[3])}; } }
  }
}
}

extern "C" void kernel_launch(void* const* d_in, const int* in_sizes, int n_in,
                              void* d_out, int out_size, void* d_ws, size_t ws_size,
                              hipStream_t stream) {
  using namespace k;
  Params p;
  p.x=(const u16*)d_in[0]; p.wqkv=(const u16*)d_in[1]; p.wo=(const u16*)d_in[2];
  p.wg=(const u16*)d_in[4]; p.wu=(const u16*)d_in[5]; p.wd=(const u16*)d_in[6];
  p.out=(u16*)d_out; p.ws=(char*)d_ws;
  fused_k<<<256,512,0,stream>>>(p);
}
```

```cpp
#include <hip/hip_runtime.h>
#include <hip/hip_bf16.h>
typedef unsigned short u16;
typedef unsigned long long u64;
typedef short s16x4 __attribute__((ext_vector_type(4)));
typedef __bf16 bf16x8 __attribute__((ext_vector_type(8)));
typedef __bf16 bf16x2 __attribute__((ext_vector_type(2)));
typedef float f32x4 __attribute__((ext_vector_type(4)));
typedef float f32x2 __attribute__((ext_vector_type(2)));
typedef unsigned u32x4 __attribute__((ext_vector_type(4)));
typedef unsigned u32x2 __attribute__((ext_vector_type(2)));
#define LAS __attribute__((address_space(3)))
#define GAS __attribute__((address_space(1)))
#define DI __device__ __forceinline__

#define WARM_SLEEP 20
#define QSPLIT 24
#define PA1 7
#define PA2 13
#define CODE_KB 48
#define T2A 40
#define WSL 24

namespace k {
constexpr int S=2048, D=576, F=4608;
constexpr int TAG_LDS=155*1024;
DI int fresh_lane(){ int l; asm volatile("v_mbcnt_lo_u32_b32 %0, -1, 0\n\tv_mbcnt_hi_u32_b32 %0, -1, %0" : "=v"(l)); return l; }
DI u16 f2bf(float f){ __bf16 b=(__bf16)f; return __builtin_bit_cast(u16,b); }
DI unsigned pk2(float lo,float hi){ bf16x2 v; v[0]=(__bf16)lo; v[1]=(__bf16)hi; return __builtin_bit_cast(unsigned,v); }
DI float lo16(unsigned v){ return __uint_as_float(v<<16); }
DI float hi16(unsigned v){ return __uint_as_float(v&0xffff0000u); }
DI float phi(float t){ return (t>0.f? t+1.f: __builtin_amdgcn_exp2f(t*1.44269504089f))+1e-6f; }
DI float bcast_hi(float x){ unsigned a=__float_as_uint(x); auto r=__builtin_amdgcn_permlane32_swap(a,a,false,false); return __uint_as_float(r[1]); }
DI float sum_g(float x){
  unsigned a=__float_as_uint(x); auto r=__builtin_amdgcn_permlane32_swap(a,a,false,false); x=__uint_as_float(r[0])+__uint_as_float(r[1]);
  a=__float_as_uint(x); auto r2=__builtin_amdgcn_permlane16_swap(a,a,false,false); return __uint_as_float(r2[0])+__uint_as_float(r2[1]); }
template<int CTRL> DI float dppf(float x){ return __uint_as_float(__builtin_amdgcn_update_dpp(0u,__float_as_uint(x),CTRL,0xf,0xf,false)); }
DI float ssq_sum(f32x4 v,int){
  const int l=fresh_lane(); const int k=(l>>2)&3;
  #pragma unroll
  for(int r=0;r<4;r++){ float x=v[r]; x+=dppf<0x124>(x); x+=dppf<0x128>(x); v[r]=x; }
  const float t=sum_g(k==0? v[0] : (k==1? v[1] : (k==2? v[2]:v[3])));
  return __uint_as_float(__builtin_amdgcn_ds_bpermute((((l&3)<<2)|k)*4,__float_as_uint(t))); }
DI float dot2(unsigned a,unsigned b,float c){ return __builtin_amdgcn_fdot2_f32_bf16(__builtin_bit_cast(bf16x2,a),__builtin_bit_cast(bf16x2,b),c,false); }
DI s16x4 trread(unsigned a){ return __builtin_amdgcn_ds_read_tr16_b64_v4i16((LAS s16x4*)(size_t)a); }
DI bf16x8 cat(s16x4 a,s16x4 b){ return __builtin_bit_cast(bf16x8,__builtin_shufflevector(a,b,0,1,2,3,4,5,6,7)); }
DI bf16x8 cat2(u32x2 a,u32x2 b){ return __builtin_bit_cast(bf16x8,(u32x4){a[0],a[1],b[0],b[1]}); }
#define BAR() do{ asm volatile("":::"memory"); __builtin_amdgcn_s_barrier(); asm volatile("":::"memory"); }while(0)
#define LBAR() do{ asm volatile("s_waitcnt lgkmcnt(0)":::"memory"); __builtin_amdgcn_s_barrier(); asm volatile("":::"memory"); }while(0)
#define MF16(a,bb,c) __builtin_amdgcn_mfma_f32_16x16x32_bf16(a,bb,c,0,0,0)
#define SB() __builtin_amdgcn_sched_barrier(0)
template<int I> struct IC{ static constexpr int v=I; };
template<int B,int E,class Fn> DI void sfor(Fn&& f){ if constexpr(B<E){ f(IC<B>{}); sfor<B+1,E>(f); } }
DI void glds16c(const void* sbase, unsigned voff, unsigned lds){
  unsigned keep;
  asm volatile("s_mov_b32 %0, m0\n\ts_mov_b32 m0, %3\n\ts_nop 0\n\tglobal_load_lds_dwordx4 %1, %2 sc1\n\ts_mov_b32 m0, %0" : "=&s"(keep) : "v"(voff), "s"(sbase), "s"(lds) : "memory");
}
DI u32x4 mkrs4(const void* p){ const u64 a=(u64)p; u32x4 r; r[0]=__builtin_amdgcn_readfirstlane((unsigned)a); r[1]=__builtin_amdgcn_readfirstlane((unsigned)(a>>32)); r[2]=0x7fffffffu; r[3]=0x00020000u; return r; }
template<int SOFF,int LOFF> DI void bl(u32x4 rs,unsigned voff,unsigned lds){ unsigned t;
  asm volatile("s_add_i32 m0, %3, %5\n\ts_mov_b32 %0, %4\n\tbuffer_load_dwordx4 %1, %2, %0 offen lds" : "=&s"(t) : "v"(voff), "s"(rs), "s"(lds), "i"(SOFF), "i"(LOFF) : "memory","scc"); }
template<int SOFF,int LOFF> DI void blc(u32x4 rs,unsigned voff,unsigned lds){ unsigned t;
  asm volatile("s_add_i32 m0, %3, %5\n\ts_mov_b32 %0, %4\n\tbuffer_load_dwordx4 %1, %2, %0 offen sc1 lds" : "=&s"(t) : "v"(voff), "s"(rs), "s"(lds), "i"(SOFF), "i"(LOFF) : "memory","scc"); }
template<int SOFF,int LOFF> DI void blm(u32x4 rs,unsigned voff,unsigned lds,u64 mask){ unsigned t;
  asm volatile("s_add_i32 m0, %3, %5\n\ts_mov_b32 %0, %4\n\ts_mov_b64 exec, %6\n\tbuffer_load_dwordx4 %1, %2, %0 offen lds\n\ts_mov_b64 exec, -1" : "=&s"(t) : "v"(voff), "s"(rs), "s"(lds), "i"(SOFF), "i"(LOFF), "s"(mask) : "memory","scc"); }
template<int SOFF,int LOFF,int EQ> DI void blq(u32x4 rs,unsigned voff,unsigned lds,int sel){ unsigned t;
  asm volatile("s_cmp_lg_u32 %6, %7\n\ts_cbranch_scc1 1f\n\ts_add_i32 m0, %3, %5\n\ts_mov_b32 %0, %4\n\tbuffer_load_dwordx4 %1, %2, %0 offen lds\n1:" : "=&s"(t) : "v"(voff), "s"(rs), "s"(lds), "i"(SOFF), "i"(LOFF), "s"(sel), "i"(EQ) : "memory","scc"); }
template<int SOFF,class T> DI void bload16c(T& r,u32x4 rs,unsigned voff){ unsigned t;
  asm volatile("s_mov_b32 %1, %4\n\tbuffer_load_dwordx4 %0, %2, %3, %1 offen sc1" : "=v"(r), "=&s"(t) : "v"(voff), "s"(rs), "i"(SOFF) : "memory"); }
template<int OFF,class T> DI void gload16(T& r,const void* sbase,unsigned voff){ asm volatile("global_load_dwordx4 %0, %1, %2 offset:%3" : "=v"(r) : "v"(voff), "s"(sbase), "i"(OFF) : "memory"); }
template<int OFF,class T> DI void gload16c(T& r,const void* sbase,unsigned voff){ asm volatile("global_load_dwordx4 %0, %1, %2 offset:%3 sc1" : "=v"(r) : "v"(voff), "s"(sbase), "i"(OFF) : "memory"); }
DI void gload8c(u32x2& r,const void* sbase,unsigned voff){ asm volatile("global_load_dwordx2 %0, %1, %2 sc1" : "=v"(r) : "v"(voff), "s"(sbase) : "memory"); }
DI void gload4c(unsigned& r,const void* sbase,unsigned voff){ asm volatile("global_load_dword %0, %1, %2 sc1" : "=v"(r) : "v"(voff), "s"(sbase) : "memory"); }
DI void gtouch(unsigned& r,const void* sbase,unsigned voff){ asm volatile("global_load_dword %0, %1, %2" : "=v"(r) : "v"(voff), "s"(sbase) : "memory"); }
DI void asm_use(unsigned r){ asm volatile("" :: "v"(r)); }
template<int N,class T> DI void gwait(T& a,T& b){ asm volatile("s_waitcnt vmcnt(%2)" : "+v"(a),"+v"(b) : "i"(N) : "memory"); }
template<int N> DI void waitv(){ asm volatile("s_waitcnt vmcnt(%0)" :: "i"(N) : "memory"); }
typedef __amdgpu_buffer_rsrc_t rsrc_t;
DI rsrc_t mkrs(const void* p,unsigned bytes){ return __builtin_amdgcn_make_buffer_rsrc((void*)p,0,bytes,0x00020000); }
#define ST16(v,rs,vo,so) __builtin_amdgcn_raw_buffer_store_b128(__builtin_bit_cast(u32x4,v),rs,vo,so,16)
#define ST8(v,rs,vo,so)  __builtin_amdgcn_raw_buffer_store_b64(__builtin_bit_cast(u32x2,v),rs,vo,so,16)
#define ST4(v,rs,vo,so)  __builtin_amdgcn_raw_buffer_store_b32(__float_as_uint(v),rs,vo,so,16)
#define LD16(rs,vo,so) __builtin_amdgcn_raw_buffer_load_b128(rs,vo,so,16)
#define LD8(rs,vo,so)  __builtin_amdgcn_raw_buffer_load_b64(rs,vo,so,16)

constexpr size_t O_EP=0;
constexpr size_t O_F0 =256;
constexpr size_t O_FW =O_F0+2048;
constexpr size_t O_FWO=O_FW+2048;
constexpr size_t O_FKV=O_FWO+1024;
constexpr size_t O_FO =O_FKV+16*256;
constexpr size_t O_FH =O_FO+16*256;
constexpr size_t O_FA =O_FH+8*256;
constexpr size_t O_FA2=O_FA+8*256;
constexpr size_t O_FX =O_FA2+8*256;
constexpr size_t O_KVP=32768;
constexpr size_t O_XN =O_KVP+(size_t)256*8192;
constexpr size_t O_MX =O_XN+(size_t)S*D*2;
constexpr size_t O_WB =O_MX+(size_t)S*4;
constexpr size_t O_WQ =O_WB+4096;
constexpr size_t O_WO =O_WQ+(size_t)16*144*1024;
constexpr size_t O_O  =O_WO+(size_t)8*120*1024;
constexpr size_t O_MU =O_O+(size_t)128*16*1536;
constexpr size_t O_MH =O_MU+(size_t)S*64;
constexpr size_t O_C  =O_MH+(size_t)S*4;
constexpr size_t O_SSQ=O_C+(size_t)S*D*2;
constexpr size_t O_ACT=O_SSQ+(size_t)S*64;
constexpr size_t O_X  =O_ACT+(size_t)S*F*2;
constexpr size_t O_END=O_X+(size_t)64*160*1024;
struct Params { const u16 *x,*wqkv,*wo,*wg,*wu,*wd; u16* out; char* ws; };

DI void publish(char* ws,size_t off,u64 tag,int w){
  asm volatile("s_waitcnt vmcnt(0) lgkmcnt(0)":::"memory");
  BAR();
  if(w==0 && fresh_lane()==0) __hip_atomic_store((GAS u64*)(ws+off),tag,__ATOMIC_RELAXED,__HIP_MEMORY_SCOPE_AGENT);
}
DI void flag_store(char* ws,size_t off,u64 tag){ if(fresh_lane()==0) __hip_atomic_store((GAS u64*)(ws+off),tag,__ATOMIC_RELAXED,__HIP_MEMORY_SCOPE_AGENT); }
DI void pollp(const GAS u64* p,u64 tag){
  for(unsigned spins=0; spins<(1u<<20); ++spins){
    u64 v=__hip_atomic_load(p,__ATOMIC_RELAXED,__HIP_MEMORY_SCOPE_AGENT);
    if(__all(v==tag)) break;
    __builtin_amdgcn_s_sleep(1);
  }
  __builtin_amdgcn_fence(__ATOMIC_ACQUIRE,"wavefront");
}
template<int N> DI void poll(char* ws,size_t off,u64 tag,int lane){ pollp((const GAS u64*)(ws+off)+(lane<N?lane:0),tag); }

namespace f1 {
constexpr int PF=6, XA=8, WA=6;
constexpr int wr(int r){ return r<WA? -1 : 5*(r-WA)+3; }
constexpr int roundAt(int i){ for(int r=WA;r<18;r++) if(wr(r)==i) return r; return -1; }
constexpr int vmc(int iw,int xs,int pw){
  int cnt=0;
  for(int k=0;k<XA;k++){ if(k<WA){ if(8*k<pw) cnt=0; else cnt++; } if(k<=xs) cnt=0; else cnt++; }
  for(int i=0;i<iw;i++){
    if(i%5==1 && i/5+XA<18){ if(i/5+XA<=xs) cnt=0; else cnt++; }
    int r=roundAt(i); if(r>=0){ if(8*r<pw) cnt=0; else cnt++; }
  }
  return cnt;
}
constexpr int lpos(int p){ return p<90? p : p+12; }
}
namespace g1 {
constexpr int PF=4, LAK=4;
constexpr int pT(int i){ return i<120? (i%8)/2 : (i<144? (i-120)/6 : 4+(i-144)/36); }
constexpr int pS(int i){ return i<120? i/8 : (i<144? 15+((i-120)%6)/2 : ((i-144)%36)/2); }
constexpr int krow(int kc){ return kc<16? 72*(kc>>1)+32*(kc&1) : 288*(kc-16)+64; }
constexpr int iB(int r){ return r<10? 81+4*r : (r<15? 122+3*(r-10) : 147+2*(r-15)); }
constexpr int iA(int s,int t){ return s<LAK? -1 : 5*(s-LAK)+1+2*t; }
constexpr int aAt(int i){ for(int s=LAK;s<18;s++) for(int t=0;t<2;t++) if(iA(s,t)==i) return s*2+t; return -1; }
constexpr int nAafter(int i,int s){ int n=0; for(int x=s+1;x<18;x++) for(int t=0;t<2;t++) if(iA(x,t)<i) n++; return n; }
constexpr int iC(int r){ return 225+4*r; }
constexpr int rB(int i){ for(int r=0;r<18;r++) if(iB(r)==i) return r; return -1; }
constexpr int rC(int i){ for(int r=0;r<9;r++) if(iC(r)==i) return r; return -1; }
constexpr int nB(int i){ int n=0; for(int r=0;r<18;r++) if(iB(r)<i) n++; return n; }
constexpr int firstUse(int T,int s){ for(int i=0;i<324;i++) if(pT(i)==T&&pS(i)==s) return i; return -1; }
}
namespace g2 {
constexpr int LW=5, PF=5, BO=8, NP=162, AR=6;
constexpr int kord(int j){ constexpr int t[18]={0,1,4,5,8,9,12,13, 2,3,6,7,10,11,14,15,16,17}; return t[j]; }
constexpr int PRO=4;
constexpr int actStep(int j,int t){ return j<PRO? -1 : (j<6? 5*(j-PRO)+2+2*t : ((j>=8&&j<=10)? 50+2*(j-8)+t : (j<8? 9*(j-4)+2+3*t : 9*(j-5)+2+3*t))); }
constexpr int wStep(int j,int x){ return j<PRO? -1 : (j<8? 5*(j-PRO)+(x==2?3:x) : 9*(j-LW)+(x==0?1:(x==1?3:6))); }
constexpr int vmc(int iw,int ja,int jw){
  int best=63;
  for(int tq=0;tq<4;tq++){
    int cnt=0;
    for(int j=0;j<PRO;j++){ int n=2+((j%4)==tq); if(j<=jw) cnt=0; else cnt+=n; if(j<=ja) cnt=0; else cnt+=2; }
    for(int i=0;i<iw;i++){
      for(int j=PRO;j<18;j++) for(int t=0;t<2;t++) if(actStep(j,t)==i){ if(j<=ja) cnt=0; else cnt++; }
      for(int j=PRO;j<18;j++) for(int x=0;x<3;x++) if(wStep(j,x)==i && (x<2||(j%4)==tq)){ if(j<=jw) cnt=0; else cnt++; }
    }
    if(cnt<best) best=cnt;
  }
  return best;
}
constexpr int wAt(int i){ for(int j=PRO;j<18;j++) for(int x=0;x<3;x++) if(wStep(j,x)==i) return j*3+x; return -1; }
constexpr int actAt(int i){ for(int j=PRO;j<18;j++) for(int t=0;t<2;t++) if(actStep(j,t)==i) return j*2+t; return -1; }
}

__global__ __launch_bounds__(512,2) void fused_k(Params p){
  __shared__ __attribute__((aligned(16))) char sm[160*1024];
  u64 pc0; asm volatile("s_getpc_b64 %0" : "=s"(pc0));
  const int tid=threadIdx.x, lane=tid&63, w=__builtin_amdgcn_readfirstlane(tid>>6);
  const int b=blockIdx.x, xcd=b&7, idx=b>>3;
  const unsigned smb=(unsigned)(size_t)(LAS char*)sm;
  const unsigned voff=lane*16;
  const int g=lane>>4, q=(lane>>2)&3, pp=lane&3, c16=lane&15;
  char* const ws=p.ws;
  u64 tag;
  const int hp=(xcd&1)*8+(idx>>2), rt=(xcd>>1)*4+(idx&3);
  unsigned wm[3]={0,0,0};
  auto touch_gu=[&](int slot,int kind,int n,int lo){
    const int per= kind? 5:4, lim= kind? 179:143; if(n>lim) n=lim;
    const int grp=n/per, r=n-grp*per; int ln=grp*9+(kind? (r<4? 2*r+1:8) : 2*r); const int mt=ln>=162; ln-=mt*162;
    const char* src=(const char*)(mt? p.wu : p.wg)+(size_t)b*20736+ln*128;
    asm volatile("s_cmp_lt_u32 %2, %3\n\ts_cbranch_scc1 1f\n\tglobal_load_dword %0, %1, off\n1:" : "+v"(wm[slot]) : "v"(src), "s"(w), "s"(lo) : "memory","scc"); };
  unsigned okm, src16;
  {
    char* const smw=sm+w*16384;
    const unsigned smwb=smb+w*16384;
    if(w<2){
      u32x2 ep; { const unsigned z=0; asm volatile("global_load_dwordx2 %0, %1, %2 sc1" : "=v"(ep) : "v"(z), "s"(ws+(w?(size_t)O_XN:0)) : "memory"); }
      u32x4 v[5];
      { const char* xb=(const char*)p.x+(size_t)(b*8+w*4)*1152; const unsigned v0=g*1152+c16*16, v4=g*1152+(64+(c16&7))*16;
        gload16<0>(v[0],xb,v0); gload16<256>(v[1],xb,v0); gload16<512>(v[2],xb,v0); gload16<768>(v[3],xb,v0); gload16<0>(v[4],xb,v4); }
      asm volatile("s_waitcnt vmcnt(0)" : "+v"(ep),"+v"(v[0]),"+v"(v[1]),"+v"(v[2]),"+v"(v[3]),"+v"(v[4]) :: "memory");
      if(tid==0) *(u32x2*)(sm+TAG_LDS)=ep;
      float s1=0;
      #pragma unroll
      for(int i=0;i<5;i++){ const unsigned one=(i<4||c16<8)?0x3f803f80u:0u;
        #pragma unroll
        for(int j=0;j<4;j++) s1=dot2(v[i][j],one,s1); }
      s1+=__shfl_xor(s1,1); s1+=__shfl_xor(s1,2); s1+=__shfl_xor(s1,4); s1+=__shfl_xor(s1,8);
      const float m=s1*(1.f/576.f);
      float f[5][8]; float s2=0,s2t=0;
      #pragma unroll
      for(int i=0;i<5;i++)
        #pragma unroll
        for(int j=0;j<4;j++){ f[i][2*j]=lo16(v[i][j])-m; f[i][2*j+1]=hi16(v[i][j])-m;
          if(i<4){ s2=fmaf(f[i][2*j],f[i][2*j],s2); s2=fmaf(f[i][2*j+1],f[i][2*j+1],s2); } else { s2t=fmaf(f[i][2*j],f[i][2*j],s2t); s2t=fmaf(f[i][2*j+1],f[i][2*j+1],s2t); } }
      s2+= c16<8? s2t:0.f;
      s2+=__shfl_xor(s2,1); s2+=__shfl_xor(s2,2); s2+=__shfl_xor(s2,4); s2+=__shfl_xor(s2,8);
      const float r=__builtin_amdgcn_rsqf(s2*(1.f/576.f)+1e-5f);
      #pragma unroll
      for(int i=0;i<5;i++){ u32x4 o;
        #pragma unroll
        for(int j=0;j<4;j++) o[j]=pk2(f[i][2*j]*r,f[i][2*j+1]*r);
        *(u32x4*)(smw+(i<4? c16+16*i: 64+(c16&7))*80+g*16)=o; }
      asm volatile("s_waitcnt lgkmcnt(0)":::"memory");
      const rsrc_t rx=mkrs(ws+O_XN+(size_t)(b>>1)*18432+(b&1)*128+w*64,18432);
      #pragma unroll
      for(int i=0;i<5;i++){ const int ch=(lane>>2)+16*i; if(ch<72) ST16(*(const u32x4*)(smw+ch*80+pp*16),rx,ch*256+pp*16,0); }
      const rsrc_t rm=mkrs(ws+O_MX+(size_t)b*32+w*16,16);
      if(c16==0) ST4(m,rm,g*4,0);
    } else if(w<4){
      if(b<144){
        const int s=b>>3, hq=b&7, h2=w-2;
        u32x4 v[8];
        const char* src=(const char*)p.wqkv+(size_t)s*32*3456+hq*144+h2*64;
        int lo_[8];
        #pragma unroll
        for(int i=0;i<8;i++){ const int r=g+4*i; int c=c16<15?c16:14; const int part=c/5, cc=c-part*5; lo_[i]=r*272+c*16; gload16<0>(v[i],src,r*3456+part*1152+cc*16); }
        asm volatile("s_waitcnt vmcnt(0)" : "+v"(v[0]),"+v"(v[1]),"+v"(v[2]),"+v"(v[3]),"+v"(v[4]),"+v"(v[5]),"+v"(v[6]),"+v"(v[7]) :: "memory");
        #pragma unroll
        for(int i=0;i<8;i++) *(u32x4*)(smw+lo_[i])=v[i];
        asm volatile("s_waitcnt lgkmcnt(0)":::"memory");
        const rsrc_t rw=mkrs(ws+O_WQ+(size_t)(hq*2+h2)*144*1024,144*1024);
        #pragma unroll
        for(int t=0;t<8;t++){
          const int vc=(t<3? 16*t:16*(t-3))+4*pp;
          const int col= t<3? (vc<36? vc:0) : (vc<36? 40+vc : (vc<72? 80+vc-36 : 0));
          const unsigned ba=smwb+(8*g+q)*272+(h2*4+col)*2;
          u32x4 o=__builtin_bit_cast(u32x4,cat(trread(ba),trread(ba+4*272)));
          if((t<3? 16*t:16*(t-3))+c16>=(t<3?36:72)) o=(u32x4){0,0,0,0};
          ST16(o,rw,voff,(t<3? 90+s*3+t : s*5+t-3)*1024); }
      } else if(b<240){
        const int jb=b-144, kap=jb>>2, ct=(jb&3)*2+(w-2);
        const int kq=kap/6, kl=kap%6;
        u32x4 v[5];
        const char* src=(const char*)p.wo+ct*144;
        bool okr[5]; int lo_[5];
        #pragma unroll
        for(int i=0;i<5;i++){ int e=lane+64*i; if(e>287) e=287; const int r=e/9, c=e-r*9, row=r&15, hi=r>>4;
          int kr; if(kl<4){ kr=(4*kq+kl)*36+(hi? 12+row: row-4); okr[i]= hi|| row>=4; } else { kr=(4*kq+2*(kl-4)+hi)*36+28+row; okr[i]=row<8; }
          lo_[i]=r*160+c*16; gload16<0>(v[i],src,(okr[i]?kr:0)*1152+c*16); }
        asm volatile("s_waitcnt vmcnt(0)" : "+v"(v[0]),"+v"(v[1]),"+v"(v[2]),"+v"(v[3]),"+v"(v[4]) :: "memory");
        #pragma unroll
        for(int i=0;i<5;i++) *(u32x4*)(smw+lo_[i])= okr[i]? v[i]:(u32x4){0,0,0,0};
        asm volatile("s_waitcnt lgkmcnt(0)":::"memory");
        const rsrc_t rw=mkrs(ws+O_WO+(size_t)ct*120*1024,120*1024);
        #pragma unroll
        for(int nt=0;nt<5;nt++){
          const int col=nt*16+4*((nt==4)?(pp&1):pp);
          const unsigned ba=smwb+(4*g+q)*160+col*2;
          u32x4 o=__builtin_bit_cast(u32x4,cat(trread(ba),trread(ba+16*160)));
          if(nt==4 && c16>=8) o=(u32x4){ (kl<4 && g==0 && (c16&3)==0)? 0x3f803f80u:0u ,0u,0u,0u};
          ST16(o,rw,voff,(kap*5+nt)*1024); }
      }
    } else if(w==4 && rt<9){
      u32x4 v[5];
      const char* rp=(const char*)p.wo+(size_t)(hp*36+rt*4)*1152;
      const unsigned v0=g*1152+c16*16, v4=g*1152+(64+(c16&7))*16;
      gload16<0>(v[0],rp,v0); gload16<256>(v[1],rp,v0); gload16<512>(v[2],rp,v0); gload16<768>(v[3],rp,v0); gload16<0>(v[4],rp,v4);
      asm volatile("s_waitcnt vmcnt(0)" : "+v"(v[0]),"+v"(v[1]),"+v"(v[2]),"+v"(v[3]),"+v"(v[4]) :: "memory");
      float sacc=0;
      #pragma unroll
      for(int i=0;i<5;i++){ const unsigned one=(i<4||c16<8)?0x3f803f80u:0u;
        #pragma unroll
        for(int j=0;j<4;j++) sacc=dot2(v[i][j],one,sacc); }
      sacc+=__shfl_xor(sacc,1); sacc+=__shfl_xor(sacc,2); sacc+=__shfl_xor(sacc,4); sacc+=__shfl_xor(sacc,8);
      if(c16==0) ST4(sacc*(1.f/576.f),mkrs(ws+O_WB+(size_t)(hp*36+rt*4)*4,16),g*4,0);
    }
    if(w==7){
      asm volatile("global_load_dword %0, %1, %2" : "+v"(wm[1]) : "v"(((idx%(CODE_KB/8))*64+lane)*128), "s"(pc0&~(u64)255) : "memory"); }
    else if(w>=5){ __builtin_amdgcn_s_sleep(WARM_SLEEP); touch_gu(1,1,(w-5)*T2A+(lane<T2A?lane:0),5); }
    if(w<5) asm volatile("s_waitcnt vmcnt(0) lgkmcnt(0)":::"memory");
    BAR();
    { const u32x2 e=*(const u32x2*)(sm+TAG_LDS); unsigned lo=__builtin_amdgcn_readfirstlane(e[0]), hi=__builtin_amdgcn_readfirstlane(e[1]); tag=(((u64)hi<<32)|lo)+1ull; }
    if(tid<2 && (tid==0 || b<240)){
      const size_t fo= tid==0? O_F0+b*8 : (b<144? O_FW+(b&7)*256+(b>>3)*8 : O_FWO+((b-144)&3)*256+((b-144)>>2)*8);
      __hip_atomic_store((GAS u64*)(ws+fo),tag,__ATOMIC_RELAXED,__HIP_MEMORY_SCOPE_AGENT); }
    { okm=0;
      const int tl= w==0? 0 : (w==1? (c16<8?2:5) : (w==2? 3 : (w==3? 6 : (g<3? 1+3*g:1))));
      const int gg= w<4? g:0;
      const int kt=(tl==0||tl==2||tl==5)?0:((tl==1||tl==3||tl==6)?1:2), vt= tl<2?2:(tl<5?3:4);
      #pragma unroll
      for(int r4=0;r4<4;r4++){ int d=16*kt+4*gg+r4, v=16*vt+c16;
        bool ok; if(v<72) ok= v>=36 && d<36 && ((d<18)==(v-36<18)); else ok=(c16==8||c16==12)? d<18 : ((c16==9||c16==13)? (d>=18&&d<36):false);
        okm|= ok? (1u<<r4):0u; }
      if(w>=4 && g==3) okm=0;
      src16=tl*64+gg*16+c16;
      asm volatile("" : "+v"(okm),"+v"(src16)); }
    if(w==0) pollp((const GAS u64*)(ws+(lane<16? O_F0+rt*128+lane*8 : (lane<34? O_FW+(hp>>1)*256+(lane-16)*8 : O_FWO+((idx&7)>>1)*256+(lane<58?lane-34:0)*8))),tag);
    BAR();
  }
  const int tt=rt*8+w;
  const int ct3=idx&7, r3=xcd*4+(idx>>3);
  constexpr int WOL=8*1024;
  const u32x4 rwo=mkrs4(ws+O_WO+(size_t)ct3*120*1024+(w>=1?w-1:0)*1024);
  bf16x8 q01; u32x2 q2;
  {
    using namespace f1;
    const char* wbase=ws+O_WQ+(size_t)hp*144*1024+w*1024;
    const char* abase=ws+O_XN+(size_t)tt*18*1024;
    bf16x8 af[18];
    auto ldW=[&](auto R){ constexpr int r=R.v; const int pc=r*8+w; glds16c(wbase+r*8192,voff,smb+((r<11||(r==11&&0))? pc : (pc<90? pc:pc+12))*1024); };
    auto ldX=[&](auto Sx){ constexpr int s=Sx.v; gload16c<(s&3)*1024>(af[s],abase+(s>>2)*4096,voff); };
    sfor<0,XA>([&](auto K){ if constexpr(K.v<WA) ldW(K); ldX(K); });
    f32x4 acc[8];
    unsigned lwb[4];
    #pragma unroll
    for(int i=0;i<4;i++){ lwb[i]=smb+voff+i*49152; asm volatile("" : "+v"(lwb[i])); }
    bf16x8 wf[PF];
    #define LDW1(i) (*(const LAS bf16x8*)((const LAS char*)(size_t)lwb[lpos(i)/48]+(lpos(i)%48)*1024))
    #define WAITP(m,n) asm volatile("s_waitcnt vmcnt(%2)" : "+v"(af[2*(m)]),"+v"(af[2*(m)+1]) : "i"(n) : "memory")
    WAITP(0,vmc(0,1,10)); BAR();
    sfor<0,PF-1>([&](auto I){ wf[I.v]=LDW1(I.v); });
    sfor<0,90>([&](auto I){ constexpr int i=I.v; constexpr int s=i/5,t=i%5;
      if constexpr(i%10==4 && i/10<8){ constexpr int m=i/10+1; WAITP(m,(m==8?0:vmc(i,2*m+1,10*(m+1)))); BAR(); }
      if constexpr(i==75) touch_gu(0,0,(w-5)*48+(lane<48?lane:0),5);
      if constexpr(i+PF-1<90) wf[(i+PF-1)%PF]=LDW1(i+PF-1);
      SB();
      if constexpr(s==0) acc[3+t]=MF16(af[s],wf[i%PF],(f32x4){}); else acc[3+t]=MF16(af[s],wf[i%PF],acc[3+t]);
      SB();
      if constexpr(t==1 && s+XA<18) ldX(IC<s+XA>{});
      if constexpr(roundAt(i)>=0) ldW(IC<roundAt(i)>{});
      SB();
    });
    u32x2 kvb[5];
    #pragma unroll
    for(int t=0;t<5;t++){ float x[4];
      #pragma unroll
      for(int r4=0;r4<4;r4++){ float a=acc[3+t][r4];
        if(t<2) a=phi(a); else if(t==2) a= c16<4? phi(a):a; else if(t==4) a= c16<8? a : 1.f;
        x[r4]=a; }
      kvb[t][0]=pk2(x[0],x[1]); kvb[t][1]=pk2(x[2],x[3]); }
    BAR();
    { char* xs=sm+w*8192+voff;
      #define KVM(idx,kt,vt) *(f32x4*)(xs+(idx)*1024)=__builtin_amdgcn_mfma_f32_16x16x16bf16_1k(__builtin_bit_cast(s16x4,kvb[kt]),__builtin_bit_cast(s16x4,kvb[vt]),(f32x4){},0,0,0)
      KVM(0,0,2); KVM(1,1,2); KVM(2,0,3); KVM(3,1,3); KVM(4,2,3); KVM(5,0,4); KVM(6,1,4); KVM(7,2,4); }
    LBAR();
    if(w<5){
      const rsrc_t rkv=mkrs(ws+O_KVP+(size_t)hp*16*8192,16*8192);
      f32x4 sacc=*(const f32x4*)(sm+src16*16);
      #pragma unroll
      for(int ww=1;ww<8;ww++) sacc+=*(const f32x4*)(sm+ww*8192+src16*16);
      if(okm) ST16(sacc,rkv,voff,rt*8192+w*1024);
    }
    auto qsteps=[&](auto B,auto E){ constexpr int b0=B.v,e0=E.v;
      sfor<b0,(b0+PF-1<e0?b0+PF-1:e0)>([&](auto I){ wf[I.v%PF]=LDW1(I.v); });
      sfor<b0,e0>([&](auto I){ constexpr int i=I.v; constexpr int s=(i-90)/3,t=(i-90)%3;
        if constexpr(i+PF-1<e0) wf[(i+PF-1)%PF]=LDW1(i+PF-1);
        SB();
        if constexpr(s==0) acc[t]=MF16(wf[i%PF],af[s],(f32x4){}); else acc[t]=MF16(wf[i%PF],af[s],acc[t]);
        SB(); }); };
    qsteps(IC<90>{},IC<90+QSPLIT>{});
    if(w<5) waitv<0>();
    BAR();
    if(tid==0) __hip_atomic_store((GAS u64*)(ws+O_FKV+hp*256+rt*8),tag,__ATOMIC_RELAXED,__HIP_MEMORY_SCOPE_AGENT);
    touch_gu(2,1,2*T2A+(w-6)*64+lane,6);
    if(w>=1) sfor<0,13>([&](auto I){ blc<I.v*7168,WOL+I.v*7168>(rwo,voff,smb+(w-1)*1024); });
    if(w<4) *(u32x4*)(sm+w*1024+voff)=(u32x4){0,0,0,0};
    qsteps(IC<90+QSPLIT>{},IC<144>{});
    { u32x4 v; v[0]=pk2(phi(acc[0][0]),phi(acc[0][1])); v[1]=pk2(phi(acc[0][2]),phi(acc[0][3])); v[2]=pk2(phi(acc[1][0]),phi(acc[1][1])); v[3]=pk2(phi(acc[1][2]),phi(acc[1][3]));
      q01=__builtin_bit_cast(bf16x8,v);
      q2[0]=pk2(phi(acc[2][0]),phi(acc[2][1])); q2[1]=pk2(phi(acc[2][2]),phi(acc[2][3])); }
  }
  if(w==0) poll<16>(ws,O_FKV+hp*256,tag,lane);
  LBAR();
  if(w<5){
    u32x4 pv[16]; u32x4 wb4;
    { const char* pb=ws+O_KVP+(size_t)hp*16*8192+w*1024; const unsigned vo=okm?voff:64u;
      #pragma unroll
      for(int i=0;i<16;i++) gload16c<0>(pv[i],pb+i*8192,vo);
      gload16c<0>(wb4,ws+O_WB+hp*144,(lane<9?lane:0)*16); }
    asm volatile("s_waitcnt vmcnt(0)" : "+v"(pv[0]),"+v"(pv[1]),"+v"(pv[2]),"+v"(pv[3]),"+v"(pv[4]),"+v"(pv[5]),"+v"(pv[6]),"+v"(pv[7]),
       "+v"(pv[8]),"+v"(pv[9]),"+v"(pv[10]),"+v"(pv[11]),"+v"(pv[12]),"+v"(pv[13]),"+v"(pv[14]),"+v"(pv[15]),"+v"(wb4) :: "memory");
    f32x4 sacc=__builtin_bit_cast(f32x4,pv[0]);
    #pragma unroll
    for(int i=1;i<16;i++) sacc+=__builtin_bit_cast(f32x4,pv[i]);
    u32x2 pk; pk[0]=pk2(okm&1u?sacc[0]:0.f,okm&2u?sacc[1]:0.f); pk[1]=pk2(okm&4u?sacc[2]:0.f,okm&8u?sacc[3]:0.f);
    if(okm) *(u32x2*)(sm+src16*8)=pk;
    if(w==0 && lane<9) *(u32x4*)(sm+4096+lane*16)=wb4;
  } else {
    __builtin_amdgcn_s_sleep(WSL);
    sfor<0,10>([&](auto I){ blc<(86+I.v*3)*1024,WOL+(86+I.v*3)*1024>(rwo,voff,smb+(w-1)*1024); });
  }
  LBAR();
  { const char* kk=sm+lane*8; const u32x2 z2={0,0};
    #define KF(i) (*(const u32x2*)(kk+(i)*512))
    f32x4 A=MF16(cat2(KF(0),KF(1)),q01,(f32x4){});
    f32x4 B=MF16(cat2(KF(2),KF(3)),q01,(f32x4){}); B=MF16(cat2(KF(4),z2),cat2(q2,z2),B);
    f32x4 C=MF16(cat2(KF(5),KF(6)),q01,(f32x4){}); C=MF16(cat2(KF(7),z2),cat2(q2,z2),C);
    const float ra=__builtin_amdgcn_rcpf(bcast_hi(C[0])+1e-6f), rb=__builtin_amdgcn_rcpf(bcast_hi(C[1])+1e-6f);
    const float b01= g<2? ra:rb, b23= g<1? ra:rb, cc= g<2? rb:0.f;
    A*=ra; B[0]*=b01; B[1]*=b01; B[2]*=b23; B[3]*=b23; C*=cc;
    const char* wbl=sm+4096;
    const f32x4 t4=A*(*(const f32x4*)(wbl+(g>0?g-1:0)*16))+B*(*(const f32x4*)(wbl+48+g*16))+C*(*(const f32x4*)(wbl+112+(g<2?g:0)*16));
    const float mu=sum_g((t4[0]+t4[1])+(t4[2]+t4[3]));
    const float muh=lo16((unsigned)f2bf(mu));
    if(g==0){ A[0]=muh; A[1]=mu-muh; }
    const rsrc_t ro=mkrs(ws+O_O+((size_t)tt*8+(hp>>1))*3072,3072);
    ST16(((u32x4){pk2(A[0],A[1]),pk2(A[2],A[3]),pk2(B[0],B[1]),pk2(B[2],B[3])}),ro,voff,(hp&1)*1024);
    if(g<2) ST8(((u32x2){pk2(C[0],C[1]),pk2(C[2],C[3])}),ro,voff,2048+(hp&1)*8);
  }
  publish(ws,O_FO+rt*256+hp*8,tag,w);
  asm volatile("" :: "v"(wm[0]),"v"(wm[1]),"v"(wm[2]));
  const int r1=(xcd&1)*4+(idx&3), fs=(xcd>>1)*8+(idx>>2);
  const int L0=9*(fs>>2)+2*(fs&3);
  const char* wsrc=(const char*)((w>>2)? p.wu:p.wg)+(size_t)L0*128;
  const unsigned wdst=smb+w*1024;
  unsigned vA,vC; u64 mC;
  { const int r8=lane>>3, pos=lane&7, sl=(pos>>1)^((r8>>1)&3);
    vA=r8*(F*2)+sl*32+(pos&1)*16;
    mC=__ballot(sl<2); }
  const int w3=w&3;
  const u32x4 rwA=mkrs4(wsrc+(size_t)w3*8*(F*2)), rwB=mkrs4(wsrc+(size_t)w3*72*(F*2));
  auto ldG1=[&](auto R){ constexpr int r=R.v; bl<g1::krow(r)*(F*2),r*8192>(r<16?rwA:rwB,vA,wdst); };
  {
    const int th=w&1, kq=w>>1;
    const int tq=th*2+(kq>>1), hf=kq&1;
    const int t3=r3*4+tq;
    constexpr int XS=116*1024;
    bf16x8 wpre[30];
    { const char* rb=sm+WOL+kq*30720+voff;
      #pragma unroll
      for(int i=0;i<30;i++) wpre[i]=*(const bf16x8*)(rb+i*1024); }
    u32x2 xv[3]; unsigned mxu;
    { const char* xb=(const char*)p.x+(size_t)t3*16*1152+ct3*144;
      #pragma unroll
      for(int i=0;i<3;i++){ int nt= hf? 2+i : (i<2? i:1); int n=nt*16+4*g; if(n>=72) n=68;
        asm volatile("global_load_dwordx2 %0, %1, %2" : "=v"(xv[i]) : "v"(c16*1152+n*2), "s"(xb) : "memory"); } }
    LBAR();
    #pragma unroll
    for(int i=0;i<30;i++) asm volatile("" : "+v"(wpre[i]));
    if(w==7){
      poll<16>(ws,O_FO+(r3>>1)*256,tag,lane);
    } else {
      sfor<0,PA1>(ldG1);
    }
    BAR();
    u32x4 oab[2][4], oc[2][2];
    { const char* ob=ws+O_O+((size_t)(r3*4+th*2)*8+kq*2)*3072; const unsigned vc=(lane&31)*16;
      #pragma unroll
      for(int i=0;i<4;i++){ gload16c<0>(oab[0][i],ob+(i>>1)*3072+(i&1)*1024,voff); gload16c<0>(oab[1][i],ob+24576+(i>>1)*3072+(i&1)*1024,voff); }
      #pragma unroll
      for(int i=0;i<2;i++){ gload16c<2048>(oc[0][i],ob+i*3072,vc); gload16c<2048>(oc[1][i],ob+24576+i*3072,vc); }
      asm volatile("global_load_dword %0, %1, %2 sc1" : "=v"(mxu) : "v"(c16*4), "s"(ws+O_MX+(size_t)t3*64) : "memory"); }
    f32x4 acc[2][5]={};
    sfor<0,4>([&](auto KL){ constexpr int kl=KL.v;
      gwait<11-2*kl>(oab[0][kl],oab[1][kl]);
      #pragma unroll
      for(int t=0;t<2;t++)
        #pragma unroll
        for(int n=0;n<5;n++) acc[t][n]=MF16(wpre[kl*5+n],__builtin_bit_cast(bf16x8,oab[t][kl]),acc[t][n]); });
    asm volatile("s_waitcnt vmcnt(0)" : "+v"(oc[0][0]),"+v"(oc[0][1]),"+v"(oc[1][0]),"+v"(oc[1][1]),"+v"(mxu),"+v"(xv[0]),"+v"(xv[1]),"+v"(xv[2]) :: "memory");
    #pragma unroll
    for(int kl=0;kl<2;kl++)
      #pragma unroll
      for(int t=0;t<2;t++)
        #pragma unroll
        for(int n=0;n<5;n++) acc[t][n]=MF16(wpre[(4+kl)*5+n],__builtin_bit_cast(bf16x8,oc[t][kl]),acc[t][n]);
    f32x4 a[5];
    { char* xs=sm+XS+w*5120+voff; const bool hi=kq>>1;
      #pragma unroll
      for(int n=0;n<5;n++){ f32x4 u0=acc[0][n],u1=acc[1][n]; asm volatile("" : "+v"(u0),"+v"(u1)); a[n]=hi?u1:u0; *(f32x4*)(xs+n*1024)=hi?u0:u1; } }
    LBAR();
    { const char* xr=sm+XS+(w^4)*5120+voff;
      #pragma unroll
      for(int n=0;n<5;n++) a[n]+=*(const f32x4*)(xr+n*1024); }
    #pragma unroll
    for(int n=0;n<5;n++) asm volatile("" : "+v"(a[n]));
    f32x4 keep[3];
    { char* xs=sm+XS+(w^4)*5120+voff;
      keep[0]=hf?a[2]:a[0]; keep[1]=hf?a[3]:a[1]; keep[2]=a[4];
      *(f32x4*)(xs)=hf?a[0]:a[2]; *(f32x4*)(xs+1024)=hf?a[1]:a[3]; *(f32x4*)(xs+2048)=a[4]; }
    LBAR();
    { const char* xr=sm+XS+(w^6)*5120+voff;
      const rsrc_t rc=mkrs(ws+O_C+(size_t)t3*18*1024,18*1024), rq=mkrs(ws+O_SSQ+(size_t)t3*1024,1024);
      #pragma unroll
      for(int i=0;i<3;i++) keep[i]+=*(const f32x4*)(xr+i*1024);
      const float m=__uint_as_float(mxu)+bcast_hi(keep[2][0]);
      float qs=0; unsigned cp[6];
      #pragma unroll
      for(int i=0;i<3;i++){
        const f32x4 v=keep[i];
        const float c0=(lo16(xv[i][0])+v[0])-m,c1=(hi16(xv[i][0])+v[1])-m,c2=(lo16(xv[i][1])+v[2])-m,c3=(hi16(xv[i][1])+v[3])-m;
        const float qq=(c0*c0+c1*c1)+(c2*c2+c3*c3);
        qs+= (i<2 || (hf && g<2))? qq:0.f;
        cp[2*i]=pk2(c0,c1); cp[2*i+1]=pk2(c2,c3); }
      ST16(((u32x4){cp[0],cp[1],cp[2],cp[3]}),rc,voff,(2*ct3+hf)*1024);
      if(hf && g<2) ST8(((u32x2){cp[4],cp[5]}),rc,((2*(ct3&1)+g)*16+c16)*16+((ct3>>1)&1)*8,(16+(ct3>>2))*1024);
      qs=sum_g(qs);
      if(lane<16){ ST4(qs,rq,lane*4,(ct3*2+hf)*64); if(ct3==0 && hf==0) ST4(m,mkrs(ws+O_MH+(size_t)t3*64,64),lane*4,0); }
    }
    publish(ws,O_FH+(r3>>2)*256+((r3&3)*8+ct3)*8,tag,w);
    if(b==0 && tid==64) __hip_atomic_store((GAS u64*)(ws+O_EP),tag,__ATOMIC_RELAXED,__HIP_MEMORY_SCOPE_AGENT);
  }
  const int sp=xcd>>1, r2t=(xcd&1)*8+(idx>>2), ct=idx&3;
  const int tq=w&3, kh=w>>2;
  const unsigned rb=smb+kh*36864;
  const u32x4 rw2=mkrs4((const char*)p.wd+(size_t)(sp*36+kh*18)*32*1152+ct*288);
  const u32x4 ra2=mkrs4(ws+O_ACT+((size_t)(r2t*8+tq*2)*144+sp*36+kh*18)*1024);
  const unsigned rbt=rb+tq*1024;
  unsigned wv[3], voff2;
  bf16x8 a2[g2::AR][2];
  auto loadW=[&](auto J,auto X_){ constexpr int j=J.v,x=X_.v;
    constexpr int so=((j%8)<4? 73728:0)+(j%4)*9216;
    if constexpr(x<2) bl<g2::kord(j)*32*1152,so+4096*x>(rw2,wv[x],rbt);
    else blq<g2::kord(j)*32*1152,so+8192,j%4>(rw2,wv[2],rb,tq); };
  auto loadA2=[&](auto J,auto T){ constexpr int j=J.v,t=T.v; bload16c<t*144*1024+g2::kord(j)*1024>(a2[j%g2::AR][t],ra2,voff2); };
  auto g2_setup=[&](){
    const int l2=fresh_lane();
    #pragma unroll
    for(int i=0;i<3;i++){ const int sub= i<2? tq+4*i:8; const int id=sub*64+l2; const int row=id/18; wv[i]=row*1152+(id-row*18)*16; } };
  auto g2_prologue=[&](){
    voff2=fresh_lane()*16;
    sfor<0,g2::PRO>([&](auto J){ loadA2(J,IC<0>{}); loadA2(J,IC<1>{}); }); };
  {
    using namespace g1;
    const int t1=r1*16+w*2;
    const char* abase=ws+O_C+(size_t)t1*18*1024;
    if(w==7){ sfor<0,PA1>(ldG1); }
    if(w==0) poll<32>(ws,O_FH+r1*256,tag,lane);
    else sfor<PA1,PA2>(ldG1);
    if(w==7) waitv<PA2-PA1>();
    BAR();
    bf16x8 af[18][2];
    f32x4 sq[2];
    auto loadA=[&](auto Sx,auto T){ constexpr int s=Sx.v,t=T.v; gload16c<(s&3)*1024>(af[s][t], abase+t*18*1024+(s>>2)*4096, voff); };
    sfor<0,LAK>([&](auto Sx){ loadA(Sx,IC<0>{}); loadA(Sx,IC<1>{}); });
    { const char* sp=ws+O_SSQ+(size_t)t1*1024; gload16c<0>(sq[0],sp,voff); gload16c<1024>(sq[1],sp,voff); }
    if(w==0) sfor<PA1,PA2>(ldG1);
    f32x4 acc[4][2][2];
    const unsigned lb0=smb+(4*g+q)*128+((((g&1)<<1)|(q>>1))*32)+pp*8;
    unsigned ab[4];
    bf16x8 wf[PF];
    f32x2 c1; unsigned wmd;
    u32x4 hold[2];
    const rsrc_t ors=mkrs(ws+O_ACT+(size_t)t1*144*1024,2*144*1024);
    const int obase=L0*2*1024;
    const int olast=((fs>>2)*18+16+((fs>>1)&1))*1024+(fs&1)*8;
    auto rdW=[&](auto I){ constexpr int i=I.v, T=pT(i), sl=T&3, s=pS(i), mat=i&1;
      if constexpr(T<8){
        if constexpr(s==0 && mat==0){ if constexpr(i<8) ab[sl]=lb0^(sl*32); else ab[sl]=(ab[(sl+3)&3]-2*57344)^((sl^((sl+3)&3))*32); asm volatile("" : "+v"(ab[sl])); }
        if constexpr((s==7||s==14) && mat==0){ ab[sl]+=57344; asm volatile("" : "+v"(ab[sl])); }
        wf[i%PF]=cat(trread(ab[sl]+(s%7)*8192+mat*4096),trread(ab[sl]+(s%7)*8192+mat*4096+2048));
      } else {
        if constexpr(mat==0){
          if constexpr(s==0){ ab[0]=(ab[3]-2*57344)^96; asm volatile("" : "+v"(ab[0])); }
          if constexpr(s==8||s==17){ ab[0]^=32; asm volatile("" : "+v"(ab[0])); }
          if constexpr(s==16){ ab[0]=(ab[0]^32)+65536; asm volatile("" : "+v"(ab[0])); } }
        constexpr int o=(s<16? (s&7)*8192:0)+mat*4096;
        wf[i%PF]=cat(trread(ab[0]+o),trread(ab[0]+o+2048));
      } };
    sfor<0,PF-1>(rdW);
    f32x2 e2,p2;
    auto epi=[&](auto E,auto K){ constexpr int e=E.v,kk=K.v; constexpr int ET=e>>2, t=(e>>1)&1, h=e&1;
      if constexpr(kk==0){ const f32x2 gg={acc[ET&3][0][t][2*h],acc[ET&3][0][t][2*h+1]}, u={acc[ET&3][1][t][2*h],acc[ET&3][1][t][2*h+1]};
        e2=gg*__builtin_shufflevector(c1,c1,t,t); p2=gg*u; asm volatile("" : "+v"(e2),"+v"(p2)); }
      if constexpr(kk==1){ e2[0]=__builtin_amdgcn_exp2f(e2[0]); asm volatile("" : "+v"(e2)); }
      if constexpr(kk==2){ e2[1]=__builtin_amdgcn_exp2f(e2[1]); asm volatile("" : "+v"(e2)); }
      if constexpr(kk==3){ e2=e2+1.f; asm volatile("" : "+v"(e2)); }
      if constexpr(kk==4){ e2[0]=__builtin_amdgcn_rcpf(e2[0]); asm volatile("" : "+v"(e2)); }
      if constexpr(kk==5){ e2[1]=__builtin_amdgcn_rcpf(e2[1]); asm volatile("" : "+v"(e2)); }
      if constexpr(kk==6){
        const f32x2 o=p2*e2;
        constexpr int hi=(ET==8||(ET&1)==0)?0:2;
        hold[t][hi+h]=pk2(o[0],o[1]);
        asm volatile("" : "+v"(hold[t][hi+h]));
        if constexpr(h==1){
          if constexpr(ET==8){ ST8(((u32x2){hold[t][0],hold[t][1]}),ors,voff,olast+t*144*1024); }
          else if constexpr((ET&1)==1){ ST16(hold[t],ors,voff,obase+t*144*1024+(ET>>1)*1024); }
        }
      } };
    auto slot=[&](auto M){ constexpr int m=M.v;
      if constexpr(m>=255 && m<283) epi(IC<(m-255)/7>{},IC<(m-255)%7>{});
      if constexpr(m>=284){ constexpr int e=4+(m-284)/13, d=(m-284)%13; if constexpr(e<32 && d<7) epi(IC<e>{},IC<d>{}); } };
    sfor<0,324>([&](auto I){ constexpr int i=I.v; constexpr int T=pT(i), s=pS(i), mat=i&1;
      if constexpr(i<126 && i==firstUse(0,s)){
        gwait<nAafter(i,s)+nB(i)+(s<LAK?2:0)>(af[s][0],af[s][1]);
        if constexpr(s==LAK){ asm volatile("" : "+v"(sq[0]),"+v"(sq[1]));
          #pragma unroll
          for(int t=0;t<2;t++){ const float ss=ssq_sum(sq[t],lane); c1[t]=-1.44269504089f*__builtin_amdgcn_rsqf(ss*(1.f/576.f)+1e-5f); }
          asm volatile("" : "+v"(c1)); } }
      if constexpr(i==40||i==80||i==120||i==146) BAR();
      if constexpr(i==139){ waitv<nB(139)-10>(); BAR(); }
      if constexpr(i==159){ waitv<nB(159)-15>(); BAR(); }
      if constexpr(i==169||i==224||i==283){ waitv<0>(); BAR(); }
      if constexpr(i==224){ if(w==0) flag_store(ws,O_FA+r1*256+fs*8,tag); }
      if constexpr(i==266){ if(w==0) poll<8>(ws,O_FA+((xcd&1)*4+(idx>>3))*256+(xcd>>1)*64,tag,fresh_lane()); }
      if constexpr(i==276) g2_setup();
      if constexpr(i==290) BAR();
      if constexpr(i==224){ vC=vA+(8-2*(fs&3))*128+(fs&3)*32+((vA&32)? 288*(F*2)-32:0); asm volatile("" : "+v"(vC)); }
      if constexpr(i+PF-1<324) rdW(IC<i+PF-1>{});
      SB();
      if constexpr(s==0) acc[T&3][mat][0]=MF16(wf[i%PF],af[s][0],(f32x4){}); else acc[T&3][mat][0]=MF16(wf[i%PF],af[s][0],acc[T&3][mat][0]);
      SB();
      slot(IC<2*i>{});
      SB();
      if constexpr(s==0) acc[T&3][mat][1]=MF16(wf[i%PF],af[s][1],(f32x4){}); else acc[T&3][mat][1]=MF16(wf[i%PF],af[s][1],acc[T&3][mat][1]);
      SB();
      slot(IC<2*i+1>{});
      if constexpr(i==170){ const int l_=fresh_lane(); unsigned o=(w*21+(l_<21?l_:20))*128; if(o>20608) o=20608; gtouch(wmd,(const char*)p.wd+(size_t)b*20736,o); }
      if constexpr(i==225) asm_use(wmd);
      if constexpr(i>=290 && i<314 && (i&1)==0) loadW(IC<(i-290)/6>{},IC<((i-290)/2)%3>{});
      if constexpr(i<40 && i%8==3 && PA2+i/8<18) ldG1(IC<PA2+i/8>{});
      if constexpr(aAt(i)>=0) loadA(IC<aAt(i)/2>{},IC<aAt(i)%2>{});
      if constexpr(rB(i)>=0){ constexpr int r=rB(i); bl<krow(r)*(F*2)+128,r*8192>(r<16?rwA:rwB,vA,wdst); }
      if constexpr(rC(i)>=0){ constexpr int r=rC(i); blm<krow(r<8?r:16)*(F*2),r*8192>(r<8?rwA:rwB,vC,wdst,mC); }
      SB();
    });
    SB();
    g2_prologue();
    sfor<32,36>([&](auto E){ sfor<0,7>([&](auto K){ epi(E,K); }); });
  }
  {
    using namespace g2;
    const int lane=fresh_lane(); const unsigned voff=lane*16; const int g=lane>>4, q=(lane>>2)&3, pp=lane&3, c16=lane&15;
    const int tl=w>>2, pr=w&3;
    const bool extra= pr==3;
    const int tk0=r2t*128+sp*32+tl*16;
    u32x2 hv[3]; int go[3]; u32x4 s4; unsigned mhu;
    auto resid=[&](){
      const char* hb=ws+O_C+(size_t)(tk0>>4)*18432;
      #pragma unroll
      for(int y=0;y<3;y++){ const int nt= y<2? 2*pr+y:8; const int nl=nt*16+g*4;
        go[y]=c16*1152+(ct*144+nl)*2;
        const int c3=ct*2+(nl>=72), o72=nl>=72? nl-72:nl;
        int kc,gp,hfh; if(o72<64){ kc=2*c3+(o72>>5); hfh=(o72>>4)&1; gp=(o72>>2)&3; } else { kc=16+(c3>>2); gp=2*(c3&1)+((o72-64)>>2); hfh=(c3>>1)&1; }
        gload8c(hv[y],hb,kc*1024+(gp*16+c16)*16+hfh*8); }
      gload16c<0>(s4,ws+O_SSQ+(size_t)tk0*64,voff);
      gload4c(mhu,ws+O_MH+(size_t)tk0*4,c16*4); };
    f32x4 acc[9][2];
    unsigned lwb[2];
    #pragma unroll
    for(int i=0;i<2;i++){ lwb[i]=rb+(4*g+q)*288+pp*8+(i?0:73728); asm volatile("" : "+v"(lwb[i])); }
    bf16x8 wf[PF];
    #define LDW2(i) cat(trread(lwb[(((i)/9)%8)/4]+((((i)/9)%8)%4)*9216+((i)%9)*32),trread(lwb[(((i)/9)%8)/4]+((((i)/9)%8)%4)*9216+((i)%9)*32+16*288))
    waitv<0>(); BAR();
    if(w==1) flag_store(ws,O_FA2+r1*256+fs*8,tag);
    sfor<0,PF-1>([&](auto I){ wf[I.v]=LDW2(I.v); });
    sfor<0,NP>([&](auto I){ constexpr int i=I.v; constexpr int j=i/9, nt=i%9;
      if constexpr((i+BO)%18==0 && i+BO<NP){
        waitv<vmc(i,-1,(i+BO)/9+1)>();
        BAR(); }
      if constexpr(i==36){ if(w==0) poll<8>(ws,O_FA2+(r2t>>1)*256+sp*64,tag,lane); }
      if constexpr(nt==0) gwait<vmc(i,j,-1)>(a2[j%AR][0],a2[j%AR][1]);
      if constexpr(i+PF-1<NP) wf[(i+PF-1)%PF]=LDW2(i+PF-1);
      SB();
      if constexpr(j==0) acc[nt][0]=MF16(wf[i%PF],a2[j%AR][0],(f32x4){}); else acc[nt][0]=MF16(wf[i%PF],a2[j%AR][0],acc[nt][0]);
      if constexpr(j==0) acc[nt][1]=MF16(wf[i%PF],a2[j%AR][1],(f32x4){}); else acc[nt][1]=MF16(wf[i%PF],a2[j%AR][1],acc[nt][1]);
      SB();
      if constexpr(actAt(i)>=0) loadA2(IC<actAt(i)/2>{},IC<actAt(i)%2>{});
      if constexpr(wAt(i)>=0) loadW(IC<wAt(i)/3>{},IC<wAt(i)%3>{});
      if constexpr(i==116) resid();
      SB();
    });
    asm volatile("s_waitcnt vmcnt(0)" : "+v"(hv[0]),"+v"(hv[1]),"+v"(hv[2]),"+v"(s4),"+v"(mhu) :: "memory");
    BAR();
    { char* xs=sm+(w*9)*1024+voff;
      #pragma unroll
      for(int n=0;n<9;n++){ f32x4 v= kh? acc[n][0]:acc[n][1]; *(f32x4*)(xs+n*1024)=v; } }
    LBAR();
    f32x4 part[10];
    { const char* xr=sm+((w^4)*9)*1024+voff;
      #pragma unroll
      for(int n=0;n<9;n++){ f32x4 mine= kh? acc[n][1]:acc[n][0]; part[n]=mine+*(const f32x4*)(xr+n*1024); }
      part[9]=part[8]; }
    const int grp=r2t*4+ct;
    const rsrc_t xr=mkrs(ws+O_X+(size_t)grp*160*1024,160*1024);
    if(tq!=sp){
      #pragma unroll
      for(int n=0;n<5;n++) ST16(((u32x4){pk2(part[2*n][0],part[2*n][1]),pk2(part[2*n][2],part[2*n][3]),pk2(part[2*n+1][0],part[2*n+1][1]),pk2(part[2*n+1][2],part[2*n+1][3])}),xr,voff,((tq*4+sp)*10+kh*5+n)*1024);
    } else {
      char* xs=sm+(72+kh*10)*1024+voff;
      #pragma unroll
      for(int n=0;n<9;n++) *(f32x4*)(xs+n*1024)=part[n];
    }
    float r2=__builtin_amdgcn_rcpf(ssq_sum(__builtin_bit_cast(f32x4,s4),lane)*(1.f/576.f)+1e-5f);
    f32x4 hr[3];
    #pragma unroll
    for(int y=0;y<3;y++){ const float mh=__uint_as_float(mhu); hr[y]=(f32x4){lo16(hv[y][0])+mh,hi16(hv[y][0])+mh,lo16(hv[y][1])+mh,hi16(hv[y][1])+mh}; }
    asm volatile("" : "+v"(r2),"+v"(hr[0]),"+v"(hr[1]),"+v"(hr[2]));
    publish(ws,O_FX+grp*128+sp*8,tag,w);
    if(w==0) poll<4>(ws,O_FX+grp*128,tag,lane);
    f32x4 own[3];
    #pragma unroll
    for(int y=0;y<3;y++) own[y]=*(const f32x4*)(sm+(72+tl*10+(y<2?2*pr+y:8))*1024+voff);
    BAR();
    u32x4 rv[3]; u32x2 rx[3];
    #pragma unroll
    for(int z=0;z<3;z++){ const int src=(sp+1+z)&3; rv[z]=LD16(xr,voff,((sp*4+src)*10+tl*5+pr)*1024); }
    if(extra){
      #pragma unroll
      for(int z=0;z<3;z++){ const int src=(sp+1+z)&3; rx[z]=LD8(xr,voff,((sp*4+src)*10+tl*5+4)*1024); } }
    char* const ob=(char*)p.out+(size_t)tk0*1152;
    #pragma unroll
    for(int y=0;y<3;y++){ if(y<2 || extra){
      f32x4 v=own[y];
      #pragma unroll
      for(int z=0;z<3;z++){ const unsigned a0= y<2? rv[z][2*(y&1)]:rx[z][0], a1= y<2? rv[z][2*(y&1)+1]:rx[z][1]; v[0]+=lo16(a0); v[1]+=hi16(a0); v[2]+=lo16(a1); v[3]+=hi16(a1); }
      v=v*r2+hr[y];
      *(u32x2*)(ob+go[y])=(u32x2){pk2(v[0],v[1]),pk2(v[2],v[3])}; } }
  }
}
}

extern "C" void kernel_launch(void* const* d_in, const int* in_sizes, int n_in,
                              void* d_out, int out_size, void* d_ws, size_t ws_size,
                              hipStream_t stream) {
  using namespace k;
  Params p;
  p.x=(const u16*)d_in[0]; p.wqkv=(const u16*)d_in[1]; p.wo=(const u16*)d_in[2];
  p.wg=(const u16*)d_in[4]; p.wu=(const u16*)d_in[5]; p.wd=(const u16*)d_in[6];
  p.out=(u16*)d_out; p.ws=(char*)d_ws;
  fused_k<<<256,512,0,stream>>>(p);
}
```
